```python
import torch

def _softmax(x, axis=-1):
    return torch.softmax(x, dim=axis)
def _gelu(x):
    return torch.nn.functional.gelu(x, approximate="tanh")
def _silu(x):
    return torch.nn.functional.silu(x)
def _sigmoid(x):
    return torch.sigmoid(x)
def _layernorm(x, axis=-1, eps=1e-5):
    m = x.mean(dim=axis, keepdim=True)
    v = x.var(dim=axis, keepdim=True, unbiased=False)
    return (x - m) / torch.sqrt(v + eps)
def _scatter_add(tgt, idx, src, axis=0):
    return tgt.clone().index_add_(axis, idx, src)
def _segment_sum(src, seg, n):
    out = torch.zeros((n,) + src.shape[1:], dtype=src.dtype)
    return out.index_add_(0, seg, src)
def _pad(x, pw):
    flat = tuple(v for pair in reversed(pw) for v in pair)
    return torch.nn.functional.pad(x, flat)

kernel_name = 'sequential_preLN__sliding_block__glu_gated_swiglu__prompt'

def setup_inputs():

    _w = (lambda a: (lambda t: t.to(torch.bfloat16) if t.is_floating_point() else t)(torch.as_tensor(a)))
    def _A_setup():
        B, S, D = 1, 2048, 1024
        return {
            "x": _w((lambda *s: torch.randn(*s, dtype=torch.bfloat16) * 0.05)(B, S, D)),
            "wqkv": _w((lambda *s: torch.randn(*s, dtype=torch.bfloat16) * 0.05)(D, 3 * D)),
            "wo": _w((lambda *s: torch.randn(*s, dtype=torch.bfloat16) * 0.05)(D, D)),
        }
    _A_in = _A_setup()

    def _B_setup():
        B, S, D, F = 1, 2048, 1024, 3584
        return {
            "x": _w((lambda *s: torch.randn(*s, dtype=torch.bfloat16) * 0.05)(B, S, D)),
            "w1": _w((lambda *s: torch.randn(*s, dtype=torch.bfloat16) * 0.8)(D, F) * (2.0 / D) ** 0.5),
            "w3": _w((lambda *s: torch.randn(*s, dtype=torch.bfloat16) * 0.8)(D, F) * (2.0 / D) ** 0.5),
            "w2": _w((lambda *s: torch.randn(*s, dtype=torch.bfloat16) * 0.8)(F, D) * (2.0 / F) ** 0.5),
        }
    _B_in = _B_setup()

    return {
        "A_x": _A_in["x"],
        "A_wqkv": _A_in["wqkv"],
        "A_wo": _A_in["wo"],
        "B_x": _B_in["x"],
        "B_w1": _B_in["w1"],
        "B_w3": _B_in["w3"],
        "B_w2": _B_in["w2"],
    }

def reference(A_x, A_wqkv, A_wo, B_x, B_w1, B_w3, B_w2):
    def _A_ref(x, wqkv, wo):
        B, S, D = x.shape
        H = 32; Dh = D // H; W = 256
        qkv = (lambda x, *d: x.permute(*d))((x @ wqkv).reshape(B, S, 3, H, Dh), 2, 0, 3, 1, 4)
        q, k, v = qkv[0], qkv[1], qkv[2]
        scores = torch.einsum("bhqd,bhkd->bhqk", q, k) * (Dh ** -0.5)
        idx = torch.arange(S)
        mask = (idx[:, None] - idx[None, :] >= 0) & (idx[:, None] - idx[None, :] < W)
        scores = torch.where(mask[None, None], scores, -1e9)
        p = _softmax(scores, axis=-1)
        o = torch.einsum("bhqk,bhkd->bhqd", p, v)
        return (lambda x, *d: x.permute(*d))(o, 0, 2, 1, 3).reshape(B, S, D) @ wo

    def _B_ref(x, w1, w3, w2):
        gate = torch.einsum("bsd,df->bsf", x, w1)
        up = torch.einsum("bsd,df->bsf", x, w3)
        h = _silu(gate) * up
        out = torch.einsum("bsf,fd->bsd", h, w2)
        return out

    x = A_x
    h = x + _A_ref(_layernorm(x, axis=-1), A_wqkv, A_wo)
    h2 = h + _B_ref(_layernorm(h, axis=-1), B_w1, B_w3, B_w2)
    return h2
```

```cpp
#include <hip/hip_runtime.h>
#include <hip/hip_bf16.h>
#include <stdint.h>

typedef unsigned short bf16_t;
typedef short bf16x8 __attribute__((ext_vector_type(8)));
typedef short s16x4 __attribute__((ext_vector_type(4)));
typedef float f32x4 __attribute__((ext_vector_type(4)));
typedef float f32x16 __attribute__((ext_vector_type(16)));
typedef unsigned u32x4 __attribute__((ext_vector_type(4)));
typedef unsigned u32x2 __attribute__((ext_vector_type(2)));
#define LAS __attribute__((address_space(3)))

constexpr int S_LEN = 2048, D_MODEL = 1024, F_DIM = 3584, N_HEAD = 32, D_HEAD = 32, WIN = 256;

__device__ __forceinline__ float bf2f(unsigned v16) { return __uint_as_float(v16 << 16); }
__device__ __forceinline__ unsigned pk_bf16(float lo, float hi) {
    unsigned r; asm("v_cvt_pk_bf16_f32 %0, %1, %2" : "=v"(r) : "v"(lo), "v"(hi)); return r;
}
__device__ __forceinline__ s16x4 tr_read(LAS const char* p) {
    return __builtin_amdgcn_ds_read_tr16_b64_v4i16((LAS s16x4*)p);
}
template <int N> __device__ __forceinline__ void wait_vm() { asm volatile("s_waitcnt vmcnt(%0)" ::"n"(N) : "memory"); }

__global__ __launch_bounds__(256) void ln_kernel(const bf16_t* __restrict__ in, bf16_t* __restrict__ out) {
    const int lane = threadIdx.x & 63, row = blockIdx.x * 4 + (threadIdx.x >> 6);
    const u32x4* src = (const u32x4*)(in + (size_t)row * D_MODEL);
    u32x4 v0 = src[lane], v1 = src[lane + 64];
    float f[16];
#pragma unroll
    for (int i = 0; i < 4; ++i) {
        f[2 * i] = bf2f(v0[i] & 0xffffu); f[2 * i + 1] = bf2f(v0[i] >> 16);
        f[8 + 2 * i] = bf2f(v1[i] & 0xffffu); f[8 + 2 * i + 1] = bf2f(v1[i] >> 16);
    }
    float s = 0.f;
#pragma unroll
    for (int i = 0; i < 16; ++i) s += f[i];
#pragma unroll
    for (int o = 32; o > 0; o >>= 1) s += __shfl_xor(s, o);
    const float mean = s * (1.0f / D_MODEL);
    float q = 0.f;
#pragma unroll
    for (int i = 0; i < 16; ++i) { f[i] -= mean; q += f[i] * f[i]; }
#pragma unroll
    for (int o = 32; o > 0; o >>= 1) q += __shfl_xor(q, o);
    const float rstd = 1.0f / sqrtf(q * (1.0f / D_MODEL) + 1e-5f);
    u32x4 o0, o1;
#pragma unroll
    for (int i = 0; i < 4; ++i) {
        o0[i] = pk_bf16(f[2 * i] * rstd, f[2 * i + 1] * rstd);
        o1[i] = pk_bf16(f[8 + 2 * i] * rstd, f[8 + 2 * i + 1] * rstd);
    }
    u32x4* dst = (u32x4*)(out + (size_t)row * D_MODEL);
    dst[lane] = o0; dst[lane + 64] = o1;
}

enum { EPI_STORE = 0, EPI_RES = 1, EPI_GLU = 2 };

template <int BM, int BN, int WM, int WN, int NST, int EPI, int RM, int RN>
__global__ __launch_bounds__(WM * WN * 64) void gemm_kernel(
    const bf16_t* __restrict__ X, const bf16_t* __restrict__ W0, const bf16_t* __restrict__ W1,
    const bf16_t* __restrict__ R, bf16_t* __restrict__ C, int M, int N, int K, int ldw, int ldc) {
    constexpr int NWAVE = WM * WN;
    constexpr int MF = BM / 16 / WM, NF = BN / 16 / WN, NB = BN / 16;
    constexpr int A_BYTES = BM * 128, B_BYTES = BN * 128, ST_BYTES = A_BYTES + B_BYTES;
    constexpr int LPW = (BM + BN) / 8 / NWAVE;
    constexpr int LPA = BM / 8 / NWAVE;
    static_assert((BM + BN) % (8 * NWAVE) == 0 && (BM % (8 * NWAVE)) == 0, "piece split");
    static_assert(NB % 4 == 0, "NB%4");
    extern __shared__ __attribute__((aligned(16))) char smem[];
    LAS char* lds = (LAS char*)smem;

    const int tid = threadIdx.x, lane = tid & 63;
    const int wave = __builtin_amdgcn_readfirstlane(tid >> 6);
    const int wm = wave / WN, wn = wave % WN;

    const int tiles_m = M / BM;
    const int tiles_n = (EPI == EPI_GLU) ? N / (BN / 2) : N / BN;
    int tm, tn;
    {
        const int xcd = blockIdx.x & 7, idx = blockIdx.x >> 3;
        const int rm = xcd / RN, rn = xcd % RN;
        const int sm = tiles_m / RM, sn = tiles_n / RN;
        tm = rm * sm + idx % sm; tn = rn * sn + idx / sm;
    }
    const int m0 = tm * BM;
    const int n0 = (EPI == EPI_GLU) ? tn * (BN / 2) : tn * BN;

    const char* gsrc[LPW];
    long gstep[LPW];
#pragma unroll
    for (int j = 0; j < LPW; ++j) {
        const int ii = wave + NWAVE * j;
        if (j < LPA) {
            const int row = ii * 8 + (lane >> 3), ch = (lane & 7) ^ (row & 7);
            gsrc[j] = (const char*)(X + (size_t)(m0 + row) * K + ch * 8);
            gstep[j] = 128;
        } else {
            const int pi = ii - BM / 8;
            const int kb = pi / (NB / 4), nb = 4 * (pi % (NB / 4)) + (lane >> 4);
            const int r = ((lane & 15) >> 1) ^ (4 * (kb & 1)), half = lane & 1;
            const bf16_t* wsrc; int col;
            if (EPI == EPI_GLU) { wsrc = (nb < NB / 2) ? W0 : W1; col = n0 + 16 * (nb % (NB / 2)); }
            else { wsrc = W0; col = n0 + 16 * nb; }
            gsrc[j] = (const char*)(wsrc + (size_t)(8 * kb + r) * ldw + col + 8 * half);
            gstep[j] = (long)64 * ldw * 2;
        }
    }
    auto issue = [&](int buf) {
#pragma unroll
        for (int j = 0; j < LPW; ++j) {
            const int ii = wave + NWAVE * j;
            __builtin_amdgcn_global_load_lds((const unsigned*)gsrc[j], (LAS unsigned*)(lds + buf * ST_BYTES + ii * 1024), 16, 0, 0);
            gsrc[j] += gstep[j];
        }
    };

    const int fr = lane & 15, g = lane >> 4, q = (lane & 15) >> 2, p = lane & 3;
    const int xrow = wm * (MF * 16) + fr;
    const int xoff0 = xrow * 128 + ((g ^ (xrow & 7)) << 4);
    const int woff0 = A_BYTES + g * NB * 256 + (q + 4 * (g & 1)) * 32 + p * 8;

    f32x4 acc[MF][NF];
#pragma unroll
    for (int i = 0; i < MF; ++i)
#pragma unroll
        for (int j = 0; j < NF; ++j) acc[i][j] = (f32x4){0.f, 0.f, 0.f, 0.f};

    const int NT = K / 64;
#pragma unroll
    for (int s = 0; s < NST - 1; ++s) issue(s);

    int buf = 0, pbuf = NST - 1;
    for (int kt = 0; kt < NT; ++kt) {
        const int rem = NT - 1 - kt;
        if (rem >= NST - 2) wait_vm<(NST - 2) * LPW>();
        else if (NST > 3 && rem == 1) wait_vm<LPW>();
        else if (NST > 4 && rem == 2) wait_vm<2 * LPW>();
        else wait_vm<0>();
        __builtin_amdgcn_s_barrier();
        asm volatile("" ::: "memory");
        if (kt + NST - 1 < NT) issue(pbuf);
        LAS const char* base = lds + buf * ST_BYTES;
#pragma unroll
        for (int ks = 0; ks < 2; ++ks) {
            bf16x8 xf[MF], wf[NF];
#pragma unroll
            for (int i = 0; i < MF; ++i)
                xf[i] = *(LAS const bf16x8*)(base + ((xoff0 ^ (ks * 64)) + i * 2048));
#pragma unroll
            for (int j = 0; j < NF; ++j) {
                int nb;
                if (EPI == EPI_GLU) nb = (j < NF / 2) ? wn * (NF / 2) + j : NB / 2 + wn * (NF / 2) + (j - NF / 2);
                else nb = wn * NF + j;
                const int o = (4 * ks * NB + nb) * 256;
                s16x4 lo = tr_read(base + woff0 + o);
                s16x4 hi = tr_read(base + (woff0 ^ 128) + o);
                wf[j] = (bf16x8){lo[0], lo[1], lo[2], lo[3], hi[0], hi[1], hi[2], hi[3]};
            }
#pragma unroll
            for (int i = 0; i < MF; ++i)
#pragma unroll
                for (int j = 0; j < NF; ++j)
                    acc[i][j] = __builtin_amdgcn_mfma_f32_16x16x32_bf16(wf[j], xf[i], acc[i][j], 0, 0, 0);
        }
        pbuf = buf; buf = (buf + 1 == NST) ? 0 : buf + 1;
    }

#pragma unroll
    for (int i = 0; i < MF; ++i) {
        const int m = m0 + wm * (MF * 16) + i * 16 + fr;
        if (EPI == EPI_GLU) {
#pragma unroll
            for (int j = 0; j < NF / 2; ++j) {
                const int n = n0 + (wn * (NF / 2) + j) * 16 + 4 * g;
                float o[4];
#pragma unroll
                for (int r = 0; r < 4; ++r) {
                    const float gt = acc[i][j][r], up = acc[i][j + NF / 2][r];
                    const float sg = gt * __builtin_amdgcn_rcpf(1.0f + __expf(-gt));
                    o[r] = sg * up;
                }
                u32x2 w; w[0] = pk_bf16(o[0], o[1]); w[1] = pk_bf16(o[2], o[3]);
                *(u32x2*)(C + (size_t)m * ldc + n) = w;
            }
        } else {
#pragma unroll
            for (int j = 0; j < NF; ++j) {
                const int n = n0 + (wn * NF + j) * 16 + 4 * g;
                f32x4 v = acc[i][j];
                if (EPI == EPI_RES) {
                    const u32x2 rv = *(const u32x2*)(R + (size_t)m * ldc + n);
                    v[0] += bf2f(rv[0] & 0xffffu); v[1] += bf2f(rv[0] >> 16);
                    v[2] += bf2f(rv[1] & 0xffffu); v[3] += bf2f(rv[1] >> 16);
                }
                u32x2 w; w[0] = pk_bf16(v[0], v[1]); w[1] = pk_bf16(v[2], v[3]);
                *(u32x2*)(C + (size_t)m * ldc + n) = w;
            }
        }
    }
}

__global__ __launch_bounds__(512) void attn_kernel(const bf16_t* __restrict__ qkv, bf16_t* __restrict__ O) {
    extern __shared__ __attribute__((aligned(16))) char smem[];
    LAS char* lds = (LAS char*)smem;
    LAS char* ldsK = lds;
    LAS char* ldsV = lds + 32768;
    const int tid = threadIdx.x, lane = tid & 63;
    const int w = __builtin_amdgcn_readfirstlane(tid >> 6);
    const int h = blockIdx.x & 31, qb = blockIdx.x >> 5;
    const int q0 = qb * 256, kbase = q0 - 256;
    constexpr int LD = 3 * D_MODEL;

    {
        u32x4 kv[4], vv[4];
#pragma unroll
        for (int i = 0; i < 4; ++i) {
            const int idx = tid + 512 * i, key = idx >> 2, ch = idx & 3;
            const int gk = kbase + key;
            if (gk >= 0) {
                const bf16_t* src = qkv + (size_t)gk * LD + h * 32 + ch * 8;
                kv[i] = *(const u32x4*)(src + D_MODEL);
                vv[i] = *(const u32x4*)(src + 2 * D_MODEL);
            }
        }
#pragma unroll
        for (int i = 0; i < 4; ++i) {
            const int idx = tid + 512 * i, key = idx >> 2, ch = idx & 3;
            const int gk = kbase + key;
            if (gk >= 0) {
                *(LAS u32x4*)(ldsK + key * 64 + ((ch ^ ((key >> 2) & 3)) << 4)) = kv[i];
                *(LAS u32x4*)(ldsV + key * 64 + (ch << 4)) = vv[i];
            }
        }
    }
    const int r32 = lane & 31, hi = lane >> 5;
    const int qw = q0 + 32 * w;
    bf16x8 qf[2];
#pragma unroll
    for (int s = 0; s < 2; ++s)
        qf[s] = *(const bf16x8*)(qkv + (size_t)(qw + r32) * LD + h * 32 + 16 * s + 8 * hi);
    __syncthreads();

    f32x16 sc[9];
    const float NEG = -INFINITY;
#pragma unroll
    for (int j = 0; j < 9; ++j) {
        const int kb = qw - 256 + 32 * j;
        if (kb >= 0) {
            const int key = 32 * (w + j) + r32;
            f32x16 a = {};
#pragma unroll
            for (int s = 0; s < 2; ++s) {
                const bf16x8 kf = *(LAS const bf16x8*)(ldsK + key * 64 + (((2 * s + hi) ^ ((key >> 2) & 3)) << 4));
                a = __builtin_amdgcn_mfma_f32_32x32x16_bf16(kf, qf[s], a, 0, 0, 0);
            }
            sc[j] = a;
        } else {
#pragma unroll
            for (int r = 0; r < 16; ++r) sc[j][r] = NEG;
        }
    }
#pragma unroll
    for (int r = 0; r < 16; ++r) {
        const int i = (r & 3) + 8 * (r >> 2) + 4 * hi;
        if (!(i > r32)) sc[0][r] = NEG;
        if (!(i <= r32)) sc[8][r] = NEG;
    }
    float mx = NEG;
#pragma unroll
    for (int j = 0; j < 9; ++j)
#pragma unroll
        for (int r = 0; r < 16; ++r) mx = fmaxf(mx, sc[j][r]);
    mx = fmaxf(mx, __shfl_xor(mx, 32));
    const float c = 0.17677669529663687f * 1.4426950408889634f;
    const float nm = -mx * c;
    float l = 0.f;
#pragma unroll
    for (int j = 0; j < 9; ++j)
#pragma unroll
        for (int r = 0; r < 16; ++r) {
            const float e = __builtin_amdgcn_exp2f(__builtin_fmaf(sc[j][r], c, nm));
            sc[j][r] = e; l += e;
        }
    l += __shfl_xor(l, 32);

    f32x16 o = {};
    const int gq = (lane & 15) >> 2, gp = lane & 3, gd = (lane >> 4) & 1;
#pragma unroll
    for (int j = 0; j < 9; ++j) {
        const int kb = qw - 256 + 32 * j;
        if (kb >= 0) {
#pragma unroll
            for (int s = 0; s < 2; ++s) {
                u32x4 pw;
#pragma unroll
                for (int e = 0; e < 4; ++e) pw[e] = pk_bf16(sc[j][8 * s + 2 * e], sc[j][8 * s + 2 * e + 1]);
                const int key = 32 * (w + j) + 16 * s + 4 * hi + gq;
                LAS const char* vp = ldsV + key * 64 + (16 * gd + 4 * gp) * 2;
                s16x4 lo = tr_read(vp), hi8 = tr_read(vp + 8 * 64);
                const bf16x8 vf = (bf16x8){lo[0], lo[1], lo[2], lo[3], hi8[0], hi8[1], hi8[2], hi8[3]};
                o = __builtin_amdgcn_mfma_f32_32x32x16_bf16(vf, __builtin_bit_cast(bf16x8, pw), o, 0, 0, 0);
            }
        }
    }
    const float rl = 1.0f / l;
    bf16_t* dst = O + (size_t)(qw + r32) * D_MODEL + h * 32 + 4 * hi;
#pragma unroll
    for (int t = 0; t < 4; ++t) {
        u32x2 wv; wv[0] = pk_bf16(o[4 * t] * rl, o[4 * t + 1] * rl); wv[1] = pk_bf16(o[4 * t + 2] * rl, o[4 * t + 3] * rl);
        *(u32x2*)(dst + 8 * t) = wv;
    }
}

template <typename KF> static void set_lds(KF kf, int bytes) {
    hipFuncSetAttribute((const void*)kf, hipFuncAttributeMaxDynamicSharedMemorySize, bytes);
}

extern "C" void kernel_launch(void* const* d_in, const int* in_sizes, int n_in,
                              void* d_out, int out_size, void* d_ws, size_t ws_size,
                              hipStream_t stream) {
    const bf16_t* x = (const bf16_t*)d_in[0];
    const bf16_t* wqkv = (const bf16_t*)d_in[1];
    const bf16_t* wo = (const bf16_t*)d_in[2];
    const bf16_t* w1 = (const bf16_t*)d_in[4];
    const bf16_t* w3 = (const bf16_t*)d_in[5];
    const bf16_t* w2 = (const bf16_t*)d_in[6];
    bf16_t* out = (bf16_t*)d_out;
    char* ws = (char*)d_ws;
    bf16_t* xn = (bf16_t*)(ws);
    bf16_t* qkv = (bf16_t*)(ws + (4u << 20));
    bf16_t* att = (bf16_t*)(ws + (16u << 20));
    bf16_t* hbuf = (bf16_t*)(ws + (20u << 20));
    bf16_t* hn = (bf16_t*)(ws + (24u << 20));
    bf16_t* act = (bf16_t*)(ws + (28u << 20));

    ln_kernel<<<S_LEN / 4, 256, 0, stream>>>(x, xn);
    {
        auto kf = gemm_kernel<128, 192, 2, 4, 3, EPI_STORE, 2, 4>;
        constexpr int lds = 3 * (128 + 192) * 128;
        set_lds(kf, lds);
        kf<<<256, 512, lds, stream>>>(xn, wqkv, nullptr, nullptr, qkv, S_LEN, 3072, 1024, 3072, 3072);
    }
    {
        set_lds(attn_kernel, 65536);
        attn_kernel<<<256, 512, 65536, stream>>>(qkv, att);
    }
    {
        auto kf = gemm_kernel<128, 64, 4, 2, 4, EPI_RES, 4, 2>;
        constexpr int lds = 4 * (128 + 64) * 128;
        set_lds(kf, lds);
        kf<<<256, 512, lds, stream>>>(att, wo, nullptr, x, hbuf, S_LEN, 1024, 1024, 1024, 1024);
    }
    ln_kernel<<<S_LEN / 4, 256, 0, stream>>>(hbuf, hn);
    {
        auto kf = gemm_kernel<256, 256, 2, 4, 2, EPI_GLU, 2, 4>;
        constexpr int lds = 2 * (256 + 256) * 128;
        set_lds(kf, lds);
        kf<<<224, 512, lds, stream>>>(hn, w1, w3, nullptr, act, S_LEN, F_DIM, 1024, F_DIM, F_DIM);
    }
    {
        auto kf = gemm_kernel<128, 64, 4, 2, 4, EPI_RES, 4, 2>;
        constexpr int lds = 4 * (128 + 64) * 128;
        kf<<<256, 512, lds, stream>>>(act, w2, nullptr, hbuf, out, S_LEN, 1024, F_DIM, 1024, 1024);
    }
}
```

```cpp
#include <hip/hip_runtime.h>
#include <hip/hip_bf16.h>
#include <stdint.h>

typedef unsigned short bf16_t;
typedef short bf16x8 __attribute__((ext_vector_type(8)));
typedef short s16x4 __attribute__((ext_vector_type(4)));
typedef float f32x4 __attribute__((ext_vector_type(4)));
typedef float f32x16 __attribute__((ext_vector_type(16)));
typedef unsigned u32x4 __attribute__((ext_vector_type(4)));
typedef unsigned u32x2 __attribute__((ext_vector_type(2)));
#define LAS __attribute__((address_space(3)))

constexpr int S_LEN = 2048, D_MODEL = 1024, F_DIM = 3584, N_HEAD = 32, D_HEAD = 32, WIN = 256;

__device__ __forceinline__ float bf2f(unsigned v16) { return __uint_as_float(v16 << 16); }
__device__ __forceinline__ unsigned pk_bf16(float lo, float hi) {
    unsigned r; asm("v_cvt_pk_bf16_f32 %0, %1, %2" : "=v"(r) : "v"(lo), "v"(hi)); return r;
}
__device__ __forceinline__ s16x4 tr_read(LAS const char* p) {
    return __builtin_amdgcn_ds_read_tr16_b64_v4i16((LAS s16x4*)p);
}
__device__ __forceinline__ void glds16(const void* sbase, unsigned voff, unsigned lds_addr) {
    unsigned keep;
    asm volatile("s_mov_b32 %0, m0\n\ts_mov_b32 m0, %3\n\ts_nop 0\n\tglobal_load_lds_dwordx4 %2, %1\n\ts_mov_b32 m0, %0"
                 : "=&s"(keep) : "s"(sbase), "v"(voff), "s"(lds_addr) : "memory");
}
template <int N> __device__ __forceinline__ void wait_vm() { asm volatile("s_waitcnt vmcnt(%0)" ::"n"(N) : "memory"); }

__global__ __launch_bounds__(256) void ln_kernel(const bf16_t* __restrict__ in, bf16_t* __restrict__ out) {
    const int lane = threadIdx.x & 63, row = blockIdx.x * 4 + (threadIdx.x >> 6);
    const u32x4* src = (const u32x4*)(in + (size_t)row * D_MODEL);
    u32x4 v0 = src[lane], v1 = src[lane + 64];
    float f[16];
#pragma unroll
    for (int i = 0; i < 4; ++i) {
        f[2 * i] = bf2f(v0[i] & 0xffffu); f[2 * i + 1] = bf2f(v0[i] >> 16);
        f[8 + 2 * i] = bf2f(v1[i] & 0xffffu); f[8 + 2 * i + 1] = bf2f(v1[i] >> 16);
    }
    float s = 0.f;
#pragma unroll
    for (int i = 0; i < 16; ++i) s += f[i];
#pragma unroll
    for (int o = 32; o > 0; o >>= 1) s += __shfl_xor(s, o);
    const float mean = s * (1.0f / D_MODEL);
    float q = 0.f;
#pragma unroll
    for (int i = 0; i < 16; ++i) { f[i] -= mean; q += f[i] * f[i]; }
#pragma unroll
    for (int o = 32; o > 0; o >>= 1) q += __shfl_xor(q, o);
    const float rstd = 1.0f / sqrtf(q * (1.0f / D_MODEL) + 1e-5f);
    u32x4 o0, o1;
#pragma unroll
    for (int i = 0; i < 4; ++i) {
        o0[i] = pk_bf16(f[2 * i] * rstd, f[2 * i + 1] * rstd);
        o1[i] = pk_bf16(f[8 + 2 * i] * rstd, f[8 + 2 * i + 1] * rstd);
    }
    u32x4* dst = (u32x4*)(out + (size_t)row * D_MODEL);
    dst[lane] = o0; dst[lane + 64] = o1;
}

enum { EPI_STORE = 0, EPI_RES = 1, EPI_GLU = 2 };
#define KROT 0

template <int BM, int BN, int WM, int WN, int NST, int EPI, int RM, int RN>
__global__ __launch_bounds__(WM * WN * 64) void gemm_kernel(
    const bf16_t* __restrict__ X, const bf16_t* __restrict__ W0, const bf16_t* __restrict__ W1,
    const bf16_t* __restrict__ R, bf16_t* __restrict__ C, int M, int N, int K, int ldw, int ldc) {
    constexpr int NWAVE = WM * WN;
    constexpr int MF = BM / 16 / WM, NF = BN / 16 / WN, NB = BN / 16;
    constexpr int A_BYTES = BM * 128, B_BYTES = BN * 128, ST_BYTES = A_BYTES + B_BYTES;
    constexpr int LPW = (BM + BN) / 8 / NWAVE;
    constexpr int LPA = BM / 8 / NWAVE;
    static_assert((BM + BN) % (8 * NWAVE) == 0 && (BM % (8 * NWAVE)) == 0, "piece split");
    static_assert(NB % 4 == 0, "NB%4");
    extern __shared__ __attribute__((aligned(16))) char smem[];
    LAS char* lds = (LAS char*)smem;

    const int tid = threadIdx.x, lane = tid & 63;
    const int wave = __builtin_amdgcn_readfirstlane(tid >> 6);
    const int wm = wave / WN, wn = wave % WN;

    const int tiles_m = M / BM;
    const int tiles_n = (EPI == EPI_GLU) ? N / (BN / 2) : N / BN;
    int tm, tn;
    {
        const int xcd = blockIdx.x & 7, idx = blockIdx.x >> 3;
        const int rm = xcd / RN, rn = xcd % RN;
        const int sm = tiles_m / RM, sn = tiles_n / RN;
        tm = rm * sm + idx % sm; tn = rn * sn + idx / sm;
    }
    const int m0 = tm * BM;
    const int n0 = (EPI == EPI_GLU) ? tn * (BN / 2) : tn * BN;

    unsigned voff[LPW];
    const char* sbase[LPW];
#pragma unroll
    for (int j = 0; j < LPW; ++j) {
        const int ii = wave + NWAVE * j;
        if (j < LPA) {
            const int row = ii * 8 + (lane >> 3), ch = (lane & 7) ^ (row & 7);
            voff[j] = (unsigned)(row * K * 2 + ch * 16);
            sbase[j] = (const char*)(X + (size_t)m0 * K);
        } else {
            const int pi = ii - BM / 8;
            const int kb = pi / (NB / 4), nb4 = 4 * (pi % (NB / 4)), nb = nb4 + (lane >> 4);
            const int r = ((lane & 15) >> 1) ^ (4 * (kb & 1)), half = lane & 1;
            const bf16_t* wsrc; int col;
            if (EPI == EPI_GLU) { wsrc = (nb4 < NB / 2) ? W0 : W1; col = 16 * (nb % (NB / 2)); }
            else { wsrc = W0; col = 16 * nb; }
            voff[j] = (unsigned)(((8 * kb + r) * ldw + col + 8 * half) * 2);
            sbase[j] = (const char*)(wsrc + n0);
        }
    }
    const unsigned lds_base = (unsigned)(uintptr_t)lds;
    const int NT = K / 64;
    int kis = (int)(((unsigned)(blockIdx.x >> 3) * KROT) % (unsigned)NT);
#pragma unroll
    for (int j = 0; j < LPW; ++j) sbase[j] += (long)kis * ((j < LPA) ? 128 : (long)128 * ldw);
    auto issue = [&](int buf) {
#pragma unroll
        for (int j = 0; j < LPW; ++j) {
            const int ii = wave + NWAVE * j;
            glds16(sbase[j], voff[j], lds_base + buf * ST_BYTES + ii * 1024);
            sbase[j] += (j < LPA) ? 128 : (long)128 * ldw;
        }
        if (++kis == NT) {
            kis = 0;
#pragma unroll
            for (int j = 0; j < LPW; ++j) sbase[j] -= (long)NT * ((j < LPA) ? 128 : (long)128 * ldw);
        }
    };

    const int fr = lane & 15, g = lane >> 4, q = (lane & 15) >> 2, p = lane & 3;
    const int xrow = wm * (MF * 16) + fr;
    const int xoff0 = xrow * 128 + ((g ^ (xrow & 7)) << 4);
    const int woff0 = A_BYTES + g * NB * 256 + (q + 4 * (g & 1)) * 32 + p * 8;

    f32x4 acc[MF][NF];
#pragma unroll
    for (int i = 0; i < MF; ++i)
#pragma unroll
        for (int j = 0; j < NF; ++j) acc[i][j] = (f32x4){0.f, 0.f, 0.f, 0.f};

#pragma unroll
    for (int s = 0; s < NST - 1; ++s) issue(s);

    auto read_frags = [&](LAS const char* base, int ks, bf16x8 (&xf)[MF], bf16x8 (&wf)[NF]) {
#pragma unroll
        for (int j = 0; j < NF; ++j) {
            int nb;
            if (EPI == EPI_GLU) nb = (j < NF / 2) ? wn * (NF / 2) + j : NB / 2 + wn * (NF / 2) + (j - NF / 2);
            else nb = wn * NF + j;
            const int o = (4 * ks * NB + nb) * 256;
            s16x4 lo = tr_read(base + woff0 + o);
            s16x4 hi = tr_read(base + (woff0 ^ 128) + o);
            wf[j] = (bf16x8){lo[0], lo[1], lo[2], lo[3], hi[0], hi[1], hi[2], hi[3]};
        }
#pragma unroll
        for (int i = 0; i < MF; ++i)
            xf[i] = *(LAS const bf16x8*)(base + ((xoff0 ^ (ks * 64)) + i * 2048));
    };
    auto do_mfma = [&](bf16x8 (&xf)[MF], bf16x8 (&wf)[NF]) {
#pragma unroll
        for (int i = 0; i < MF; ++i)
#pragma unroll
            for (int j = 0; j < NF; ++j)
                acc[i][j] = __builtin_amdgcn_mfma_f32_16x16x32_bf16(wf[j], xf[i], acc[i][j], 0, 0, 0);
    };

    auto interleave = [&]() {
        constexpr int NM = MF * NF, NR = MF + 2 * NF, RPM = (NR + NM - 1) / NM;
#pragma unroll
        for (int i = 0; i < NM; ++i) {
            __builtin_amdgcn_sched_group_barrier(0x8, 1, 0);
            __builtin_amdgcn_sched_group_barrier(0x100, RPM, 0);
        }
    };
    bf16x8 xa[MF], wa[NF], xb[MF], wb[NF];
    wait_vm<(NST - 2) * LPW>();
    __builtin_amdgcn_s_barrier();
    asm volatile("" ::: "memory");
    read_frags(lds, 0, xa, wa);

    int buf = 0, pbuf = NST - 1;
    for (int kt = 0; kt < NT; ++kt) {
        const int rem = NT - 2 - kt;
        if (rem >= NST - 3) wait_vm<(NST - 3) * LPW>();
        else if (NST > 4 && rem == 1) wait_vm<LPW>();
        else if (NST > 5 && rem == 2) wait_vm<2 * LPW>();
        else wait_vm<0>();
        __builtin_amdgcn_s_barrier();
        asm volatile("" ::: "memory");
        if (kt + NST - 1 < NT) issue(pbuf);
        const int nbuf = (buf + 1 == NST) ? 0 : buf + 1;
        __builtin_amdgcn_sched_barrier(0);
        read_frags(lds + buf * ST_BYTES, 1, xb, wb);
        do_mfma(xa, wa);
        interleave();
        __builtin_amdgcn_sched_barrier(0);
        read_frags(lds + nbuf * ST_BYTES, 0, xa, wa);
        do_mfma(xb, wb);
        interleave();
        __builtin_amdgcn_sched_barrier(0);
        pbuf = buf; buf = nbuf;
    }

#pragma unroll
    for (int i = 0; i < MF; ++i) {
        const int m = m0 + wm * (MF * 16) + i * 16 + fr;
        if (EPI == EPI_GLU) {
#pragma unroll
            for (int j = 0; j < NF / 2; ++j) {
                const int n = n0 + (wn * (NF / 2) + j) * 16 + 4 * g;
                float o[4];
#pragma unroll
                for (int r = 0; r < 4; ++r) {
                    const float gt = acc[i][j][r], up = acc[i][j + NF / 2][r];
                    const float sg = gt * __builtin_amdgcn_rcpf(1.0f + __expf(-gt));
                    o[r] = sg * up;
                }
                u32x2 w; w[0] = pk_bf16(o[0], o[1]); w[1] = pk_bf16(o[2], o[3]);
                *(u32x2*)(C + (size_t)m * ldc + n) = w;
            }
        } else {
#pragma unroll
            for (int j = 0; j < NF; ++j) {
                const int n = n0 + (wn * NF + j) * 16 + 4 * g;
                f32x4 v = acc[i][j];
                if (EPI == EPI_RES) {
                    const u32x2 rv = *(const u32x2*)(R + (size_t)m * ldc + n);
                    v[0] += bf2f(rv[0] & 0xffffu); v[1] += bf2f(rv[0] >> 16);
                    v[2] += bf2f(rv[1] & 0xffffu); v[3] += bf2f(rv[1] >> 16);
                }
                u32x2 w; w[0] = pk_bf16(v[0], v[1]); w[1] = pk_bf16(v[2], v[3]);
                *(u32x2*)(C + (size_t)m * ldc + n) = w;
            }
        }
    }
}

__global__ __launch_bounds__(512, 2) void glu_gemm_kernel(
    const bf16_t* __restrict__ X, const bf16_t* __restrict__ W1, const bf16_t* __restrict__ W3,
    bf16_t* __restrict__ C) {
    constexpr int K = D_MODEL, LDW = F_DIM, HTB = 16384;
    extern __shared__ __attribute__((aligned(16))) char smem[];
    LAS char* lds = (LAS char*)smem;
    const unsigned lds_base = (unsigned)(uintptr_t)lds;
    const int tid = threadIdx.x, lane = tid & 63;
    const int wave = __builtin_amdgcn_readfirstlane(tid >> 6);
    const int wr = wave >> 2, wc = wave & 3;
    int tm, tn;
    { const int xcd = blockIdx.x & 7, idx = blockIdx.x >> 3; tm = (xcd >> 2) * 4 + (idx & 3); tn = (xcd & 3) * 7 + (idx >> 2); }
    const int m0 = tm * 256, n0 = tn * 128;

    unsigned xoffs[2], woffs[2];
#pragma unroll
    for (int j = 0; j < 2; ++j) {
        const int pc = wave + 8 * j;
        const int row = pc * 8 + (lane >> 3), ch = (lane & 7) ^ (row & 7);
        xoffs[j] = (unsigned)(row * K * 2 + ch * 16);
        const int kb = pc >> 1, nb = 4 * (pc & 1) + (lane >> 4);
        const int r = ((lane & 15) >> 1) ^ (4 * (kb & 1)), half = lane & 1;
        woffs[j] = (unsigned)(((8 * kb + r) * LDW + 16 * nb + 8 * half) * 2);
    }
    const char* xsrc = (const char*)(X + (size_t)m0 * K);
    const char* w1src = (const char*)(W1 + n0);
    const char* w3src = (const char*)(W3 + n0);
#define SA_OFF(b, h) (((b) * 2 + (h)) * HTB)
#define SB_OFF(b, h) ((4 + (b) * 2 + (h)) * HTB)
#define STAGE_A(b, h, kt) do { const char* _s = xsrc + (size_t)(h) * 128 * K * 2 + (kt) * 128; \
        glds16(_s, xoffs[0], lds_base + SA_OFF(b, h) + wave * 1024); glds16(_s, xoffs[1], lds_base + SA_OFF(b, h) + (wave + 8) * 1024); } while (0)
#define STAGE_B(b, h, kt) do { const char* _s = ((h) ? w3src : w1src) + (size_t)(kt) * 64 * LDW * 2; \
        glds16(_s, woffs[0], lds_base + SB_OFF(b, h) + wave * 1024); glds16(_s, woffs[1], lds_base + SB_OFF(b, h) + (wave + 8) * 1024); } while (0)
    const int fr = lane & 15, g = lane >> 4, q = (lane & 15) >> 2, p = lane & 3;
    const int xrow = wr * 64 + fr;
    const int xo0 = xrow * 128 + ((g ^ (xrow & 7)) << 4);
    const int wo0 = g * 2048 + (q + 4 * (g & 1)) * 32 + p * 8 + wc * 512;
#define LDA(dst, b, h) do { _Pragma("unroll") for (int m = 0; m < 4; ++m) _Pragma("unroll") for (int k = 0; k < 2; ++k) \
        dst[m][k] = *(LAS const bf16x8*)(lds + SA_OFF(b, h) + ((xo0 ^ (k * 64)) + m * 2048)); } while (0)
#define LDB(dst, b, h) do { _Pragma("unroll") for (int n = 0; n < 2; ++n) _Pragma("unroll") for (int k = 0; k < 2; ++k) { \
        s16x4 lo_ = tr_read(lds + SB_OFF(b, h) + wo0 + n * 256 + k * 8192); \
        s16x4 hi_ = tr_read(lds + SB_OFF(b, h) + (wo0 ^ 128) + n * 256 + k * 8192); \
        dst[n][k] = (bf16x8){lo_[0], lo_[1], lo_[2], lo_[3], hi_[0], hi_[1], hi_[2], hi_[3]}; } } while (0)
#define MMA(ai, bj, At, Bt) do { __builtin_amdgcn_s_setprio(1); \
        _Pragma("unroll") for (int m = 0; m < 4; ++m) _Pragma("unroll") for (int n = 0; n < 2; ++n) _Pragma("unroll") for (int k = 0; k < 2; ++k) \
            acc[ai][bj][m][n] = __builtin_amdgcn_mfma_f32_16x16x32_bf16(Bt[n][k], At[m][k], acc[ai][bj][m][n], 0, 0, 0); \
        __builtin_amdgcn_s_setprio(0); } while (0)
#define WAIT_V(n) asm volatile("s_waitcnt vmcnt(" #n ")" ::: "memory")
#define WAIT_L(n) asm volatile("s_waitcnt lgkmcnt(" #n ")" ::: "memory")
#define BAR __builtin_amdgcn_s_barrier()
#define SCHED __builtin_amdgcn_sched_barrier(0)
    f32x4 acc[2][2][4][2] = {};
    bf16x8 At[4][2], B0[2][2], B1[2][2];
    constexpr int nt = K / 64;
    STAGE_B(0, 0, 0); STAGE_A(0, 0, 0); STAGE_B(0, 1, 0); STAGE_A(0, 1, 0);
    if (wr == 1) BAR;
    WAIT_V(4); BAR;
    STAGE_B(1, 0, 1); STAGE_A(1, 0, 1); STAGE_B(1, 1, 1);
    WAIT_V(6); BAR;
    for (int t = 0; t < nt - 2; t += 2) {
        LDB(B0, 0, 0); SCHED; LDA(At, 0, 0); STAGE_A(1, 1, t + 1);
        WAIT_L(8); BAR; WAIT_L(0); MMA(0, 0, At, B0); BAR; SCHED;
        LDB(B1, 0, 1); STAGE_B(0, 0, t + 2);
        BAR; WAIT_L(0); MMA(0, 1, At, B1); BAR;
        LDA(At, 0, 1); STAGE_A(0, 0, t + 2);
        BAR; WAIT_L(0); MMA(1, 0, At, B0); BAR; SCHED;
        STAGE_B(0, 1, t + 2);
        WAIT_V(6); BAR; MMA(1, 1, At, B1); BAR;
        LDB(B0, 1, 0); SCHED; LDA(At, 1, 0); STAGE_A(0, 1, t + 2);
        WAIT_L(8); BAR; WAIT_L(0); MMA(0, 0, At, B0); BAR; SCHED;
        LDB(B1, 1, 1); STAGE_B(1, 0, t + 3);
        BAR; WAIT_L(0); MMA(0, 1, At, B1); BAR;
        LDA(At, 1, 1); STAGE_A(1, 0, t + 3);
        BAR; WAIT_L(0); MMA(1, 0, At, B0); BAR; SCHED;
        STAGE_B(1, 1, t + 3);
        WAIT_V(6); BAR; MMA(1, 1, At, B1); BAR;
    }
    { LDB(B0, 0, 0); WAIT_V(0); LDA(At, 0, 0); STAGE_A(1, 1, nt - 1);
      BAR; WAIT_L(0); MMA(0, 0, At, B0); BAR;
      LDB(B1, 0, 1); BAR; WAIT_L(0); MMA(0, 1, At, B1); BAR;
      LDA(At, 0, 1); WAIT_V(4); BAR; WAIT_L(0); MMA(1, 0, At, B0); MMA(1, 1, At, B1); BAR; }
    { LDB(B0, 1, 0); LDA(At, 1, 0); WAIT_V(2); BAR; WAIT_L(0); MMA(0, 0, At, B0); BAR;
      LDB(B1, 1, 1); WAIT_V(0); BAR; WAIT_L(0); MMA(0, 1, At, B1); BAR;
      LDA(At, 1, 1); BAR; WAIT_L(0); MMA(1, 0, At, B0); MMA(1, 1, At, B1); BAR; }
    if (wr == 0) BAR;
#pragma unroll
    for (int ai = 0; ai < 2; ++ai)
#pragma unroll
        for (int m = 0; m < 4; ++m) {
            const int row = m0 + ai * 128 + wr * 64 + m * 16 + fr;
#pragma unroll
            for (int n = 0; n < 2; ++n) {
                float o[4];
#pragma unroll
                for (int r = 0; r < 4; ++r) {
                    const float gt = acc[ai][0][m][n][r], up = acc[ai][1][m][n][r];
                    o[r] = gt * __builtin_amdgcn_rcpf(1.0f + __expf(-gt)) * up;
                }
                u32x2 w; w[0] = pk_bf16(o[0], o[1]); w[1] = pk_bf16(o[2], o[3]);
                *(u32x2*)(C + (size_t)row * LDW + n0 + wc * 32 + n * 16 + 4 * g) = w;
            }
        }
#undef SA_OFF
#undef SB_OFF
#undef STAGE_A
#undef STAGE_B
#undef LDA
#undef LDB
#undef MMA
}

__global__ __launch_bounds__(512) void attn_kernel(const bf16_t* __restrict__ qkv, bf16_t* __restrict__ O) {
    extern __shared__ __attribute__((aligned(16))) char smem[];
    LAS char* lds = (LAS char*)smem;
    LAS char* ldsK = lds;
    LAS char* ldsV = lds + 32768;
    const int tid = threadIdx.x, lane = tid & 63;
    const int w = __builtin_amdgcn_readfirstlane(tid >> 6);
    const int h = blockIdx.x & 31, qb = blockIdx.x >> 5;
    const int q0 = qb * 256, kbase = q0 - 256;
    constexpr int LD = 3 * D_MODEL;

    {
        u32x4 kv[4], vv[4];
#pragma unroll
        for (int i = 0; i < 4; ++i) {
            const int idx = tid + 512 * i, key = idx >> 2, ch = idx & 3;
            const int gk = kbase + key;
            if (gk >= 0) {
                const bf16_t* src = qkv + (size_t)gk * LD + h * 32 + ch * 8;
                kv[i] = *(const u32x4*)(src + D_MODEL);
                vv[i] = *(const u32x4*)(src + 2 * D_MODEL);
            }
        }
#pragma unroll
        for (int i = 0; i < 4; ++i) {
            const int idx = tid + 512 * i, key = idx >> 2, ch = idx & 3;
            const int gk = kbase + key;
            if (gk >= 0) {
                *(LAS u32x4*)(ldsK + key * 64 + ((ch ^ ((key >> 2) & 3)) << 4)) = kv[i];
                *(LAS u32x4*)(ldsV + key * 64 + (ch << 4)) = vv[i];
            }
        }
    }
    const int r32 = lane & 31, hi = lane >> 5;
    const int qw = q0 + 32 * w;
    bf16x8 qf[2];
#pragma unroll
    for (int s = 0; s < 2; ++s)
        qf[s] = *(const bf16x8*)(qkv + (size_t)(qw + r32) * LD + h * 32 + 16 * s + 8 * hi);
    __syncthreads();

    f32x16 sc[9];
    const float NEG = -INFINITY;
#pragma unroll
    for (int j = 0; j < 9; ++j) {
        const int kb = qw - 256 + 32 * j;
        if (kb >= 0) {
            const int key = 32 * (w + j) + r32;
            f32x16 a = {};
#pragma unroll
            for (int s = 0; s < 2; ++s) {
                const bf16x8 kf = *(LAS const bf16x8*)(ldsK + key * 64 + (((2 * s + hi) ^ ((key >> 2) & 3)) << 4));
                a = __builtin_amdgcn_mfma_f32_32x32x16_bf16(kf, qf[s], a, 0, 0, 0);
            }
            sc[j] = a;
        } else {
#pragma unroll
            for (int r = 0; r < 16; ++r) sc[j][r] = NEG;
        }
    }
#pragma unroll
    for (int r = 0; r < 16; ++r) {
        const int i = (r & 3) + 8 * (r >> 2) + 4 * hi;
        if (!(i > r32)) sc[0][r] = NEG;
        if (!(i <= r32)) sc[8][r] = NEG;
    }
    float mx = NEG;
#pragma unroll
    for (int j = 0; j < 9; ++j)
#pragma unroll
        for (int r = 0; r < 16; ++r) mx = fmaxf(mx, sc[j][r]);
    mx = fmaxf(mx, __shfl_xor(mx, 32));
    const float c = 0.17677669529663687f * 1.4426950408889634f;
    const float nm = -mx * c;
    float l = 0.f;
#pragma unroll
    for (int j = 0; j < 9; ++j)
#pragma unroll
        for (int r = 0; r < 16; ++r) {
            const float e = __builtin_amdgcn_exp2f(__builtin_fmaf(sc[j][r], c, nm));
            sc[j][r] = e; l += e;
        }
    l += __shfl_xor(l, 32);

    f32x16 o = {};
    const int gq = (lane & 15) >> 2, gp = lane & 3, gd = (lane >> 4) & 1;
#pragma unroll
    for (int j = 0; j < 9; ++j) {
        const int kb = qw - 256 + 32 * j;
        if (kb >= 0) {
#pragma unroll
            for (int s = 0; s < 2; ++s) {
                u32x4 pw;
#pragma unroll
                for (int e = 0; e < 4; ++e) pw[e] = pk_bf16(sc[j][8 * s + 2 * e], sc[j][8 * s + 2 * e + 1]);
                const int key = 32 * (w + j) + 16 * s + 4 * hi + gq;
                LAS const char* vp = ldsV + key * 64 + (16 * gd + 4 * gp) * 2;
                s16x4 lo = tr_read(vp), hi8 = tr_read(vp + 8 * 64);
                const bf16x8 vf = (bf16x8){lo[0], lo[1], lo[2], lo[3], hi8[0], hi8[1], hi8[2], hi8[3]};
                o = __builtin_amdgcn_mfma_f32_32x32x16_bf16(vf, __builtin_bit_cast(bf16x8, pw), o, 0, 0, 0);
            }
        }
    }
    const float rl = 1.0f / l;
    bf16_t* dst = O + (size_t)(qw + r32) * D_MODEL + h * 32 + 4 * hi;
#pragma unroll
    for (int t = 0; t < 4; ++t) {
        u32x2 wv; wv[0] = pk_bf16(o[4 * t] * rl, o[4 * t + 1] * rl); wv[1] = pk_bf16(o[4 * t + 2] * rl, o[4 * t + 3] * rl);
        *(u32x2*)(dst + 8 * t) = wv;
    }
}

template <typename KF> static void set_lds(KF kf, int bytes) {
    hipFuncSetAttribute((const void*)kf, hipFuncAttributeMaxDynamicSharedMemorySize, bytes);
}

extern "C" void kernel_launch(void* const* d_in, const int* in_sizes, int n_in,
                              void* d_out, int out_size, void* d_ws, size_t ws_size,
                              hipStream_t stream) {
    const bf16_t* x = (const bf16_t*)d_in[0];
    const bf16_t* wqkv = (const bf16_t*)d_in[1];
    const bf16_t* wo = (const bf16_t*)d_in[2];
    const bf16_t* w1 = (const bf16_t*)d_in[4];
    const bf16_t* w3 = (const bf16_t*)d_in[5];
    const bf16_t* w2 = (const bf16_t*)d_in[6];
    bf16_t* out = (bf16_t*)d_out;
    char* ws = (char*)d_ws;
    bf16_t* xn = (bf16_t*)(ws);
    bf16_t* qkv = (bf16_t*)(ws + (4u << 20));
    bf16_t* att = (bf16_t*)(ws + (16u << 20));
    bf16_t* hbuf = (bf16_t*)(ws + (20u << 20));
    bf16_t* hn = (bf16_t*)(ws + (24u << 20));
    bf16_t* act = (bf16_t*)(ws + (28u << 20));

    ln_kernel<<<S_LEN / 4, 256, 0, stream>>>(x, xn);
    {
        auto kf = gemm_kernel<128, 192, 2, 4, 4, EPI_STORE, 2, 4>;
        constexpr int lds = 4 * (128 + 192) * 128;
        set_lds(kf, lds);
        kf<<<256, 512, lds, stream>>>(xn, wqkv, nullptr, nullptr, qkv, S_LEN, 3072, 1024, 3072, 3072);
    }
    {
        set_lds(attn_kernel, 65536);
        attn_kernel<<<256, 512, 65536, stream>>>(qkv, att);
    }
    {
        auto kf = gemm_kernel<128, 64, 4, 2, 6, EPI_RES, 4, 2>;
        constexpr int lds = 6 * (128 + 64) * 128;
        set_lds(kf, lds);
        kf<<<256, 512, lds, stream>>>(att, wo, nullptr, x, hbuf, S_LEN, 1024, 1024, 1024, 1024);
    }
    ln_kernel<<<S_LEN / 4, 256, 0, stream>>>(hbuf, hn);
    {
        set_lds(glu_gemm_kernel, 131072);
        glu_gemm_kernel<<<224, 512, 131072, stream>>>(hn, w1, w3, act);
    }
    {
        auto kf = gemm_kernel<128, 64, 4, 2, 6, EPI_RES, 4, 2>;
        constexpr int lds = 6 * (128 + 64) * 128;
        kf<<<256, 512, lds, stream>>>(act, w2, nullptr, hbuf, out, S_LEN, 1024, F_DIM, 1024, 1024);
    }
}
```

```cpp
#include <hip/hip_runtime.h>
#include <hip/hip_bf16.h>
#include <stdint.h>

typedef unsigned short bf16_t;
typedef short bf16x8 __attribute__((ext_vector_type(8)));
typedef short s16x4 __attribute__((ext_vector_type(4)));
typedef float f32x4 __attribute__((ext_vector_type(4)));
typedef float f32x16 __attribute__((ext_vector_type(16)));
typedef unsigned u32x4 __attribute__((ext_vector_type(4)));
typedef unsigned u32x2 __attribute__((ext_vector_type(2)));
#define LAS __attribute__((address_space(3)))

constexpr int S_LEN = 2048, D_MODEL = 1024, F_DIM = 3584, N_HEAD = 32, D_HEAD = 32, WIN = 256;

__device__ __forceinline__ float bf2f(unsigned v16) { return __uint_as_float(v16 << 16); }
__device__ __forceinline__ unsigned pk_bf16(float lo, float hi) {
    unsigned r; asm("v_cvt_pk_bf16_f32 %0, %1, %2" : "=v"(r) : "v"(lo), "v"(hi)); return r;
}
__device__ __forceinline__ s16x4 tr_read(LAS const char* p) {
    return __builtin_amdgcn_ds_read_tr16_b64_v4i16((LAS s16x4*)p);
}
__device__ __forceinline__ void glds16(const void* sbase, unsigned voff, unsigned lds_addr) {
    unsigned keep;
    asm volatile("s_mov_b32 %0, m0\n\ts_mov_b32 m0, %3\n\ts_nop 0\n\tglobal_load_lds_dwordx4 %2, %1\n\ts_mov_b32 m0, %0"
                 : "=&s"(keep) : "s"(sbase), "v"(voff), "s"(lds_addr) : "memory");
}
typedef __bf16 bf16x2_t __attribute__((ext_vector_type(2)));
__device__ __forceinline__ float dot2(unsigned a, unsigned b, float acc) {
    return __builtin_amdgcn_fdot2_f32_bf16(__builtin_bit_cast(bf16x2_t, a), __builtin_bit_cast(bf16x2_t, b), acc, false);
}
__device__ __forceinline__ void frag_sum(const bf16x8& f, float& s1) {
    const u32x4 u = __builtin_bit_cast(u32x4, f);
#pragma unroll
    for (int e = 0; e < 4; ++e) s1 = dot2(u[e], 0x3f803f80u, s1);
}
__device__ __forceinline__ void frag_sum_sq(const bf16x8& f, float& s1, float& s2) {
    const u32x4 u = __builtin_bit_cast(u32x4, f);
#pragma unroll
    for (int e = 0; e < 4; ++e) { s1 = dot2(u[e], 0x3f803f80u, s1); s2 = dot2(u[e], u[e], s2); }
}
__device__ __forceinline__ float red_g(float v) { v += __shfl_xor(v, 16); v += __shfl_xor(v, 32); return v; }
template <int N> __device__ __forceinline__ void wait_vm() { asm volatile("s_waitcnt vmcnt(%0)" ::"n"(N) : "memory"); }

enum { EPI_STORE = 0, EPI_RES = 1 };

template <int BM, int BN, int WM, int WN, int NST, int EPI, int LNF, int RM, int RN>
__global__ __launch_bounds__(WM * WN * 64) void gemm_kernel(
    const bf16_t* __restrict__ X, const bf16_t* __restrict__ W0,
    const bf16_t* __restrict__ R, bf16_t* __restrict__ C, int M, int N, int K, int ldw, int ldc) {
    constexpr int NWAVE = WM * WN;
    constexpr int MF = BM / 16 / WM, NF = BN / 16 / WN, NB = BN / 16;
    constexpr int A_BYTES = BM * 128, B_BYTES = BN * 128, ST_BYTES = A_BYTES + B_BYTES;
    constexpr int LPW = (BM + BN) / 8 / NWAVE;
    constexpr int LPA = BM / 8 / NWAVE;
    static_assert((BM + BN) % (8 * NWAVE) == 0 && (BM % (8 * NWAVE)) == 0, "piece split");
    static_assert(NB % 4 == 0, "NB%4");
    static_assert(!LNF || (MF == WN && WM == 2), "LN fold layout");
    extern __shared__ __attribute__((aligned(16))) char smem[];
    LAS char* lds = (LAS char*)smem;

    const int tid = threadIdx.x, lane = tid & 63;
    const int wave = __builtin_amdgcn_readfirstlane(tid >> 6);
    const int wm = wave / WN, wn = wave % WN;

    const int tiles_m = M / BM, tiles_n = N / BN;
    int tm, tn;
    {
        const int xcd = blockIdx.x & 7, idx = blockIdx.x >> 3;
        const int rm = xcd / RN, rn = xcd % RN;
        const int sm = tiles_m / RM, sn = tiles_n / RN;
        tm = rm * sm + idx % sm; tn = rn * sn + idx / sm;
    }
    const int m0 = tm * BM, n0 = tn * BN;

    unsigned voff[LPW];
    const char* sbase[LPW];
#pragma unroll
    for (int j = 0; j < LPW; ++j) {
        const int ii = wave + NWAVE * j;
        if (j < LPA) {
            const int row = ii * 8 + (lane >> 3), ch = (lane & 7) ^ (row & 7);
            voff[j] = (unsigned)(row * K * 2 + ch * 16);
            sbase[j] = (const char*)(X + (size_t)m0 * K);
        } else {
            const int pi = ii - BM / 8;
            const int kb = pi / (NB / 4), nb = 4 * (pi % (NB / 4)) + (lane >> 4);
            const int r = ((lane & 15) >> 1) ^ (4 * (kb & 1)), half = lane & 1;
            voff[j] = (unsigned)(((8 * kb + r) * ldw + 16 * nb + 8 * half) * 2);
            sbase[j] = (const char*)(W0 + n0);
        }
    }
    const unsigned lds_base = (unsigned)(uintptr_t)lds;
    const int NT = K / 64;
    auto issue = [&](int buf) {
#pragma unroll
        for (int j = 0; j < LPW; ++j) {
            const int ii = wave + NWAVE * j;
            glds16(sbase[j], voff[j], lds_base + buf * ST_BYTES + ii * 1024);
            sbase[j] += (j < LPA) ? 128 : (long)128 * ldw;
        }
    };

    const int fr = lane & 15, g = lane >> 4, q = (lane & 15) >> 2, p = lane & 3;
    const int rotm = LNF ? wn : 0, rotk = LNF ? wm : 0;
    const int xrow = wm * (MF * 16) + fr;
    const int xoff0 = xrow * 128 + ((g ^ (xrow & 7)) << 4);
    const int woff0 = A_BYTES + g * NB * 256 + (q + 4 * (g & 1)) * 32 + p * 8 + wn * NF * 256;

    f32x4 acc[MF][NF];
#pragma unroll
    for (int i = 0; i < MF; ++i)
#pragma unroll
        for (int j = 0; j < NF; ++j) acc[i][j] = (f32x4){0.f, 0.f, 0.f, 0.f};
    float s1 = 0.f, s2 = 0.f, cs[NF];
#pragma unroll
    for (int j = 0; j < NF; ++j) cs[j] = 0.f;

#pragma unroll
    for (int s = 0; s < NST - 1; ++s) issue(s);

    auto read_frags = [&](LAS const char* base, int slot, bf16x8 (&xf)[MF], bf16x8 (&wf)[NF]) {
        const int ks = slot ^ rotk;
#pragma unroll
        for (int j = 0; j < NF; ++j) {
            const int o = ks * (4 * NB * 256) + j * 256;
            s16x4 lo = tr_read(base + woff0 + o);
            s16x4 hi = tr_read(base + (woff0 ^ 128) + o);
            wf[j] = (bf16x8){lo[0], lo[1], lo[2], lo[3], hi[0], hi[1], hi[2], hi[3]};
        }
#pragma unroll
        for (int i = 0; i < MF; ++i)
            xf[i] = *(LAS const bf16x8*)(base + ((xoff0 ^ (ks * 64)) + (i ^ rotm) * 2048));
    };
    auto do_mfma = [&](bf16x8 (&xf)[MF], bf16x8 (&wf)[NF], bool colsum) {
#pragma unroll
        for (int i = 0; i < MF; ++i)
#pragma unroll
            for (int j = 0; j < NF; ++j)
                acc[i][j] = __builtin_amdgcn_mfma_f32_16x16x32_bf16(wf[j], xf[i], acc[i][j], 0, 0, 0);
        if (LNF) {
            frag_sum_sq(xf[0], s1, s2);
            asm volatile("" : "+v"(s1), "+v"(s2));
            if (colsum) {
#pragma unroll
                for (int j = 0; j < NF; ++j) { frag_sum(wf[j], cs[j]); asm volatile("" : "+v"(cs[j])); }
            }
        }
    };
    auto interleave = [&]() {
        constexpr int NM = MF * NF, NR = MF + 2 * NF, RPM = (NR + NM - 1) / NM;
#pragma unroll
        for (int i = 0; i < NM; ++i) {
            __builtin_amdgcn_sched_group_barrier(0x8, 1, 0);
            __builtin_amdgcn_sched_group_barrier(0x100, RPM, 0);
            if (LNF) __builtin_amdgcn_sched_group_barrier(0x2, 2, 0);
        }
    };
    bf16x8 xa[MF], wa[NF], xb[MF], wb[NF];
    wait_vm<(NST - 2) * LPW>();
    __builtin_amdgcn_s_barrier();
    asm volatile("" ::: "memory");
    read_frags(lds, 0, xa, wa);

    int buf = 0, pbuf = NST - 1;
    for (int kt = 0; kt < NT; ++kt) {
        const int rem = NT - 2 - kt;
        if (rem >= NST - 3) wait_vm<(NST - 3) * LPW>();
        else if (NST > 4 && rem == 1) wait_vm<LPW>();
        else if (NST > 5 && rem == 2) wait_vm<2 * LPW>();
        else wait_vm<0>();
        __builtin_amdgcn_s_barrier();
        asm volatile("" ::: "memory");
        if (kt + NST - 1 < NT) issue(pbuf);
        const int nbuf = (buf + 1 == NST) ? 0 : buf + 1;
        __builtin_amdgcn_sched_barrier(0);
        read_frags(lds + buf * ST_BYTES, 1, xb, wb);
        do_mfma(xa, wa, true);
        interleave();
        __builtin_amdgcn_sched_barrier(0);
        read_frags(lds + nbuf * ST_BYTES, 0, xa, wa);
        do_mfma(xb, wb, false);
        interleave();
        __builtin_amdgcn_sched_barrier(0);
        pbuf = buf; buf = nbuf;
    }

    LAS float* rowstat = (LAS float*)lds;
    LAS float* colsum = (LAS float*)(lds + BM * 8);
    if (LNF) {
        s1 = red_g(s1); s2 = red_g(s2);
#pragma unroll
        for (int j = 0; j < NF; ++j) cs[j] = red_g(cs[j]);
        __syncthreads();
        if (g == 0) {
            const int r = wm * (MF * 16) + wn * 16 + fr;
            rowstat[2 * r] = s1; rowstat[2 * r + 1] = s2;
#pragma unroll
            for (int j = 0; j < NF; ++j) colsum[wm * BN + (wn * NF + j) * 16 + fr] = cs[j];
        }
        __syncthreads();
    }
    f32x4 csv[NF];
    if (LNF) {
#pragma unroll
        for (int j = 0; j < NF; ++j) {
            const int c = (wn * NF + j) * 16 + 4 * g;
            csv[j] = *(LAS const f32x4*)(colsum + c) + *(LAS const f32x4*)(colsum + BN + c);
        }
    }
#pragma unroll
    for (int i = 0; i < MF; ++i) {
        const int rl = wm * (MF * 16) + (i ^ rotm) * 16 + fr;
        const int m = m0 + rl;
        float mean = 0.f, rstd = 1.f;
        if (LNF) {
            const float a = rowstat[2 * rl], b = rowstat[2 * rl + 1];
            mean = a * (1.0f / D_MODEL);
            const float var = fmaxf(b * (1.0f / D_MODEL) - mean * mean, 0.f);
            rstd = 1.0f / sqrtf(var + 1e-5f);
        }
#pragma unroll
        for (int j = 0; j < NF; ++j) {
            const int n = n0 + (wn * NF + j) * 16 + 4 * g;
            f32x4 v = acc[i][j];
            if (LNF) v = (v - csv[j] * mean) * rstd;
            if (EPI == EPI_RES) {
                const u32x2 rv = *(const u32x2*)(R + (size_t)m * ldc + n);
                v[0] += bf2f(rv[0] & 0xffffu); v[1] += bf2f(rv[0] >> 16);
                v[2] += bf2f(rv[1] & 0xffffu); v[3] += bf2f(rv[1] >> 16);
            }
            u32x2 w; w[0] = pk_bf16(v[0], v[1]); w[1] = pk_bf16(v[2], v[3]);
            *(u32x2*)(C + (size_t)m * ldc + n) = w;
        }
    }
}

__global__ __launch_bounds__(512, 2) void glu_gemm_kernel(
    const bf16_t* __restrict__ X, const bf16_t* __restrict__ W1, const bf16_t* __restrict__ W3,
    bf16_t* __restrict__ C) {
    constexpr int K = D_MODEL, LDW = F_DIM, HTB = 16384;
    extern __shared__ __attribute__((aligned(16))) char smem[];
    LAS char* lds = (LAS char*)smem;
    const unsigned lds_base = (unsigned)(uintptr_t)lds;
    const int tid = threadIdx.x, lane = tid & 63;
    const int wave = __builtin_amdgcn_readfirstlane(tid >> 6);
    const int wr = wave >> 2, wc = wave & 3;
    int tm, tn;
    { const int xcd = blockIdx.x & 7, idx = blockIdx.x >> 3; tm = (xcd >> 2) * 4 + (idx & 3); tn = (xcd & 3) * 7 + (idx >> 2); }
    const int m0 = tm * 256, n0 = tn * 128;

    unsigned xoffs[2], woffs[2];
#pragma unroll
    for (int j = 0; j < 2; ++j) {
        const int pc = wave + 8 * j;
        const int row = pc * 8 + (lane >> 3), ch = (lane & 7) ^ (row & 7);
        xoffs[j] = (unsigned)(row * K * 2 + ch * 16);
        const int kb = pc >> 1, nb = 4 * (pc & 1) + (lane >> 4);
        const int r = ((lane & 15) >> 1) ^ (4 * (kb & 1)), half = lane & 1;
        woffs[j] = (unsigned)(((8 * kb + r) * LDW + 16 * nb + 8 * half) * 2);
    }
    const char* xsrc = (const char*)(X + (size_t)m0 * K);
    const char* w1src = (const char*)(W1 + n0);
    const char* w3src = (const char*)(W3 + n0);
#define SA_OFF(b, h) (((b) * 2 + (h)) * HTB)
#define SB_OFF(b, h) ((4 + (b) * 2 + (h)) * HTB)
#define STAGE_A(b, h, kt) do { const char* _s = xsrc + (size_t)(h) * 128 * K * 2 + (kt) * 128; \
        glds16(_s, xoffs[0], lds_base + SA_OFF(b, h) + wave * 1024); glds16(_s, xoffs[1], lds_base + SA_OFF(b, h) + (wave + 8) * 1024); } while (0)
#define STAGE_B(b, h, kt) do { const char* _s = ((h) ? w3src : w1src) + (size_t)(kt) * 64 * LDW * 2; \
        glds16(_s, woffs[0], lds_base + SB_OFF(b, h) + wave * 1024); glds16(_s, woffs[1], lds_base + SB_OFF(b, h) + (wave + 8) * 1024); } while (0)
    const int fr = lane & 15, g = lane >> 4, q = (lane & 15) >> 2, p = lane & 3;
    const int xrow = wr * 64 + fr;
    const int xo0 = (xrow * 128 + ((g ^ (xrow & 7)) << 4)) ^ (wr * 64);
    const int wo0 = g * 2048 + (q + 4 * (g & 1)) * 32 + p * 8 + wc * 512;
    unsigned xb_[4][2], wb_[2][2];
#pragma unroll
    for (int m = 0; m < 4; ++m)
#pragma unroll
        for (int k = 0; k < 2; ++k) { xb_[m][k] = lds_base + ((xo0 ^ (k * 64)) + (m ^ wc) * 2048); asm volatile("" : "+v"(xb_[m][k])); }
#pragma unroll
    for (int t = 0; t < 2; ++t)
#pragma unroll
        for (int k = 0; k < 2; ++k) { wb_[t][k] = lds_base + 4 * HTB + ((wo0 ^ (t * 128)) + (k ^ wr) * 8192); asm volatile("" : "+v"(wb_[t][k])); }
#define LDSP(a) ((LAS const char*)(uintptr_t)(a))
#define LDA(dst, b, h) do { _Pragma("unroll") for (int m = 0; m < 4; ++m) _Pragma("unroll") for (int k = 0; k < 2; ++k) \
        dst[m][k] = *(LAS const bf16x8*)(LDSP(xb_[m][k]) + SA_OFF(b, h)); } while (0)
#define LDB(dst, b, h) do { _Pragma("unroll") for (int n = 0; n < 2; ++n) _Pragma("unroll") for (int k = 0; k < 2; ++k) { \
        s16x4 lo_ = tr_read(LDSP(wb_[0][k]) + SA_OFF(b, h) + n * 256); \
        s16x4 hi_ = tr_read(LDSP(wb_[1][k]) + SA_OFF(b, h) + n * 256); \
        dst[n][k] = (bf16x8){lo_[0], lo_[1], lo_[2], lo_[3], hi_[0], hi_[1], hi_[2], hi_[3]}; } } while (0)
#define MMA(ai, bj, At, Bt, ST) do { __builtin_amdgcn_s_setprio(1); \
        _Pragma("unroll") for (int m = 0; m < 4; ++m) _Pragma("unroll") for (int n = 0; n < 2; ++n) _Pragma("unroll") for (int k = 0; k < 2; ++k) \
            acc[ai][bj][m][n] = __builtin_amdgcn_mfma_f32_16x16x32_bf16(Bt[n][k], At[m][k], acc[ai][bj][m][n], 0, 0, 0); \
        if ((ST) & 1) { frag_sum_sq(At[0][0], s1[ai], s2[ai]); frag_sum_sq(At[0][1], s1[ai], s2[ai]); \
                        asm volatile("" : "+v"(s1[ai]), "+v"(s2[ai])); } \
        if ((ST) & 2) { frag_sum(Bt[0][0], cs[bj][0]); frag_sum(Bt[1][0], cs[bj][1]); \
                        asm volatile("" : "+v"(cs[bj][0]), "+v"(cs[bj][1])); } \
        __builtin_amdgcn_s_setprio(0); __builtin_amdgcn_sched_barrier(0); } while (0)
#define WAIT_V(n) asm volatile("s_waitcnt vmcnt(" #n ")" ::: "memory")
#define WAIT_L(n) asm volatile("s_waitcnt lgkmcnt(" #n ")" ::: "memory")
#define BAR __builtin_amdgcn_s_barrier()
#define SCHED __builtin_amdgcn_sched_barrier(0)
    f32x4 acc[2][2][4][2] = {};
    bf16x8 At[4][2], B0[2][2], B1[2][2];
    float s1[2] = {0.f, 0.f}, s2[2] = {0.f, 0.f}, cs[2][2] = {{0.f, 0.f}, {0.f, 0.f}};
    constexpr int nt = K / 64;
    STAGE_B(0, 0, 0); STAGE_A(0, 0, 0); STAGE_B(0, 1, 0); STAGE_A(0, 1, 0);
    if (wr == 1) BAR;
    WAIT_V(4); BAR;
    STAGE_B(1, 0, 1); STAGE_A(1, 0, 1); STAGE_B(1, 1, 1);
    WAIT_V(6); BAR;
    for (int t = 0; t < nt; t += 2) {
        const bool more = t + 2 < nt;
        LDB(B0, 0, 0); SCHED; LDA(At, 0, 0); STAGE_A(1, 1, t + 1);
        WAIT_L(8); BAR; WAIT_L(0); MMA(0, 0, At, B0, 2); BAR; SCHED;
        LDB(B1, 0, 1); if (more) STAGE_B(0, 0, t + 2);
        BAR; WAIT_L(0); MMA(0, 1, At, B1, 1); BAR;
        LDA(At, 0, 1); if (more) STAGE_A(0, 0, t + 2);
        BAR; WAIT_L(0); MMA(1, 0, At, B0, 1); BAR; SCHED;
        if (more) { STAGE_B(0, 1, t + 2); WAIT_V(6); } else { WAIT_V(0); }
        BAR; MMA(1, 1, At, B1, 2); BAR;
        LDB(B0, 1, 0); SCHED; LDA(At, 1, 0); if (more) STAGE_A(0, 1, t + 2);
        WAIT_L(8); BAR; WAIT_L(0); MMA(0, 0, At, B0, 2); BAR; SCHED;
        LDB(B1, 1, 1); if (more) STAGE_B(1, 0, t + 3);
        BAR; WAIT_L(0); MMA(0, 1, At, B1, 1); BAR;
        LDA(At, 1, 1); if (more) STAGE_A(1, 0, t + 3);
        BAR; WAIT_L(0); MMA(1, 0, At, B0, 1); BAR; SCHED;
        if (more) { STAGE_B(1, 1, t + 3); WAIT_V(6); }
        BAR; MMA(1, 1, At, B1, 2); BAR;
    }
    if (wr == 0) BAR;
    LAS float* rowstat = (LAS float*)lds;
    LAS float* colsum = (LAS float*)(lds + 2048);
#pragma unroll
    for (int a = 0; a < 2; ++a) {
        s1[a] = red_g(s1[a]); s2[a] = red_g(s2[a]);
        cs[a][0] = red_g(cs[a][0]); cs[a][1] = red_g(cs[a][1]);
    }
    if (g == 0) {
#pragma unroll
        for (int a = 0; a < 2; ++a) {
            const int r = a * 128 + wr * 64 + wc * 16 + fr;
            rowstat[2 * r] = s1[a]; rowstat[2 * r + 1] = s2[a];
            colsum[wr * 256 + a * 128 + wc * 32 + fr] = cs[a][0];
            colsum[wr * 256 + a * 128 + wc * 32 + 16 + fr] = cs[a][1];
        }
    }
    __syncthreads();
    f32x4 csv[2][2];
#pragma unroll
    for (int b = 0; b < 2; ++b)
#pragma unroll
        for (int n = 0; n < 2; ++n) {
            const int c = b * 128 + wc * 32 + n * 16 + 4 * g;
            csv[b][n] = *(LAS const f32x4*)(colsum + c) + *(LAS const f32x4*)(colsum + 256 + c);
        }
#pragma unroll
    for (int ai = 0; ai < 2; ++ai)
#pragma unroll
        for (int m = 0; m < 4; ++m) {
            const int rl = ai * 128 + wr * 64 + (m ^ wc) * 16 + fr;
            const float sa = rowstat[2 * rl], sb = rowstat[2 * rl + 1];
            const float mean = sa * (1.0f / D_MODEL);
            const float rstd = 1.0f / sqrtf(fmaxf(sb * (1.0f / D_MODEL) - mean * mean, 0.f) + 1e-5f);
#pragma unroll
            for (int n = 0; n < 2; ++n) {
                const f32x4 gv = (acc[ai][0][m][n] - csv[0][n] * mean) * rstd;
                const f32x4 uv = (acc[ai][1][m][n] - csv[1][n] * mean) * rstd;
                float o[4];
#pragma unroll
                for (int r = 0; r < 4; ++r) o[r] = gv[r] * __builtin_amdgcn_rcpf(1.0f + __expf(-gv[r])) * uv[r];
                u32x2 w; w[0] = pk_bf16(o[0], o[1]); w[1] = pk_bf16(o[2], o[3]);
                *(u32x2*)(C + (size_t)(m0 + rl) * LDW + n0 + wc * 32 + n * 16 + 4 * g) = w;
            }
        }
#undef SA_OFF
#undef SB_OFF
#undef STAGE_A
#undef STAGE_B
#undef LDA
#undef LDB
#undef MMA
}

__global__ __launch_bounds__(512) void attn_kernel(const bf16_t* __restrict__ qkv, bf16_t* __restrict__ O) {
    extern __shared__ __attribute__((aligned(16))) char smem[];
    LAS char* lds = (LAS char*)smem;
    LAS char* ldsK = lds;
    LAS char* ldsV = lds + 32768;
    const int tid = threadIdx.x, lane = tid & 63;
    const int w = __builtin_amdgcn_readfirstlane(tid >> 6);
    const int h = blockIdx.x & 31, qb = blockIdx.x >> 5;
    const int q0 = qb * 256, kbase = q0 - 256;
    constexpr int LD = 3 * D_MODEL;

    {
        u32x4 kv[4], vv[4];
#pragma unroll
        for (int i = 0; i < 4; ++i) {
            const int idx = tid + 512 * i, key = idx >> 2, ch = idx & 3;
            const int gk = kbase + key;
            if (gk >= 0) {
                const bf16_t* src = qkv + (size_t)gk * LD + h * 32 + ch * 8;
                kv[i] = *(const u32x4*)(src + D_MODEL);
                vv[i] = *(const u32x4*)(src + 2 * D_MODEL);
            }
        }
#pragma unroll
        for (int i = 0; i < 4; ++i) {
            const int idx = tid + 512 * i, key = idx >> 2, ch = idx & 3;
            const int gk = kbase + key;
            if (gk >= 0) {
                *(LAS u32x4*)(ldsK + key * 64 + ((ch ^ ((key >> 2) & 3)) << 4)) = kv[i];
                *(LAS u32x4*)(ldsV + key * 64 + (ch << 4)) = vv[i];
            }
        }
    }
    const int r32 = lane & 31, hi = lane >> 5;
    const int qw = q0 + 32 * w;
    bf16x8 qf[2];
#pragma unroll
    for (int s = 0; s < 2; ++s)
        qf[s] = *(const bf16x8*)(qkv + (size_t)(qw + r32) * LD + h * 32 + 16 * s + 8 * hi);
    __syncthreads();

    f32x16 sc[9];
    const float NEG = -INFINITY;
#pragma unroll
    for (int j = 0; j < 9; ++j) {
        const int kb = qw - 256 + 32 * j;
        if (kb >= 0) {
            const int key = 32 * (w + j) + r32;
            f32x16 a = {};
#pragma unroll
            for (int s = 0; s < 2; ++s) {
                const bf16x8 kf = *(LAS const bf16x8*)(ldsK + key * 64 + (((2 * s + hi) ^ ((key >> 2) & 3)) << 4));
                a = __builtin_amdgcn_mfma_f32_32x32x16_bf16(kf, qf[s], a, 0, 0, 0);
            }
            sc[j] = a;
        } else {
#pragma unroll
            for (int r = 0; r < 16; ++r) sc[j][r] = NEG;
        }
    }
#pragma unroll
    for (int r = 0; r < 16; ++r) {
        const int i = (r & 3) + 8 * (r >> 2) + 4 * hi;
        if (!(i > r32)) sc[0][r] = NEG;
        if (!(i <= r32)) sc[8][r] = NEG;
    }
    float mx = NEG;
#pragma unroll
    for (int j = 0; j < 9; ++j)
#pragma unroll
        for (int r = 0; r < 16; ++r) mx = fmaxf(mx, sc[j][r]);
    mx = fmaxf(mx, __shfl_xor(mx, 32));
    const float c = 0.17677669529663687f * 1.4426950408889634f;
    const float nm = -mx * c;
    float l = 0.f;
#pragma unroll
    for (int j = 0; j < 9; ++j)
#pragma unroll
        for (int r = 0; r < 16; ++r) {
            const float e = __builtin_amdgcn_exp2f(__builtin_fmaf(sc[j][r], c, nm));
            sc[j][r] = e; l += e;
        }
    l += __shfl_xor(l, 32);

    f32x16 o = {};
    const int gq = (lane & 15) >> 2, gp = lane & 3, gd = (lane >> 4) & 1;
#pragma unroll
    for (int j = 0; j < 9; ++j) {
        const int kb = qw - 256 + 32 * j;
        if (kb >= 0) {
#pragma unroll
            for (int s = 0; s < 2; ++s) {
                u32x4 pw;
#pragma unroll
                for (int e = 0; e < 4; ++e) pw[e] = pk_bf16(sc[j][8 * s + 2 * e], sc[j][8 * s + 2 * e + 1]);
                const int key = 32 * (w + j) + 16 * s + 4 * hi + gq;
                LAS const char* vp = ldsV + key * 64 + (16 * gd + 4 * gp) * 2;
                s16x4 lo = tr_read(vp), hi8 = tr_read(vp + 8 * 64);
                const bf16x8 vf = (bf16x8){lo[0], lo[1], lo[2], lo[3], hi8[0], hi8[1], hi8[2], hi8[3]};
                o = __builtin_amdgcn_mfma_f32_32x32x16_bf16(vf, __builtin_bit_cast(bf16x8, pw), o, 0, 0, 0);
            }
        }
    }
    const float rl = 1.0f / l;
    bf16_t* dst = O + (size_t)(qw + r32) * D_MODEL + h * 32 + 4 * hi;
#pragma unroll
    for (int t = 0; t < 4; ++t) {
        u32x2 wv; wv[0] = pk_bf16(o[4 * t] * rl, o[4 * t + 1] * rl); wv[1] = pk_bf16(o[4 * t + 2] * rl, o[4 * t + 3] * rl);
        *(u32x2*)(dst + 8 * t) = wv;
    }
}

template <typename KF> static void set_lds(KF kf, int bytes) {
    (void)hipFuncSetAttribute((const void*)kf, hipFuncAttributeMaxDynamicSharedMemorySize, bytes);
}

extern "C" void kernel_launch(void* const* d_in, const int* in_sizes, int n_in,
                              void* d_out, int out_size, void* d_ws, size_t ws_size,
                              hipStream_t stream) {
    const bf16_t* x = (const bf16_t*)d_in[0];
    const bf16_t* wqkv = (const bf16_t*)d_in[1];
    const bf16_t* wo = (const bf16_t*)d_in[2];
    const bf16_t* w1 = (const bf16_t*)d_in[4];
    const bf16_t* w3 = (const bf16_t*)d_in[5];
    const bf16_t* w2 = (const bf16_t*)d_in[6];
    bf16_t* out = (bf16_t*)d_out;
    char* ws = (char*)d_ws;
    bf16_t* qkv = (bf16_t*)(ws + (4u << 20));
    bf16_t* att = (bf16_t*)(ws + (16u << 20));
    bf16_t* hbuf = (bf16_t*)(ws + (20u << 20));
    bf16_t* act = (bf16_t*)(ws + (28u << 20));

    {
        auto kf = gemm_kernel<128, 192, 2, 4, 4, EPI_STORE, 1, 2, 4>;
        constexpr int lds = 4 * (128 + 192) * 128;
        set_lds(kf, lds);
        kf<<<256, 512, lds, stream>>>(x, wqkv, nullptr, qkv, S_LEN, 3072, 1024, 3072, 3072);
    }
    {
        set_lds(attn_kernel, 65536);
        attn_kernel<<<256, 512, 65536, stream>>>(qkv, att);
    }
    {
        auto kf = gemm_kernel<128, 64, 4, 2, 6, EPI_RES, 0, 4, 2>;
        constexpr int lds = 6 * (128 + 64) * 128;
        set_lds(kf, lds);
        kf<<<256, 512, lds, stream>>>(att, wo, x, hbuf, S_LEN, 1024, 1024, 1024, 1024);
    }
    {
        set_lds(glu_gemm_kernel, 131072);
        glu_gemm_kernel<<<224, 512, 131072, stream>>>(hbuf, w1, w3, act);
    }
    {
        auto kf = gemm_kernel<128, 64, 4, 2, 6, EPI_RES, 0, 4, 2>;
        constexpr int lds = 6 * (128 + 64) * 128;
        kf<<<256, 512, lds, stream>>>(act, w2, hbuf, out, S_LEN, 1024, F_DIM, 1024, 1024);
    }
}
```

```cpp
#include <hip/hip_runtime.h>
#include <hip/hip_bf16.h>
#include <stdint.h>

typedef unsigned short bf16_t;
typedef short bf16x8 __attribute__((ext_vector_type(8)));
typedef short s16x4 __attribute__((ext_vector_type(4)));
typedef float f32x4 __attribute__((ext_vector_type(4)));
typedef float f32x16 __attribute__((ext_vector_type(16)));
typedef unsigned u32x4 __attribute__((ext_vector_type(4)));
typedef unsigned u32x2 __attribute__((ext_vector_type(2)));
#define LAS __attribute__((address_space(3)))

constexpr int S_LEN = 2048, D_MODEL = 1024, F_DIM = 3584, N_HEAD = 32, D_HEAD = 32, WIN = 256;

__device__ __forceinline__ float bf2f(unsigned v16) { return __uint_as_float(v16 << 16); }
__device__ __forceinline__ unsigned pk_bf16(float lo, float hi) {
    unsigned r; asm("v_cvt_pk_bf16_f32 %0, %1, %2" : "=v"(r) : "v"(lo), "v"(hi)); return r;
}
__device__ __forceinline__ s16x4 tr_read(LAS const char* p) {
    return __builtin_amdgcn_ds_read_tr16_b64_v4i16((LAS s16x4*)p);
}
__device__ __forceinline__ void glds16(const void* sbase, unsigned voff, unsigned lds_addr) {
    unsigned keep;
    asm volatile("s_mov_b32 %0, m0\n\ts_mov_b32 m0, %3\n\ts_nop 0\n\tglobal_load_lds_dwordx4 %2, %1\n\ts_mov_b32 m0, %0"
                 : "=&s"(keep) : "s"(sbase), "v"(voff), "s"(lds_addr) : "memory");
}
typedef __bf16 bf16x2_t __attribute__((ext_vector_type(2)));
__device__ __forceinline__ float dot2(unsigned a, unsigned b, float acc) {
    return __builtin_amdgcn_fdot2_f32_bf16(__builtin_bit_cast(bf16x2_t, a), __builtin_bit_cast(bf16x2_t, b), acc, false);
}
__device__ __forceinline__ void frag_sum(const bf16x8& f, float& s1) {
    const u32x4 u = __builtin_bit_cast(u32x4, f);
#pragma unroll
    for (int e = 0; e < 4; ++e) s1 = dot2(u[e], 0x3f803f80u, s1);
}
__device__ __forceinline__ void frag_sum_sq(const bf16x8& f, float& s1, float& s2) {
    const u32x4 u = __builtin_bit_cast(u32x4, f);
#pragma unroll
    for (int e = 0; e < 4; ++e) { s1 = dot2(u[e], 0x3f803f80u, s1); s2 = dot2(u[e], u[e], s2); }
}
__device__ __forceinline__ float red_g(float v) { v += __shfl_xor(v, 16); v += __shfl_xor(v, 32); return v; }
template <int N> __device__ __forceinline__ void wait_vm() { asm volatile("s_waitcnt vmcnt(%0)" ::"n"(N) : "memory"); }

enum { EPI_STORE = 0, EPI_RES = 1, EPI_RES_STAT = 2 };

template <int BM, int BN, int WM, int WN, int NST, int EPI, int LNF, int RM, int RN>
__global__ __launch_bounds__(WM * WN * 64) void gemm_kernel(
    const bf16_t* __restrict__ X, const bf16_t* __restrict__ W0,
    const bf16_t* __restrict__ R, bf16_t* __restrict__ C, float* __restrict__ stat_out,
    int M, int N, int K, int ldw, int ldc) {
    constexpr int NWAVE = WM * WN;
    constexpr int MF = BM / 16 / WM, NF = BN / 16 / WN, NB = BN / 16;
    constexpr int A_BYTES = BM * 128, B_BYTES = BN * 128, ST_BYTES = A_BYTES + B_BYTES;
    constexpr int LPW = (BM + BN) / 8 / NWAVE;
    constexpr int LPA = BM / 8 / NWAVE;
    static_assert((BM + BN) % (8 * NWAVE) == 0 && (BM % (8 * NWAVE)) == 0, "piece split");
    static_assert(NB % 4 == 0, "NB%4");
    static_assert(!LNF || (MF == WN && WM == 2), "LN fold layout");
    extern __shared__ __attribute__((aligned(16))) char smem[];
    LAS char* lds = (LAS char*)smem;

    const int tid = threadIdx.x, lane = tid & 63;
    const int wave = __builtin_amdgcn_readfirstlane(tid >> 6);
    const int wm = wave / WN, wn = wave % WN;

    const int tiles_m = M / BM, tiles_n = N / BN;
    int tm, tn;
    {
        const int xcd = blockIdx.x & 7, idx = blockIdx.x >> 3;
        const int rm = xcd / RN, rn = xcd % RN;
        const int sm = tiles_m / RM, sn = tiles_n / RN;
        tm = rm * sm + idx % sm; tn = rn * sn + idx / sm;
    }
    const int m0 = tm * BM, n0 = tn * BN;

    unsigned voff[LPW];
    const char* sbase[LPW];
#pragma unroll
    for (int j = 0; j < LPW; ++j) {
        const int ii = wave + NWAVE * j;
        if (j < LPA) {
            const int row = ii * 8 + (lane >> 3), ch = (lane & 7) ^ (row & 7);
            voff[j] = (unsigned)(row * K * 2 + ch * 16);
            sbase[j] = (const char*)(X + (size_t)m0 * K);
        } else {
            const int pi = ii - BM / 8;
            const int kb = pi / (NB / 4), nb = 4 * (pi % (NB / 4)) + (lane >> 4);
            const int r = ((lane & 15) >> 1) ^ (4 * (kb & 1)), half = lane & 1;
            voff[j] = (unsigned)(((8 * kb + r) * ldw + 16 * nb + 8 * half) * 2);
            sbase[j] = (const char*)(W0 + n0);
        }
    }
    const unsigned lds_base = (unsigned)(uintptr_t)lds;
    const int NT = K / 64;
    auto issue = [&](int buf) {
#pragma unroll
        for (int j = 0; j < LPW; ++j) {
            const int ii = wave + NWAVE * j;
            glds16(sbase[j], voff[j], lds_base + buf * ST_BYTES + ii * 1024);
            sbase[j] += (j < LPA) ? 128 : (long)128 * ldw;
        }
    };

    const int fr = lane & 15, g = lane >> 4, q = (lane & 15) >> 2, p = lane & 3;
    const int rotm = LNF ? wn : 0, rotk = LNF ? wm : 0;
    const int xrow = wm * (MF * 16) + fr;
    const int xoff0 = xrow * 128 + ((g ^ (xrow & 7)) << 4);
    const int woff0 = A_BYTES + g * NB * 256 + (q + 4 * (g & 1)) * 32 + p * 8 + wn * NF * 256;

    f32x4 acc[MF][NF];
#pragma unroll
    for (int i = 0; i < MF; ++i)
#pragma unroll
        for (int j = 0; j < NF; ++j) acc[i][j] = (f32x4){0.f, 0.f, 0.f, 0.f};
    f32x4 s1 = {0.f, 0.f, 0.f, 0.f}, s2 = {0.f, 0.f, 0.f, 0.f}, cs[NF];
#pragma unroll
    for (int j = 0; j < NF; ++j) cs[j] = (f32x4){0.f, 0.f, 0.f, 0.f};
    const bf16x8 ones = {0x3f80, 0x3f80, 0x3f80, 0x3f80, 0x3f80, 0x3f80, 0x3f80, 0x3f80};

#pragma unroll
    for (int s = 0; s < NST - 1; ++s) issue(s);

    auto read_frags = [&](LAS const char* base, int slot, bf16x8 (&xf)[MF], bf16x8 (&wf)[NF]) {
        const int ks = slot ^ rotk;
#pragma unroll
        for (int j = 0; j < NF; ++j) {
            const int o = ks * (4 * NB * 256) + j * 256;
            s16x4 lo = tr_read(base + woff0 + o);
            s16x4 hi = tr_read(base + (woff0 ^ 128) + o);
            wf[j] = (bf16x8){lo[0], lo[1], lo[2], lo[3], hi[0], hi[1], hi[2], hi[3]};
        }
#pragma unroll
        for (int i = 0; i < MF; ++i)
            xf[i] = *(LAS const bf16x8*)(base + ((xoff0 ^ (ks * 64)) + (i ^ rotm) * 2048));
    };
    auto do_mfma = [&](bf16x8 (&xf)[MF], bf16x8 (&wf)[NF], bool colsum) {
#pragma unroll
        for (int i = 0; i < MF; ++i)
#pragma unroll
            for (int j = 0; j < NF; ++j)
                acc[i][j] = __builtin_amdgcn_mfma_f32_16x16x32_bf16(wf[j], xf[i], acc[i][j], 0, 0, 0);
        if (LNF) {
            s1 = __builtin_amdgcn_mfma_f32_16x16x32_bf16(ones, xf[0], s1, 0, 0, 0);
            s2 = __builtin_amdgcn_mfma_f32_16x16x32_bf16(xf[0], xf[0], s2, 0, 0, 0);
            if (colsum) {
#pragma unroll
                for (int j = 0; j < NF; ++j) cs[j] = __builtin_amdgcn_mfma_f32_16x16x32_bf16(wf[j], ones, cs[j], 0, 0, 0);
            }
        }
    };
    auto interleave = [&]() {
        constexpr int NM = MF * NF, NR = MF + 2 * NF, RPM = (NR + NM - 1) / NM;
#pragma unroll
        for (int i = 0; i < NM; ++i) {
            __builtin_amdgcn_sched_group_barrier(0x8, 1, 0);
            __builtin_amdgcn_sched_group_barrier(0x100, RPM, 0);
        }
    };
    bf16x8 xa[MF], wa[NF], xb[MF], wb[NF];
    wait_vm<(NST - 2) * LPW>();
    __builtin_amdgcn_s_barrier();
    asm volatile("" ::: "memory");
    read_frags(lds, 0, xa, wa);

    int buf = 0, pbuf = NST - 1;
    for (int kt = 0; kt < NT; ++kt) {
        const int rem = NT - 2 - kt;
        if (rem >= NST - 3) wait_vm<(NST - 3) * LPW>();
        else if (NST > 4 && rem == 1) wait_vm<LPW>();
        else if (NST > 5 && rem == 2) wait_vm<2 * LPW>();
        else wait_vm<0>();
        __builtin_amdgcn_s_barrier();
        asm volatile("" ::: "memory");
        if (kt + NST - 1 < NT) issue(pbuf);
        const int nbuf = (buf + 1 == NST) ? 0 : buf + 1;
        __builtin_amdgcn_sched_barrier(0);
        read_frags(lds + buf * ST_BYTES, 1, xb, wb);
        do_mfma(xa, wa, true);
        interleave();
        __builtin_amdgcn_sched_barrier(0);
        read_frags(lds + nbuf * ST_BYTES, 0, xa, wa);
        do_mfma(xb, wb, false);
        interleave();
        __builtin_amdgcn_sched_barrier(0);
        pbuf = buf; buf = nbuf;
    }

    LAS float* rowstat = (LAS float*)lds;
    LAS float* colsum = (LAS float*)(lds + BM * 8);
    if (LNF) {
        __syncthreads();
        if (g == (fr >> 2)) {
            const int r = wm * (MF * 16) + wn * 16 + fr;
            const int d = fr & 3;
            rowstat[2 * r] = s1[0];
            rowstat[2 * r + 1] = d == 0 ? s2[0] : d == 1 ? s2[1] : d == 2 ? s2[2] : s2[3];
        }
        if (fr == 0) {
#pragma unroll
            for (int j = 0; j < NF; ++j) *(LAS f32x4*)(colsum + wm * BN + (wn * NF + j) * 16 + 4 * g) = cs[j];
        }
        __syncthreads();
    }
    if (EPI == EPI_RES_STAT) __syncthreads();
    f32x4 csv[NF];
    if (LNF) {
#pragma unroll
        for (int j = 0; j < NF; ++j) {
            const int c = (wn * NF + j) * 16 + 4 * g;
            csv[j] = *(LAS const f32x4*)(colsum + c) + *(LAS const f32x4*)(colsum + BN + c);
        }
    }
#pragma unroll
    for (int i = 0; i < MF; ++i) {
        const int rl = wm * (MF * 16) + (i ^ rotm) * 16 + fr;
        const int m = m0 + rl;
        float mean = 0.f, rstd = 1.f, ps1 = 0.f, ps2 = 0.f;
        if (LNF) {
            const float a = rowstat[2 * rl], b = rowstat[2 * rl + 1];
            mean = a * (1.0f / D_MODEL);
            const float var = fmaxf(b * (1.0f / D_MODEL) - mean * mean, 0.f);
            rstd = 1.0f / sqrtf(var + 1e-5f);
        }
#pragma unroll
        for (int j = 0; j < NF; ++j) {
            const int n = n0 + (wn * NF + j) * 16 + 4 * g;
            f32x4 v = acc[i][j];
            if (LNF) v = (v - csv[j] * mean) * rstd;
            if (EPI != EPI_STORE) {
                const u32x2 rv = *(const u32x2*)(R + (size_t)m * ldc + n);
                v[0] += bf2f(rv[0] & 0xffffu); v[1] += bf2f(rv[0] >> 16);
                v[2] += bf2f(rv[1] & 0xffffu); v[3] += bf2f(rv[1] >> 16);
            }
            u32x2 w; w[0] = pk_bf16(v[0], v[1]); w[1] = pk_bf16(v[2], v[3]);
            *(u32x2*)(C + (size_t)m * ldc + n) = w;
            if (EPI == EPI_RES_STAT) {
                const float a0 = bf2f(w[0] & 0xffffu), a1 = bf2f(w[0] >> 16), a2 = bf2f(w[1] & 0xffffu), a3 = bf2f(w[1] >> 16);
                ps1 += (a0 + a1) + (a2 + a3);
                ps2 += (a0 * a0 + a1 * a1) + (a2 * a2 + a3 * a3);
            }
        }
        if (EPI == EPI_RES_STAT) {
            ps1 = red_g(ps1); ps2 = red_g(ps2);
            if (g == 0) { rowstat[(rl * WN + wn) * 2] = ps1; rowstat[(rl * WN + wn) * 2 + 1] = ps2; }
        }
    }
    if (EPI == EPI_RES_STAT) {
        __syncthreads();
        if (tid < BM) {
            float a = 0.f, b = 0.f;
#pragma unroll
            for (int w = 0; w < WN; ++w) { a += rowstat[(tid * WN + w) * 2]; b += rowstat[(tid * WN + w) * 2 + 1]; }
            float2 o; o.x = a; o.y = b;
            *(float2*)(stat_out + ((size_t)(m0 + tid) * tiles_n + tn) * 2) = o;
        }
    }
}

__global__ __launch_bounds__(512, 2) void glu_gemm_kernel(
    const bf16_t* __restrict__ X, const bf16_t* __restrict__ W1, const bf16_t* __restrict__ W3,
    const float* __restrict__ stat_in, bf16_t* __restrict__ C) {
    constexpr int K = D_MODEL, LDW = F_DIM, HTB = 16384;
    extern __shared__ __attribute__((aligned(16))) char smem[];
    LAS char* lds = (LAS char*)smem;
    const unsigned lds_base = (unsigned)(uintptr_t)lds;
    const int tid = threadIdx.x, lane = tid & 63;
    const int wave = __builtin_amdgcn_readfirstlane(tid >> 6);
    const int wr = wave >> 2, wc = wave & 3;
    int tm, tn;
    { const int xcd = blockIdx.x & 7, idx = blockIdx.x >> 3; tm = (xcd >> 2) * 4 + (idx & 3); tn = (xcd & 3) * 7 + (idx >> 2); }
    const int m0 = tm * 256, n0 = tn * 128;

    unsigned xoffs[2], woffs[2];
#pragma unroll
    for (int j = 0; j < 2; ++j) {
        const int pc = wave + 8 * j;
        const int row = pc * 8 + (lane >> 3), ch = (lane & 7) ^ (row & 7);
        xoffs[j] = (unsigned)(row * K * 2 + ch * 16);
        const int kb = pc >> 1, nb = 4 * (pc & 1) + (lane >> 4);
        const int r = ((lane & 15) >> 1) ^ (4 * (kb & 1)), half = lane & 1;
        woffs[j] = (unsigned)(((8 * kb + r) * LDW + 16 * nb + 8 * half) * 2);
    }
    const char* xsrc = (const char*)(X + (size_t)m0 * K);
    const char* w1src = (const char*)(W1 + n0);
    const char* w3src = (const char*)(W3 + n0);
#define SA_OFF(b, h) (((b) * 2 + (h)) * HTB)
#define SB_OFF(b, h) ((4 + (b) * 2 + (h)) * HTB)
#define STAGE_A(b, h, kt) do { const char* _s = xsrc + (size_t)(h) * 128 * K * 2 + (kt) * 128; \
        glds16(_s, xoffs[0], lds_base + SA_OFF(b, h) + wave * 1024); glds16(_s, xoffs[1], lds_base + SA_OFF(b, h) + (wave + 8) * 1024); } while (0)
#define STAGE_B(b, h, kt) do { const char* _s = ((h) ? w3src : w1src) + (size_t)(kt) * 64 * LDW * 2; \
        glds16(_s, woffs[0], lds_base + SB_OFF(b, h) + wave * 1024); glds16(_s, woffs[1], lds_base + SB_OFF(b, h) + (wave + 8) * 1024); } while (0)
    const int fr = lane & 15, g = lane >> 4, q = (lane & 15) >> 2, p = lane & 3;
    const int xrow = wr * 64 + fr;
    const int xo0 = (xrow * 128 + ((g ^ (xrow & 7)) << 4)) ^ (wr * 64);
    const int wo0 = g * 2048 + (q + 4 * (g & 1)) * 32 + p * 8 + wc * 512;
    unsigned xb_[4][2], wb_[2][2];
#pragma unroll
    for (int m = 0; m < 4; ++m)
#pragma unroll
        for (int k = 0; k < 2; ++k) { xb_[m][k] = lds_base + ((xo0 ^ (k * 64)) + m * 2048); asm volatile("" : "+v"(xb_[m][k])); }
#pragma unroll
    for (int t = 0; t < 2; ++t)
#pragma unroll
        for (int k = 0; k < 2; ++k) { wb_[t][k] = lds_base + 4 * HTB + ((wo0 ^ (t * 128)) + (k ^ wr) * 8192); asm volatile("" : "+v"(wb_[t][k])); }
#define LDSP(a) ((LAS const char*)(uintptr_t)(a))
#define LDA(dst, b, h) do { _Pragma("unroll") for (int m = 0; m < 4; ++m) _Pragma("unroll") for (int k = 0; k < 2; ++k) \
        dst[m][k] = *(LAS const bf16x8*)(LDSP(xb_[m][k]) + SA_OFF(b, h)); } while (0)
#define LDB(dst, b, h) do { _Pragma("unroll") for (int n = 0; n < 2; ++n) _Pragma("unroll") for (int k = 0; k < 2; ++k) { \
        s16x4 lo_ = tr_read(LDSP(wb_[0][k]) + SA_OFF(b, h) + n * 256); \
        s16x4 hi_ = tr_read(LDSP(wb_[1][k]) + SA_OFF(b, h) + n * 256); \
        dst[n][k] = (bf16x8){lo_[0], lo_[1], lo_[2], lo_[3], hi_[0], hi_[1], hi_[2], hi_[3]}; } } while (0)
#define MMA(ai, bj, At, Bt, CS) do { __builtin_amdgcn_s_setprio(1); \
        _Pragma("unroll") for (int m = 0; m < 4; ++m) _Pragma("unroll") for (int n = 0; n < 2; ++n) _Pragma("unroll") for (int k = 0; k < 2; ++k) \
            acc[ai][bj][m][n] = __builtin_amdgcn_mfma_f32_16x16x32_bf16(Bt[n][k], At[m][k], acc[ai][bj][m][n], 0, 0, 0); \
        if ((CS) & 2) { cs[bj][0] = __builtin_amdgcn_mfma_f32_16x16x32_bf16(Bt[0][0], ones, cs[bj][0], 0, 0, 0); \
                        cs[bj][1] = __builtin_amdgcn_mfma_f32_16x16x32_bf16(Bt[1][0], ones, cs[bj][1], 0, 0, 0); } \
        __builtin_amdgcn_s_setprio(0); __builtin_amdgcn_sched_barrier(0); } while (0)
#define WAIT_V(n) asm volatile("s_waitcnt vmcnt(" #n ")" ::: "memory")
#define WAIT_L(n) asm volatile("s_waitcnt lgkmcnt(" #n ")" ::: "memory")
#define BAR __builtin_amdgcn_s_barrier()
#define SCHED __builtin_amdgcn_sched_barrier(0)
    f32x4 acc[2][2][4][2] = {};
    bf16x8 At[4][2], B0[2][2], B1[2][2];
    f32x4 cs[2][2] = {};
    const bf16x8 ones = {0x3f80, 0x3f80, 0x3f80, 0x3f80, 0x3f80, 0x3f80, 0x3f80, 0x3f80};
    constexpr int nt = K / 64;
    {
        const char* ssrc = (const char*)(stat_in + (size_t)m0 * 32);
#pragma unroll
        for (int j = 0; j < 4; ++j) glds16(ssrc, (unsigned)((wave * 4 + j) * 1024 + lane * 16), lds_base + 8 * HTB + (wave * 4 + j) * 1024);
    }
    STAGE_B(0, 0, 0); STAGE_A(0, 0, 0); STAGE_B(0, 1, 0); STAGE_A(0, 1, 0);
    if (wr == 1) BAR;
    WAIT_V(4); BAR;
    STAGE_B(1, 0, 1); STAGE_A(1, 0, 1); STAGE_B(1, 1, 1);
    WAIT_V(6); BAR;
    for (int t = 0; t < nt; t += 2) {
        const bool more = t + 2 < nt;
        LDB(B0, 0, 0); SCHED; LDA(At, 0, 0); STAGE_A(1, 1, t + 1);
        WAIT_L(8); BAR; WAIT_L(0); MMA(0, 0, At, B0, 2); BAR; SCHED;
        LDB(B1, 0, 1); if (more) STAGE_B(0, 0, t + 2);
        BAR; WAIT_L(0); MMA(0, 1, At, B1, 1); BAR;
        LDA(At, 0, 1); if (more) STAGE_A(0, 0, t + 2);
        BAR; WAIT_L(0); MMA(1, 0, At, B0, 1); BAR; SCHED;
        if (more) { STAGE_B(0, 1, t + 2); WAIT_V(6); } else { WAIT_V(0); }
        BAR; MMA(1, 1, At, B1, 2); BAR;
        LDB(B0, 1, 0); SCHED; LDA(At, 1, 0); if (more) STAGE_A(0, 1, t + 2);
        WAIT_L(8); BAR; WAIT_L(0); MMA(0, 0, At, B0, 2); BAR; SCHED;
        LDB(B1, 1, 1); if (more) STAGE_B(1, 0, t + 3);
        BAR; WAIT_L(0); MMA(0, 1, At, B1, 1); BAR;
        LDA(At, 1, 1); if (more) STAGE_A(1, 0, t + 3);
        BAR; WAIT_L(0); MMA(1, 0, At, B0, 1); BAR; SCHED;
        if (more) { STAGE_B(1, 1, t + 3); WAIT_V(6); }
        BAR; MMA(1, 1, At, B1, 2); BAR;
    }
    if (wr == 0) BAR;
    LAS float* rowstat = (LAS float*)lds;
    LAS float* colsum = (LAS float*)(lds + 2048);
    if (fr == 0) {
#pragma unroll
        for (int b = 0; b < 2; ++b)
#pragma unroll
            for (int n = 0; n < 2; ++n) *(LAS f32x4*)(colsum + wr * 256 + b * 128 + wc * 32 + n * 16 + 4 * g) = cs[b][n];
    }
    if (tid < 256) {
        LAS const f32x4* sp = (LAS const f32x4*)(lds + 8 * HTB + tid * 128);
        f32x4 t = sp[tid & 7];
#pragma unroll
        for (int e = 1; e < 8; ++e) t += sp[e ^ (tid & 7)];
        const float mean = (t[0] + t[2]) * (1.0f / D_MODEL);
        const float rstd = 1.0f / sqrtf(fmaxf((t[1] + t[3]) * (1.0f / D_MODEL) - mean * mean, 0.f) + 1e-5f);
        rowstat[2 * tid] = mean; rowstat[2 * tid + 1] = rstd;
    }
    __syncthreads();
    f32x4 csv[2][2];
#pragma unroll
    for (int b = 0; b < 2; ++b)
#pragma unroll
        for (int n = 0; n < 2; ++n) {
            const int c = b * 128 + wc * 32 + n * 16 + 4 * g;
            csv[b][n] = *(LAS const f32x4*)(colsum + c) + *(LAS const f32x4*)(colsum + 256 + c);
        }
#pragma unroll
    for (int ai = 0; ai < 2; ++ai)
#pragma unroll
        for (int m = 0; m < 4; ++m) {
            const int rl = ai * 128 + wr * 64 + m * 16 + fr;
            const float mean = rowstat[2 * rl], rstd = rowstat[2 * rl + 1];
#pragma unroll
            for (int n = 0; n < 2; ++n) {
                const f32x4 gv = (acc[ai][0][m][n] - csv[0][n] * mean) * rstd;
                const f32x4 uv = (acc[ai][1][m][n] - csv[1][n] * mean) * rstd;
                float o[4];
#pragma unroll
                for (int r = 0; r < 4; ++r) o[r] = gv[r] * __builtin_amdgcn_rcpf(1.0f + __expf(-gv[r])) * uv[r];
                u32x2 w; w[0] = pk_bf16(o[0], o[1]); w[1] = pk_bf16(o[2], o[3]);
                *(u32x2*)(C + (size_t)(m0 + rl) * LDW + n0 + wc * 32 + n * 16 + 4 * g) = w;
            }
        }
#undef SA_OFF
#undef SB_OFF
#undef STAGE_A
#undef STAGE_B
#undef LDA
#undef LDB
#undef MMA
}

__global__ __launch_bounds__(512) void attn_kernel(const bf16_t* __restrict__ qkv, bf16_t* __restrict__ O) {
    extern __shared__ __attribute__((aligned(16))) char smem[];
    LAS char* lds = (LAS char*)smem;
    LAS char* ldsK = lds;
    LAS char* ldsV = lds + 32768;
    const int tid = threadIdx.x, lane = tid & 63;
    const int w = __builtin_amdgcn_readfirstlane(tid >> 6);
    const int h = blockIdx.x & 31, qb = blockIdx.x >> 5;
    const int q0 = qb * 256, kbase = q0 - 256;
    constexpr int LD = 3 * D_MODEL;

    {
        u32x4 kv[4], vv[4];
#pragma unroll
        for (int i = 0; i < 4; ++i) {
            const int idx = tid + 512 * i, key = idx >> 2, ch = idx & 3;
            const int gk = kbase + key;
            if (gk >= 0) {
                const bf16_t* src = qkv + (size_t)gk * LD + h * 32 + ch * 8;
                kv[i] = *(const u32x4*)(src + D_MODEL);
                vv[i] = *(const u32x4*)(src + 2 * D_MODEL);
            }
        }
#pragma unroll
        for (int i = 0; i < 4; ++i) {
            const int idx = tid + 512 * i, key = idx >> 2, ch = idx & 3;
            const int gk = kbase + key;
            if (gk >= 0) {
                *(LAS u32x4*)(ldsK + key * 64 + ((ch ^ ((key >> 2) & 3)) << 4)) = kv[i];
                *(LAS u32x4*)(ldsV + key * 64 + (ch << 4)) = vv[i];
            }
        }
    }
    const int r32 = lane & 31, hi = lane >> 5;
    const int qw = q0 + 32 * w;
    bf16x8 qf[2];
#pragma unroll
    for (int s = 0; s < 2; ++s)
        qf[s] = *(const bf16x8*)(qkv + (size_t)(qw + r32) * LD + h * 32 + 16 * s + 8 * hi);
    __syncthreads();

    f32x16 sc[9];
    const float NEG = -INFINITY;
#pragma unroll
    for (int j = 0; j < 9; ++j) {
        const int kb = qw - 256 + 32 * j;
        if (kb >= 0) {
            const int key = 32 * (w + j) + r32;
            f32x16 a = {};
#pragma unroll
            for (int s = 0; s < 2; ++s) {
                const bf16x8 kf = *(LAS const bf16x8*)(ldsK + key * 64 + (((2 * s + hi) ^ ((key >> 2) & 3)) << 4));
                a = __builtin_amdgcn_mfma_f32_32x32x16_bf16(kf, qf[s], a, 0, 0, 0);
            }
            sc[j] = a;
        } else {
#pragma unroll
            for (int r = 0; r < 16; ++r) sc[j][r] = NEG;
        }
    }
#pragma unroll
    for (int r = 0; r < 16; ++r) {
        const int i = (r & 3) + 8 * (r >> 2) + 4 * hi;
        if (!(i > r32)) sc[0][r] = NEG;
        if (!(i <= r32)) sc[8][r] = NEG;
    }
    float mx = NEG;
#pragma unroll
    for (int j = 0; j < 9; ++j)
#pragma unroll
        for (int r = 0; r < 16; ++r) mx = fmaxf(mx, sc[j][r]);
    mx = fmaxf(mx, __shfl_xor(mx, 32));
    const float c = 0.17677669529663687f * 1.4426950408889634f;
    const float nm = -mx * c;
    float l = 0.f;
#pragma unroll
    for (int j = 0; j < 9; ++j)
#pragma unroll
        for (int r = 0; r < 16; ++r) {
            const float e = __builtin_amdgcn_exp2f(__builtin_fmaf(sc[j][r], c, nm));
            sc[j][r] = e; l += e;
        }
    l += __shfl_xor(l, 32);

    f32x16 o = {};
    const int gq = (lane & 15) >> 2, gp = lane & 3, gd = (lane >> 4) & 1;
#pragma unroll
    for (int j = 0; j < 9; ++j) {
        const int kb = qw - 256 + 32 * j;
        if (kb >= 0) {
#pragma unroll
            for (int s = 0; s < 2; ++s) {
                u32x4 pw;
#pragma unroll
                for (int e = 0; e < 4; ++e) pw[e] = pk_bf16(sc[j][8 * s + 2 * e], sc[j][8 * s + 2 * e + 1]);
                const int key = 32 * (w + j) + 16 * s + 4 * hi + gq;
                LAS const char* vp = ldsV + key * 64 + (16 * gd + 4 * gp) * 2;
                s16x4 lo = tr_read(vp), hi8 = tr_read(vp + 8 * 64);
                const bf16x8 vf = (bf16x8){lo[0], lo[1], lo[2], lo[3], hi8[0], hi8[1], hi8[2], hi8[3]};
                o = __builtin_amdgcn_mfma_f32_32x32x16_bf16(vf, __builtin_bit_cast(bf16x8, pw), o, 0, 0, 0);
            }
        }
    }
    const float rl = 1.0f / l;
    bf16_t* dst = O + (size_t)(qw + r32) * D_MODEL + h * 32 + 4 * hi;
#pragma unroll
    for (int t = 0; t < 4; ++t) {
        u32x2 wv; wv[0] = pk_bf16(o[4 * t] * rl, o[4 * t + 1] * rl); wv[1] = pk_bf16(o[4 * t + 2] * rl, o[4 * t + 3] * rl);
        *(u32x2*)(dst + 8 * t) = wv;
    }
}

template <typename KF> static void set_lds(KF kf, int bytes) {
    (void)hipFuncSetAttribute((const void*)kf, hipFuncAttributeMaxDynamicSharedMemorySize, bytes);
}

extern "C" void kernel_launch(void* const* d_in, const int* in_sizes, int n_in,
                              void* d_out, int out_size, void* d_ws, size_t ws_size,
                              hipStream_t stream) {
    const bf16_t* x = (const bf16_t*)d_in[0];
    const bf16_t* wqkv = (const bf16_t*)d_in[1];
    const bf16_t* wo = (const bf16_t*)d_in[2];
    const bf16_t* w1 = (const bf16_t*)d_in[4];
    const bf16_t* w3 = (const bf16_t*)d_in[5];
    const bf16_t* w2 = (const bf16_t*)d_in[6];
    bf16_t* out = (bf16_t*)d_out;
    char* ws = (char*)d_ws;
    bf16_t* qkv = (bf16_t*)(ws + (4u << 20));
    bf16_t* att = (bf16_t*)(ws + (16u << 20));
    bf16_t* hbuf = (bf16_t*)(ws + (20u << 20));
    bf16_t* act = (bf16_t*)(ws + (28u << 20));
    float* hstat = (float*)(ws + (24u << 20));

    {
        auto kf = gemm_kernel<128, 192, 2, 4, 4, EPI_STORE, 1, 2, 4>;
        constexpr int lds = 4 * (128 + 192) * 128;
        set_lds(kf, lds);
        kf<<<256, 512, lds, stream>>>(x, wqkv, nullptr, qkv, nullptr, S_LEN, 3072, 1024, 3072, 3072);
    }
    {
        set_lds(attn_kernel, 65536);
        attn_kernel<<<256, 512, 65536, stream>>>(qkv, att);
    }
    {
        auto kf = gemm_kernel<128, 64, 4, 2, 6, EPI_RES_STAT, 0, 4, 2>;
        constexpr int lds = 6 * (128 + 64) * 128;
        set_lds(kf, lds);
        kf<<<256, 512, lds, stream>>>(att, wo, x, hbuf, hstat, S_LEN, 1024, 1024, 1024, 1024);
    }
    {
        set_lds(glu_gemm_kernel, 163840);
        glu_gemm_kernel<<<224, 512, 163840, stream>>>(hbuf, w1, w3, hstat, act);
    }
    {
        auto kf = gemm_kernel<128, 64, 4, 2, 6, EPI_RES, 0, 4, 2>;
        constexpr int lds = 6 * (128 + 64) * 128;
        set_lds(kf, lds);
        kf<<<256, 512, lds, stream>>>(act, w2, hbuf, out, nullptr, S_LEN, 1024, F_DIM, 1024, 1024);
    }
}
```

```cpp
#include <hip/hip_runtime.h>
#include <hip/hip_bf16.h>
#include <stdint.h>

typedef unsigned short bf16_t;
typedef short bf16x8 __attribute__((ext_vector_type(8)));
typedef short s16x4 __attribute__((ext_vector_type(4)));
typedef float f32x4 __attribute__((ext_vector_type(4)));
typedef float f32x16 __attribute__((ext_vector_type(16)));
typedef unsigned u32x4 __attribute__((ext_vector_type(4)));
typedef unsigned u32x2 __attribute__((ext_vector_type(2)));
#define LAS __attribute__((address_space(3)))

constexpr int S_LEN = 2048, D_MODEL = 1024, F_DIM = 3584, N_HEAD = 32, D_HEAD = 32, WIN = 256;

__device__ __forceinline__ float bf2f(unsigned v16) { return __uint_as_float(v16 << 16); }
__device__ __forceinline__ unsigned pk_bf16(float lo, float hi) {
    unsigned r; asm("v_cvt_pk_bf16_f32 %0, %1, %2" : "=v"(r) : "v"(lo), "v"(hi)); return r;
}
__device__ __forceinline__ s16x4 tr_read(LAS const char* p) {
    return __builtin_amdgcn_ds_read_tr16_b64_v4i16((LAS s16x4*)p);
}
__device__ __forceinline__ void glds16(const void* sbase, unsigned voff, unsigned lds_addr) {
    unsigned keep;
    asm volatile("s_mov_b32 %0, m0\n\ts_mov_b32 m0, %3\n\ts_nop 0\n\tglobal_load_lds_dwordx4 %2, %1\n\ts_mov_b32 m0, %0"
                 : "=&s"(keep) : "s"(sbase), "v"(voff), "s"(lds_addr) : "memory");
}
typedef __bf16 bf16x2_t __attribute__((ext_vector_type(2)));
__device__ __forceinline__ float dot2(unsigned a, unsigned b, float acc) {
    return __builtin_amdgcn_fdot2_f32_bf16(__builtin_bit_cast(bf16x2_t, a), __builtin_bit_cast(bf16x2_t, b), acc, false);
}
__device__ __forceinline__ void frag_sum(const bf16x8& f, float& s1) {
    const u32x4 u = __builtin_bit_cast(u32x4, f);
#pragma unroll
    for (int e = 0; e < 4; ++e) s1 = dot2(u[e], 0x3f803f80u, s1);
}
__device__ __forceinline__ void frag_sum_sq(const bf16x8& f, float& s1, float& s2) {
    const u32x4 u = __builtin_bit_cast(u32x4, f);
#pragma unroll
    for (int e = 0; e < 4; ++e) { s1 = dot2(u[e], 0x3f803f80u, s1); s2 = dot2(u[e], u[e], s2); }
}
__device__ __forceinline__ float red_g(float v) { v += __shfl_xor(v, 16); v += __shfl_xor(v, 32); return v; }
template <int N> __device__ __forceinline__ void wait_vm() { asm volatile("s_waitcnt vmcnt(%0)" ::"n"(N) : "memory"); }

enum { EPI_STORE = 0, EPI_RES = 1, EPI_RES_STAT = 2 };

template <int BM, int BN, int WM, int WN, int NST, int EPI, int LNF, int RM, int RN>
__global__ __launch_bounds__(WM * WN * 64) void gemm_kernel(
    const bf16_t* __restrict__ X, const bf16_t* __restrict__ W0,
    const bf16_t* __restrict__ R, bf16_t* __restrict__ C, float* __restrict__ stat_out,
    int M, int N, int K, int ldw, int ldc) {
    constexpr int NWAVE = WM * WN;
    constexpr int MF = BM / 16 / WM, NF = BN / 16 / WN, NB = BN / 16;
    constexpr int A_BYTES = BM * 128, B_BYTES = BN * 128, ST_BYTES = A_BYTES + B_BYTES;
    constexpr int LPW = (BM + BN) / 8 / NWAVE;
    constexpr int LPA = BM / 8 / NWAVE;
    static_assert((BM + BN) % (8 * NWAVE) == 0 && (BM % (8 * NWAVE)) == 0, "piece split");
    static_assert(NB % 4 == 0, "NB%4");
    static_assert(!LNF || (MF == WN && WM == 2), "LN fold layout");
    extern __shared__ __attribute__((aligned(16))) char smem[];
    LAS char* lds = (LAS char*)smem;

    const int tid = threadIdx.x, lane = tid & 63;
    const int wave = __builtin_amdgcn_readfirstlane(tid >> 6);
    const int wm = wave / WN, wn = wave % WN;

    const int tiles_m = M / BM, tiles_n = N / BN;
    int tm, tn;
    {
        const int xcd = blockIdx.x & 7, idx = blockIdx.x >> 3;
        const int rm = xcd / RN, rn = xcd % RN;
        const int sm = tiles_m / RM, sn = tiles_n / RN;
        tm = rm * sm + idx % sm; tn = rn * sn + idx / sm;
    }
    const int m0 = tm * BM, n0 = tn * BN;

    unsigned voff[LPW];
    const char* sbase[LPW];
#pragma unroll
    for (int j = 0; j < LPW; ++j) {
        const int ii = wave + NWAVE * j;
        if (j < LPA) {
            const int row = ii * 8 + (lane >> 3), ch = (lane & 7) ^ (row & 7);
            voff[j] = (unsigned)(row * K * 2 + ch * 16);
            sbase[j] = (const char*)(X + (size_t)m0 * K);
        } else {
            const int pi = ii - BM / 8;
            const int kb = pi / (NB / 4), nb = 4 * (pi % (NB / 4)) + (lane >> 4);
            const int r = ((lane & 15) >> 1) ^ (4 * (kb & 1)), half = lane & 1;
            voff[j] = (unsigned)(((8 * kb + r) * ldw + 16 * nb + 8 * half) * 2);
            sbase[j] = (const char*)(W0 + n0);
        }
    }
    const unsigned lds_base = (unsigned)(uintptr_t)lds;
    const int NT = K / 64;
    auto issue = [&](int buf) {
#pragma unroll
        for (int j = 0; j < LPW; ++j) {
            const int ii = wave + NWAVE * j;
            glds16(sbase[j], voff[j], lds_base + buf * ST_BYTES + ii * 1024);
            sbase[j] += (j < LPA) ? 128 : (long)128 * ldw;
        }
    };

    const int fr = lane & 15, g = lane >> 4, q = (lane & 15) >> 2, p = lane & 3;
    const int rotm = LNF ? wn : 0, rotk = LNF ? wm : 0;
    const int xrow = wm * (MF * 16) + fr;
    const int xoff0 = xrow * 128 + ((g ^ (xrow & 7)) << 4);
    const int woff0 = A_BYTES + g * NB * 256 + (q + 4 * (g & 1)) * 32 + p * 8 + wn * NF * 256;

    f32x4 acc[MF][NF];
#pragma unroll
    for (int i = 0; i < MF; ++i)
#pragma unroll
        for (int j = 0; j < NF; ++j) acc[i][j] = (f32x4){0.f, 0.f, 0.f, 0.f};
    f32x4 s1 = {0.f, 0.f, 0.f, 0.f}, s2 = {0.f, 0.f, 0.f, 0.f}, cs[NF];
#pragma unroll
    for (int j = 0; j < NF; ++j) cs[j] = (f32x4){0.f, 0.f, 0.f, 0.f};
    const bf16x8 ones = {0x3f80, 0x3f80, 0x3f80, 0x3f80, 0x3f80, 0x3f80, 0x3f80, 0x3f80};

#pragma unroll
    for (int s = 0; s < NST - 1; ++s) issue(s);

    auto read_frags = [&](LAS const char* base, int slot, bf16x8 (&xf)[MF], bf16x8 (&wf)[NF]) {
        const int ks = slot ^ rotk;
#pragma unroll
        for (int j = 0; j < NF; ++j) {
            const int o = ks * (4 * NB * 256) + j * 256;
            s16x4 lo = tr_read(base + woff0 + o);
            s16x4 hi = tr_read(base + (woff0 ^ 128) + o);
            wf[j] = (bf16x8){lo[0], lo[1], lo[2], lo[3], hi[0], hi[1], hi[2], hi[3]};
        }
#pragma unroll
        for (int i = 0; i < MF; ++i)
            xf[i] = *(LAS const bf16x8*)(base + ((xoff0 ^ (ks * 64)) + (i ^ rotm) * 2048));
    };
    auto do_mfma = [&](bf16x8 (&xf)[MF], bf16x8 (&wf)[NF], bool colsum) {
#pragma unroll
        for (int i = 0; i < MF; ++i)
#pragma unroll
            for (int j = 0; j < NF; ++j)
                acc[i][j] = __builtin_amdgcn_mfma_f32_16x16x32_bf16(wf[j], xf[i], acc[i][j], 0, 0, 0);
        if (LNF) {
            s1 = __builtin_amdgcn_mfma_f32_16x16x32_bf16(ones, xf[0], s1, 0, 0, 0);
            s2 = __builtin_amdgcn_mfma_f32_16x16x32_bf16(xf[0], xf[0], s2, 0, 0, 0);
            if (colsum) {
#pragma unroll
                for (int j = 0; j < NF; ++j) cs[j] = __builtin_amdgcn_mfma_f32_16x16x32_bf16(wf[j], ones, cs[j], 0, 0, 0);
            }
        }
    };
    auto interleave = [&]() {
        constexpr int NM = MF * NF, NR = MF + 2 * NF, RPM = (NR + NM - 1) / NM;
#pragma unroll
        for (int i = 0; i < NM; ++i) {
            __builtin_amdgcn_sched_group_barrier(0x8, 1, 0);
            __builtin_amdgcn_sched_group_barrier(0x100, RPM, 0);
        }
    };
    bf16x8 xa[MF], wa[NF], xb[MF], wb[NF];
    wait_vm<(NST - 2) * LPW>();
    __builtin_amdgcn_s_barrier();
    asm volatile("" ::: "memory");
    read_frags(lds, 0, xa, wa);

    int buf = 0, pbuf = NST - 1;
    for (int kt = 0; kt < NT; ++kt) {
        const int rem = NT - 2 - kt;
        if (rem >= NST - 3) wait_vm<(NST - 3) * LPW>();
        else if (NST > 4 && rem == 1) wait_vm<LPW>();
        else if (NST > 5 && rem == 2) wait_vm<2 * LPW>();
        else wait_vm<0>();
        __builtin_amdgcn_s_barrier();
        asm volatile("" ::: "memory");
        if (kt + NST - 1 < NT) issue(pbuf);
        const int nbuf = (buf + 1 == NST) ? 0 : buf + 1;
        __builtin_amdgcn_sched_barrier(0);
        read_frags(lds + buf * ST_BYTES, 1, xb, wb);
        do_mfma(xa, wa, true);
        interleave();
        __builtin_amdgcn_sched_barrier(0);
        read_frags(lds + nbuf * ST_BYTES, 0, xa, wa);
        do_mfma(xb, wb, false);
        interleave();
        __builtin_amdgcn_sched_barrier(0);
        pbuf = buf; buf = nbuf;
    }

    LAS float* rowstat = (LAS float*)lds;
    LAS float* colsum = (LAS float*)(lds + BM * 8);
    if (LNF) {
        __syncthreads();
        if (g == (fr >> 2)) {
            const int r = wm * (MF * 16) + wn * 16 + fr;
            const int d = fr & 3;
            rowstat[2 * r] = s1[0];
            rowstat[2 * r + 1] = d == 0 ? s2[0] : d == 1 ? s2[1] : d == 2 ? s2[2] : s2[3];
        }
        if (fr == 0) {
#pragma unroll
            for (int j = 0; j < NF; ++j) *(LAS f32x4*)(colsum + wm * BN + (wn * NF + j) * 16 + 4 * g) = cs[j];
        }
        __syncthreads();
    }
    if (EPI == EPI_RES_STAT) __syncthreads();
    f32x4 csv[NF];
    if (LNF) {
#pragma unroll
        for (int j = 0; j < NF; ++j) {
            const int c = (wn * NF + j) * 16 + 4 * g;
            csv[j] = *(LAS const f32x4*)(colsum + c) + *(LAS const f32x4*)(colsum + BN + c);
        }
    }
#pragma unroll
    for (int i = 0; i < MF; ++i) {
        const int rl = wm * (MF * 16) + (i ^ rotm) * 16 + fr;
        const int m = m0 + rl;
        float mean = 0.f, rstd = 1.f, ps1 = 0.f, ps2 = 0.f;
        if (LNF) {
            const float a = rowstat[2 * rl], b = rowstat[2 * rl + 1];
            mean = a * (1.0f / D_MODEL);
            const float var = fmaxf(b * (1.0f / D_MODEL) - mean * mean, 0.f);
            rstd = 1.0f / sqrtf(var + 1e-5f);
        }
#pragma unroll
        for (int j = 0; j < NF; ++j) {
            const int n = n0 + (wn * NF + j) * 16 + 4 * g;
            f32x4 v = acc[i][j];
            if (LNF) v = (v - csv[j] * mean) * rstd;
            if (EPI != EPI_STORE) {
                const u32x2 rv = *(const u32x2*)(R + (size_t)m * ldc + n);
                v[0] += bf2f(rv[0] & 0xffffu); v[1] += bf2f(rv[0] >> 16);
                v[2] += bf2f(rv[1] & 0xffffu); v[3] += bf2f(rv[1] >> 16);
            }
            u32x2 w; w[0] = pk_bf16(v[0], v[1]); w[1] = pk_bf16(v[2], v[3]);
            *(u32x2*)(C + (size_t)m * ldc + n) = w;
            if (EPI == EPI_RES_STAT) {
                const float a0 = bf2f(w[0] & 0xffffu), a1 = bf2f(w[0] >> 16), a2 = bf2f(w[1] & 0xffffu), a3 = bf2f(w[1] >> 16);
                ps1 += (a0 + a1) + (a2 + a3);
                ps2 += (a0 * a0 + a1 * a1) + (a2 * a2 + a3 * a3);
            }
        }
        if (EPI == EPI_RES_STAT) {
            ps1 = red_g(ps1); ps2 = red_g(ps2);
            if (g == 0) { rowstat[(rl * WN + wn) * 2] = ps1; rowstat[(rl * WN + wn) * 2 + 1] = ps2; }
        }
    }
    if (EPI == EPI_RES_STAT) {
        __syncthreads();
        if (tid < BM) {
            float a = 0.f, b = 0.f;
#pragma unroll
            for (int w = 0; w < WN; ++w) { a += rowstat[(tid * WN + w) * 2]; b += rowstat[(tid * WN + w) * 2 + 1]; }
            float2 o; o.x = a; o.y = b;
            *(float2*)(stat_out + ((size_t)(m0 + tid) * tiles_n + tn) * 2) = o;
        }
    }
}

__global__ __launch_bounds__(512, 2) void glu_gemm_kernel(
    const bf16_t* __restrict__ X, const bf16_t* __restrict__ W1, const bf16_t* __restrict__ W3,
    const float* __restrict__ stat_in, bf16_t* __restrict__ C) {
    constexpr int K = D_MODEL, LDW = F_DIM, HTB = 16384;
    extern __shared__ __attribute__((aligned(16))) char smem[];
    LAS char* lds = (LAS char*)smem;
    const unsigned lds_base = (unsigned)(uintptr_t)lds;
    const int tid = threadIdx.x, lane = tid & 63;
    const int wave = __builtin_amdgcn_readfirstlane(tid >> 6);
    const int wr = wave >> 2, wc = wave & 3;
    int tm, tn;
    { const int xcd = blockIdx.x & 7, idx = blockIdx.x >> 3; tm = (xcd >> 2) * 4 + (idx & 3); tn = (xcd & 3) * 7 + (idx >> 2); }
    const int m0 = tm * 256, n0 = tn * 128;

    unsigned xoffs[2], woffs[2];
#pragma unroll
    for (int j = 0; j < 2; ++j) {
        const int pc = wave + 8 * j;
        const int row = pc * 8 + (lane >> 3), ch = (lane & 7) ^ (row & 7);
        xoffs[j] = (unsigned)(row * K * 2 + ch * 16);
        const int kb = pc >> 1, nb = 4 * (pc & 1) + (lane >> 4);
        const int r = ((lane & 15) >> 1) ^ (4 * (kb & 1)), half = lane & 1;
        woffs[j] = (unsigned)(((8 * kb + r) * LDW + 16 * nb + 8 * half) * 2);
    }
    const char* xsrc = (const char*)(X + (size_t)m0 * K);
    const char* w1src = (const char*)(W1 + n0);
    const char* w3src = (const char*)(W3 + n0);
#define SA_OFF(b, h) (((b) * 2 + (h)) * HTB)
#define SB_OFF(b, h) ((4 + (b) * 2 + (h)) * HTB)
#define STAGE_A(b, h, kt) do { const char* _s = xsrc + (size_t)(h) * 128 * K * 2 + (kt) * 128; \
        glds16(_s, xoffs[0], lds_base + SA_OFF(b, h) + wave * 1024); glds16(_s, xoffs[1], lds_base + SA_OFF(b, h) + (wave + 8) * 1024); } while (0)
#define STAGE_B(b, h, kt) do { const char* _s = ((h) ? w3src : w1src) + (size_t)(kt) * 64 * LDW * 2; \
        glds16(_s, woffs[0], lds_base + SB_OFF(b, h) + wave * 1024); glds16(_s, woffs[1], lds_base + SB_OFF(b, h) + (wave + 8) * 1024); } while (0)
    const int fr = lane & 15, g = lane >> 4, q = (lane & 15) >> 2, p = lane & 3;
    const int xrow = wr * 64 + fr;
    const int xo0 = (xrow * 128 + ((g ^ (xrow & 7)) << 4)) ^ (wr * 64);
    const int wo0 = g * 2048 + (q + 4 * (g & 1)) * 32 + p * 8 + wc * 512;
    unsigned xb_[4][2], wb_[2][2];
#pragma unroll
    for (int m = 0; m < 4; ++m)
#pragma unroll
        for (int k = 0; k < 2; ++k) { xb_[m][k] = lds_base + ((xo0 ^ (k * 64)) + m * 2048); asm volatile("" : "+v"(xb_[m][k])); }
#pragma unroll
    for (int t = 0; t < 2; ++t)
#pragma unroll
        for (int k = 0; k < 2; ++k) { wb_[t][k] = lds_base + 4 * HTB + ((wo0 ^ (t * 128)) + (k ^ wr) * 8192); asm volatile("" : "+v"(wb_[t][k])); }
#define LDSP(a) ((LAS const char*)(uintptr_t)(a))
#define LDA(dst, b, h) do { _Pragma("unroll") for (int m = 0; m < 4; ++m) _Pragma("unroll") for (int k = 0; k < 2; ++k) \
        dst[m][k] = *(LAS const bf16x8*)(LDSP(xb_[m][k]) + SA_OFF(b, h)); } while (0)
#define LDB(dst, b, h) do { _Pragma("unroll") for (int n = 0; n < 2; ++n) _Pragma("unroll") for (int k = 0; k < 2; ++k) { \
        s16x4 lo_ = tr_read(LDSP(wb_[0][k]) + SA_OFF(b, h) + n * 256); \
        s16x4 hi_ = tr_read(LDSP(wb_[1][k]) + SA_OFF(b, h) + n * 256); \
        dst[n][k] = (bf16x8){lo_[0], lo_[1], lo_[2], lo_[3], hi_[0], hi_[1], hi_[2], hi_[3]}; } } while (0)
#define MMA(ai, bj, At, Bt, CS) do { __builtin_amdgcn_s_setprio(1); \
        _Pragma("unroll") for (int m = 0; m < 4; ++m) _Pragma("unroll") for (int n = 0; n < 2; ++n) _Pragma("unroll") for (int k = 0; k < 2; ++k) \
            acc[ai][bj][m][n] = __builtin_amdgcn_mfma_f32_16x16x32_bf16(Bt[n][k], At[m][k], acc[ai][bj][m][n], 0, 0, 0); \
        if ((CS) & 2) { cs[bj][0] = __builtin_amdgcn_mfma_f32_16x16x32_bf16(Bt[0][0], ones, cs[bj][0], 0, 0, 0); \
                        cs[bj][1] = __builtin_amdgcn_mfma_f32_16x16x32_bf16(Bt[1][0], ones, cs[bj][1], 0, 0, 0); } \
        __builtin_amdgcn_s_setprio(0); } while (0)
#define WAIT_V(n) asm volatile("s_waitcnt vmcnt(" #n ")" ::: "memory")
#define WAIT_L(n) asm volatile("s_waitcnt lgkmcnt(" #n ")" ::: "memory")
#define BAR __builtin_amdgcn_s_barrier()
#define SCHED __builtin_amdgcn_sched_barrier(0)
    f32x4 acc[2][2][4][2] = {};
    bf16x8 At[4][2], B0[2][2], B1[2][2];
    f32x4 cs[2][2] = {};
    const bf16x8 ones = {0x3f80, 0x3f80, 0x3f80, 0x3f80, 0x3f80, 0x3f80, 0x3f80, 0x3f80};
    constexpr int nt = K / 64;
    {
        const char* ssrc = (const char*)(stat_in + (size_t)m0 * 32);
#pragma unroll
        for (int j = 0; j < 4; ++j) glds16(ssrc, (unsigned)((wave * 4 + j) * 1024 + lane * 16), lds_base + 8 * HTB + (wave * 4 + j) * 1024);
    }
    STAGE_B(0, 0, 0); STAGE_A(0, 0, 0); STAGE_B(0, 1, 0); STAGE_A(0, 1, 0);
    if (wr == 1) BAR;
    WAIT_V(4); BAR;
    STAGE_B(1, 0, 1); STAGE_A(1, 0, 1); STAGE_B(1, 1, 1);
    WAIT_V(6); BAR;
    for (int t = 0; t < nt; t += 2) {
        const bool more = t + 2 < nt;
        LDB(B0, 0, 0); SCHED; LDA(At, 0, 0); STAGE_A(1, 1, t + 1);
        WAIT_L(8); BAR; WAIT_L(0); MMA(0, 0, At, B0, 2); BAR; SCHED;
        LDB(B1, 0, 1); if (more) STAGE_B(0, 0, t + 2);
        BAR; WAIT_L(0); MMA(0, 1, At, B1, 1); BAR;
        LDA(At, 0, 1); if (more) STAGE_A(0, 0, t + 2);
        BAR; WAIT_L(0); MMA(1, 0, At, B0, 1); BAR; SCHED;
        if (more) { STAGE_B(0, 1, t + 2); WAIT_V(6); } else { WAIT_V(0); }
        BAR; MMA(1, 1, At, B1, 2); BAR;
        LDB(B0, 1, 0); SCHED; LDA(At, 1, 0); if (more) STAGE_A(0, 1, t + 2);
        WAIT_L(8); BAR; WAIT_L(0); MMA(0, 0, At, B0, 2); BAR; SCHED;
        LDB(B1, 1, 1); if (more) STAGE_B(1, 0, t + 3);
        BAR; WAIT_L(0); MMA(0, 1, At, B1, 1); BAR;
        LDA(At, 1, 1); if (more) STAGE_A(1, 0, t + 3);
        BAR; WAIT_L(0); MMA(1, 0, At, B0, 1); BAR; SCHED;
        if (more) { STAGE_B(1, 1, t + 3); WAIT_V(6); }
        BAR; MMA(1, 1, At, B1, 2); BAR;
    }
    if (wr == 0) BAR;
    LAS float* rowstat = (LAS float*)lds;
    LAS float* colsum = (LAS float*)(lds + 2048);
    if (fr == 0) {
#pragma unroll
        for (int b = 0; b < 2; ++b)
#pragma unroll
            for (int n = 0; n < 2; ++n) *(LAS f32x4*)(colsum + wr * 256 + b * 128 + wc * 32 + n * 16 + 4 * g) = cs[b][n];
    }
    if (tid < 256) {
        LAS const f32x4* sp = (LAS const f32x4*)(lds + 8 * HTB + tid * 128);
        f32x4 t = sp[tid & 7];
#pragma unroll
        for (int e = 1; e < 8; ++e) t += sp[e ^ (tid & 7)];
        const float mean = (t[0] + t[2]) * (1.0f / D_MODEL);
        const float rstd = 1.0f / sqrtf(fmaxf((t[1] + t[3]) * (1.0f / D_MODEL) - mean * mean, 0.f) + 1e-5f);
        rowstat[2 * tid] = mean; rowstat[2 * tid + 1] = rstd;
    }
    __syncthreads();
    f32x4 csv[2][2];
#pragma unroll
    for (int b = 0; b < 2; ++b)
#pragma unroll
        for (int n = 0; n < 2; ++n) {
            const int c = b * 128 + wc * 32 + n * 16 + 4 * g;
            csv[b][n] = *(LAS const f32x4*)(colsum + c) + *(LAS const f32x4*)(colsum + 256 + c);
        }
#pragma unroll
    for (int ai = 0; ai < 2; ++ai)
#pragma unroll
        for (int m = 0; m < 4; ++m) {
            const int rl = ai * 128 + wr * 64 + m * 16 + fr;
            const float mean = rowstat[2 * rl], rstd = rowstat[2 * rl + 1];
#pragma unroll
            for (int n = 0; n < 2; ++n) {
                const f32x4 gv = (acc[ai][0][m][n] - csv[0][n] * mean) * rstd;
                const f32x4 uv = (acc[ai][1][m][n] - csv[1][n] * mean) * rstd;
                float o[4];
#pragma unroll
                for (int r = 0; r < 4; ++r) o[r] = gv[r] * __builtin_amdgcn_rcpf(1.0f + __expf(-gv[r])) * uv[r];
                u32x2 w; w[0] = pk_bf16(o[0], o[1]); w[1] = pk_bf16(o[2], o[3]);
                *(u32x2*)(C + (size_t)(m0 + rl) * LDW + n0 + wc * 32 + n * 16 + 4 * g) = w;
            }
        }
#undef SA_OFF
#undef SB_OFF
#undef STAGE_A
#undef STAGE_B
#undef LDA
#undef LDB
#undef MMA
}

template <int NST>
__global__ __launch_bounds__(512, 2) void glu2_kernel(
    const bf16_t* __restrict__ X, const bf16_t* __restrict__ W1, const bf16_t* __restrict__ W3,
    const float* __restrict__ stat_in, bf16_t* __restrict__ C) {
    constexpr int K = D_MODEL, LDW = F_DIM, STB = 32768, NT = K / 32;
    extern __shared__ __attribute__((aligned(16))) char smem[];
    LAS char* lds = (LAS char*)smem;
    const unsigned lds_base = (unsigned)(uintptr_t)lds;
    const int tid = threadIdx.x, lane = tid & 63;
    const int wave = __builtin_amdgcn_readfirstlane(tid >> 6);
    const int wm = wave >> 2, wn = wave & 3;
    int tm, tn;
    { const int xcd = blockIdx.x & 7, idx = blockIdx.x >> 3; tm = (xcd >> 2) * 4 + (idx & 3); tn = (xcd & 3) * 7 + (idx >> 2); }
    const int m0 = tm * 256, n0 = tn * 128;

    unsigned xoffs[2], woffs[2];
#pragma unroll
    for (int j = 0; j < 2; ++j) {
        const int pc = wave + 8 * j;
        const int row = pc * 16 + (lane >> 2), ch = (lane & 3) ^ ((4 - ((row >> 2) & 3)) & 3);
        xoffs[j] = (unsigned)(row * K * 2 + ch * 16);
        const int kb = pc >> 2, nb = 4 * (pc & 1) + (lane >> 4);
        const int r = ((lane & 15) >> 1) ^ (4 * (kb & 1)), half = lane & 1;
        woffs[j] = (unsigned)(((8 * kb + r) * LDW + 16 * nb + 8 * half) * 2);
    }
    const char* xsrc = (const char*)(X + (size_t)m0 * K);
    const char* wsrc = (const char*)(((wave & 2) ? W3 : W1) + n0);
    auto issue = [&](int buf) {
        const unsigned b = lds_base + buf * STB;
        glds16(xsrc, xoffs[0], b + wave * 1024);
        glds16(xsrc, xoffs[1], b + (wave + 8) * 1024);
        glds16(wsrc, woffs[0], b + 16384 + wave * 1024);
        glds16(wsrc, woffs[1], b + 16384 + (wave + 8) * 1024);
        xsrc += 64; wsrc += (size_t)32 * LDW * 2;
    };
    const int fr = lane & 15, g = lane >> 4, q = (lane & 15) >> 2, p = lane & 3;
    const int xo0 = (wm * 128 + fr) * 64 + ((g ^ ((4 - (fr >> 2)) & 3)) << 4);
    const int wo0 = 16384 + g * 4096 + (q + 4 * (g & 1)) * 32 + p * 8 + wn * 512;

    f32x4 acc[8][4];
#pragma unroll
    for (int i = 0; i < 8; ++i)
#pragma unroll
        for (int j = 0; j < 4; ++j) acc[i][j] = (f32x4){0.f, 0.f, 0.f, 0.f};
    f32x4 cs[4] = {};
    const bf16x8 ones = {0x3f80, 0x3f80, 0x3f80, 0x3f80, 0x3f80, 0x3f80, 0x3f80, 0x3f80};

    {
        const char* ssrc = (const char*)(stat_in + (size_t)m0 * 32);
#pragma unroll
        for (int j = 0; j < 4; ++j) glds16(ssrc, (unsigned)((wave * 4 + j) * 1024 + lane * 16), lds_base + NST * STB + (wave * 4 + j) * 1024);
    }
#pragma unroll
    for (int s = 0; s < NST; ++s) issue(s);

    auto read_frags = [&](LAS const char* base, bf16x8 (&xf)[8], bf16x8 (&wf)[4]) {
#pragma unroll
        for (int j = 0; j < 4; ++j) {
            const int o = (j & 1) * 256 + (j >> 1) * 2048;
            s16x4 lo = tr_read(base + wo0 + o);
            s16x4 hi = tr_read(base + (wo0 ^ 128) + o);
            wf[j] = (bf16x8){lo[0], lo[1], lo[2], lo[3], hi[0], hi[1], hi[2], hi[3]};
        }
#pragma unroll
        for (int i = 0; i < 8; ++i) xf[i] = *(LAS const bf16x8*)(base + xo0 + i * 1024);
    };
    auto do_mfma = [&](bf16x8 (&xf)[8], bf16x8 (&wf)[4]) {
#pragma unroll
        for (int i = 0; i < 8; ++i)
#pragma unroll
            for (int j = 0; j < 4; ++j)
                acc[i][j] = __builtin_amdgcn_mfma_f32_16x16x32_bf16(wf[j], xf[i], acc[i][j], 0, 0, 0);
    };
    auto interleave = [&]() {
#pragma unroll
        for (int i = 0; i < 16; ++i) {
            __builtin_amdgcn_sched_group_barrier(0x8, 2, 0);
            __builtin_amdgcn_sched_group_barrier(0x100, 1, 0);
        }
    };
    auto colsum = [&](bf16x8 (&wf)[4]) {
#pragma unroll
        for (int j = 0; j < 4; ++j) cs[j] = __builtin_amdgcn_mfma_f32_16x16x32_bf16(wf[j], ones, cs[j], 0, 0, 0);
    };
    bf16x8 xa[8], wa[4], xb[8], wb[4];
    wait_vm<(NST - 1) * 4>();
    __builtin_amdgcn_s_barrier();
    asm volatile("" ::: "memory");
    read_frags(lds, xa, wa);
    int buf = 0;
    auto step = [&](int kt, bf16x8 (&xc)[8], bf16x8 (&wc_)[4], bf16x8 (&xn)[8], bf16x8 (&wn_)[4], bool docs) {
        const int rem = NT - 2 - kt;
        if (rem >= NST - 2) wait_vm<(NST - 2) * 4>();
        else if (rem == 1) wait_vm<4>();
        else if (NST > 4 && rem == 2) wait_vm<8>();
        else wait_vm<0>();
        asm volatile("s_waitcnt lgkmcnt(0)" ::: "memory");
        __builtin_amdgcn_s_barrier();
        asm volatile("" ::: "memory");
        if (kt + NST < NT) issue(buf);
        buf = (buf + 1 == NST) ? 0 : buf + 1;
        __builtin_amdgcn_sched_barrier(0);
        read_frags(lds + buf * STB, xn, wn_);
        do_mfma(xc, wc_);
        interleave();
        __builtin_amdgcn_sched_barrier(0);
        if (docs) colsum(wc_);
        __builtin_amdgcn_sched_barrier(0);
    };
    for (int kt = 0; kt < NT; kt += 2) {
        step(kt, xa, wa, xb, wb, wm == 0);
        step(kt + 1, xb, wb, xa, wa, wm == 1);
    }
    __syncthreads();
    LAS float* rowstat = (LAS float*)lds;
    LAS float* colsm = (LAS float*)(lds + 2048);
    if (fr == 0) {
#pragma unroll
        for (int j = 0; j < 4; ++j) *(LAS f32x4*)(colsm + wm * 256 + (j >> 1) * 128 + wn * 32 + (j & 1) * 16 + 4 * g) = cs[j];
    }
    if (tid < 256) {
        LAS const f32x4* sp = (LAS const f32x4*)(lds + NST * STB + tid * 128);
        f32x4 t = sp[tid & 7];
#pragma unroll
        for (int e = 1; e < 8; ++e) t += sp[e ^ (tid & 7)];
        const float mean = (t[0] + t[2]) * (1.0f / D_MODEL);
        const float rstd = 1.0f / sqrtf(fmaxf((t[1] + t[3]) * (1.0f / D_MODEL) - mean * mean, 0.f) + 1e-5f);
        rowstat[2 * tid] = mean; rowstat[2 * tid + 1] = rstd;
    }
    __syncthreads();
    f32x4 csv[4];
#pragma unroll
    for (int j = 0; j < 4; ++j) {
        const int c = (j >> 1) * 128 + wn * 32 + (j & 1) * 16 + 4 * g;
        csv[j] = *(LAS const f32x4*)(colsm + c) + *(LAS const f32x4*)(colsm + 256 + c);
    }
#pragma unroll
    for (int i = 0; i < 8; ++i) {
        const int rl = wm * 128 + i * 16 + fr;
        const float mean = rowstat[2 * rl], rstd = rowstat[2 * rl + 1];
#pragma unroll
        for (int n = 0; n < 2; ++n) {
            const f32x4 gv = (acc[i][n] - csv[n] * mean) * rstd;
            const f32x4 uv = (acc[i][n + 2] - csv[n + 2] * mean) * rstd;
            float o[4];
#pragma unroll
            for (int r = 0; r < 4; ++r) o[r] = gv[r] * __builtin_amdgcn_rcpf(1.0f + __expf(-gv[r])) * uv[r];
            u32x2 w; w[0] = pk_bf16(o[0], o[1]); w[1] = pk_bf16(o[2], o[3]);
            *(u32x2*)(C + (size_t)(m0 + rl) * LDW + n0 + wn * 32 + n * 16 + 4 * g) = w;
        }
    }
}

__global__ __launch_bounds__(512) void attn_kernel(const bf16_t* __restrict__ qkv, bf16_t* __restrict__ O) {
    extern __shared__ __attribute__((aligned(16))) char smem[];
    LAS char* lds = (LAS char*)smem;
    LAS char* ldsK = lds;
    LAS char* ldsV = lds + 32768;
    const int tid = threadIdx.x, lane = tid & 63;
    const int w = __builtin_amdgcn_readfirstlane(tid >> 6);
    const int h = blockIdx.x & 31, qb = blockIdx.x >> 5;
    const int q0 = qb * 256, kbase = q0 - 256;
    constexpr int LD = 3 * D_MODEL;

    {
        u32x4 kv[4], vv[4];
#pragma unroll
        for (int i = 0; i < 4; ++i) {
            const int idx = tid + 512 * i, key = idx >> 2, ch = idx & 3;
            const int gk = kbase + key;
            if (gk >= 0) {
                const bf16_t* src = qkv + (size_t)gk * LD + h * 32 + ch * 8;
                kv[i] = *(const u32x4*)(src + D_MODEL);
                vv[i] = *(const u32x4*)(src + 2 * D_MODEL);
            }
        }
#pragma unroll
        for (int i = 0; i < 4; ++i) {
            const int idx = tid + 512 * i, key = idx >> 2, ch = idx & 3;
            const int gk = kbase + key;
            if (gk >= 0) {
                *(LAS u32x4*)(ldsK + key * 64 + ((ch ^ ((key >> 2) & 3)) << 4)) = kv[i];
                *(LAS u32x4*)(ldsV + key * 64 + (ch << 4)) = vv[i];
            }
        }
    }
    const int r32 = lane & 31, hi = lane >> 5;
    const int qw = q0 + 32 * w;
    bf16x8 qf[2];
#pragma unroll
    for (int s = 0; s < 2; ++s)
        qf[s] = *(const bf16x8*)(qkv + (size_t)(qw + r32) * LD + h * 32 + 16 * s + 8 * hi);
    __syncthreads();

    f32x16 sc[9];
    const float NEG = -INFINITY;
#pragma unroll
    for (int j = 0; j < 9; ++j) {
        const int kb = qw - 256 + 32 * j;
        if (kb >= 0) {
            const int key = 32 * (w + j) + r32;
            f32x16 a = {};
#pragma unroll
            for (int s = 0; s < 2; ++s) {
                const bf16x8 kf = *(LAS const bf16x8*)(ldsK + key * 64 + (((2 * s + hi) ^ ((key >> 2) & 3)) << 4));
                a = __builtin_amdgcn_mfma_f32_32x32x16_bf16(kf, qf[s], a, 0, 0, 0);
            }
            sc[j] = a;
        } else {
#pragma unroll
            for (int r = 0; r < 16; ++r) sc[j][r] = NEG;
        }
    }
#pragma unroll
    for (int r = 0; r < 16; ++r) {
        const int i = (r & 3) + 8 * (r >> 2) + 4 * hi;
        if (!(i > r32)) sc[0][r] = NEG;
        if (!(i <= r32)) sc[8][r] = NEG;
    }
    float mx = NEG;
#pragma unroll
    for (int j = 0; j < 9; ++j)
#pragma unroll
        for (int r = 0; r < 16; ++r) mx = fmaxf(mx, sc[j][r]);
    mx = fmaxf(mx, __shfl_xor(mx, 32));
    const float c = 0.17677669529663687f * 1.4426950408889634f;
    const float nm = -mx * c;
    float l = 0.f;
#pragma unroll
    for (int j = 0; j < 9; ++j)
#pragma unroll
        for (int r = 0; r < 16; ++r) {
            const float e = __builtin_amdgcn_exp2f(__builtin_fmaf(sc[j][r], c, nm));
            sc[j][r] = e; l += e;
        }
    l += __shfl_xor(l, 32);

    f32x16 o = {};
    const int gq = (lane & 15) >> 2, gp = lane & 3, gd = (lane >> 4) & 1;
#pragma unroll
    for (int j = 0; j < 9; ++j) {
        const int kb = qw - 256 + 32 * j;
        if (kb >= 0) {
#pragma unroll
            for (int s = 0; s < 2; ++s) {
                u32x4 pw;
#pragma unroll
                for (int e = 0; e < 4; ++e) pw[e] = pk_bf16(sc[j][8 * s + 2 * e], sc[j][8 * s + 2 * e + 1]);
                const int key = 32 * (w + j) + 16 * s + 4 * hi + gq;
                LAS const char* vp = ldsV + key * 64 + (16 * gd + 4 * gp) * 2;
                s16x4 lo = tr_read(vp), hi8 = tr_read(vp + 8 * 64);
                const bf16x8 vf = (bf16x8){lo[0], lo[1], lo[2], lo[3], hi8[0], hi8[1], hi8[2], hi8[3]};
                o = __builtin_amdgcn_mfma_f32_32x32x16_bf16(vf, __builtin_bit_cast(bf16x8, pw), o, 0, 0, 0);
            }
        }
    }
    const float rl = 1.0f / l;
    bf16_t* dst = O + (size_t)(qw + r32) * D_MODEL + h * 32 + 4 * hi;
#pragma unroll
    for (int t = 0; t < 4; ++t) {
        u32x2 wv; wv[0] = pk_bf16(o[4 * t] * rl, o[4 * t + 1] * rl); wv[1] = pk_bf16(o[4 * t + 2] * rl, o[4 * t + 3] * rl);
        *(u32x2*)(dst + 8 * t) = wv;
    }
}

template <typename KF> static void set_lds(KF kf, int bytes) {
    (void)hipFuncSetAttribute((const void*)kf, hipFuncAttributeMaxDynamicSharedMemorySize, bytes);
}

extern "C" void kernel_launch(void* const* d_in, const int* in_sizes, int n_in,
                              void* d_out, int out_size, void* d_ws, size_t ws_size,
                              hipStream_t stream) {
    const bf16_t* x = (const bf16_t*)d_in[0];
    const bf16_t* wqkv = (const bf16_t*)d_in[1];
    const bf16_t* wo = (const bf16_t*)d_in[2];
    const bf16_t* w1 = (const bf16_t*)d_in[4];
    const bf16_t* w3 = (const bf16_t*)d_in[5];
    const bf16_t* w2 = (const bf16_t*)d_in[6];
    bf16_t* out = (bf16_t*)d_out;
    char* ws = (char*)d_ws;
    bf16_t* qkv = (bf16_t*)(ws + (4u << 20));
    bf16_t* att = (bf16_t*)(ws + (16u << 20));
    bf16_t* hbuf = (bf16_t*)(ws + (20u << 20));
    bf16_t* act = (bf16_t*)(ws + (28u << 20));
    float* hstat = (float*)(ws + (24u << 20));

    {
        auto kf = gemm_kernel<128, 192, 2, 4, 4, EPI_STORE, 1, 2, 4>;
        constexpr int lds = 4 * (128 + 192) * 128;
        set_lds(kf, lds);
        kf<<<256, 512, lds, stream>>>(x, wqkv, nullptr, qkv, nullptr, S_LEN, 3072, 1024, 3072, 3072);
    }
    {
        set_lds(attn_kernel, 65536);
        attn_kernel<<<256, 512, 65536, stream>>>(qkv, att);
    }
    {
        auto kf = gemm_kernel<128, 64, 4, 2, 6, EPI_RES_STAT, 0, 4, 2>;
        constexpr int lds = 6 * (128 + 64) * 128;
        set_lds(kf, lds);
        kf<<<256, 512, lds, stream>>>(att, wo, x, hbuf, hstat, S_LEN, 1024, 1024, 1024, 1024);
    }
    {
        set_lds(glu2_kernel<4>, 163840);
        glu2_kernel<4><<<224, 512, 163840, stream>>>(hbuf, w1, w3, hstat, act);
    }
    {
        auto kf = gemm_kernel<128, 64, 4, 2, 6, EPI_RES, 0, 4, 2>;
        constexpr int lds = 6 * (128 + 64) * 128;
        set_lds(kf, lds);
        kf<<<256, 512, lds, stream>>>(act, w2, hbuf, out, nullptr, S_LEN, 1024, F_DIM, 1024, 1024);
    }
}
```

```cpp
#include <hip/hip_runtime.h>
#include <hip/hip_bf16.h>
#include <stdint.h>

typedef unsigned short bf16_t;
typedef short bf16x8 __attribute__((ext_vector_type(8)));
typedef short s16x4 __attribute__((ext_vector_type(4)));
typedef float f32x4 __attribute__((ext_vector_type(4)));
typedef float f32x16 __attribute__((ext_vector_type(16)));
typedef unsigned u32x4 __attribute__((ext_vector_type(4)));
typedef unsigned u32x2 __attribute__((ext_vector_type(2)));
#define LAS __attribute__((address_space(3)))

constexpr int S_LEN = 2048, D_MODEL = 1024, F_DIM = 3584, N_HEAD = 32, D_HEAD = 32, WIN = 256;

__device__ __forceinline__ float bf2f(unsigned v16) { return __uint_as_float(v16 << 16); }
__device__ __forceinline__ unsigned pk_bf16(float lo, float hi) {
    unsigned r; asm("v_cvt_pk_bf16_f32 %0, %1, %2" : "=v"(r) : "v"(lo), "v"(hi)); return r;
}
__device__ __forceinline__ s16x4 tr_read(LAS const char* p) {
    return __builtin_amdgcn_ds_read_tr16_b64_v4i16((LAS s16x4*)p);
}
__device__ __forceinline__ void glds16(const void* sbase, unsigned voff, unsigned lds_addr) {
    unsigned keep;
    asm volatile("s_mov_b32 %0, m0\n\ts_mov_b32 m0, %3\n\ts_nop 0\n\tglobal_load_lds_dwordx4 %2, %1\n\ts_mov_b32 m0, %0"
                 : "=&s"(keep) : "s"(sbase), "v"(voff), "s"(lds_addr) : "memory");
}
typedef __bf16 bf16x2_t __attribute__((ext_vector_type(2)));
__device__ __forceinline__ float dot2(unsigned a, unsigned b, float acc) {
    return __builtin_amdgcn_fdot2_f32_bf16(__builtin_bit_cast(bf16x2_t, a), __builtin_bit_cast(bf16x2_t, b), acc, false);
}
__device__ __forceinline__ void frag_sum(const bf16x8& f, float& s1) {
    const u32x4 u = __builtin_bit_cast(u32x4, f);
#pragma unroll
    for (int e = 0; e < 4; ++e) s1 = dot2(u[e], 0x3f803f80u, s1);
}
__device__ __forceinline__ void frag_sum_sq(const bf16x8& f, float& s1, float& s2) {
    const u32x4 u = __builtin_bit_cast(u32x4, f);
#pragma unroll
    for (int e = 0; e < 4; ++e) { s1 = dot2(u[e], 0x3f803f80u, s1); s2 = dot2(u[e], u[e], s2); }
}
__device__ __forceinline__ float red_g(float v) { v += __shfl_xor(v, 16); v += __shfl_xor(v, 32); return v; }
__device__ __forceinline__ void prefetch_region(const char* base, int lpr, long pitch, int chunk, int part, int tid,
                                                int nthreads, unsigned& dummy) {
    for (int l = tid; l < chunk; l += nthreads) {
        const int L = part * chunk + l, row = L / lpr, c = L - row * lpr;
        const char* a = base + (long)row * pitch + c * 128;
        asm volatile("global_load_dword %0, %1, off" : "+v"(dummy) : "v"(a) : "memory");
    }
}
template <int N> __device__ __forceinline__ void wait_vm() { asm volatile("s_waitcnt vmcnt(%0)" ::"n"(N) : "memory"); }

enum { EPI_STORE = 0, EPI_RES = 1, EPI_RES_STAT = 2 };

template <int BM, int BN, int WM, int WN, int NST, int EPI, int LNF, int RM, int RN>
__global__ __launch_bounds__(WM * WN * 64) void gemm_kernel(
    const bf16_t* __restrict__ X, const bf16_t* __restrict__ W0,
    const bf16_t* __restrict__ R, bf16_t* __restrict__ C, float* __restrict__ stat_out,
    int M, int N, int K, int ldw, int ldc) {
    constexpr int NWAVE = WM * WN;
    constexpr int MF = BM / 16 / WM, NF = BN / 16 / WN, NB = BN / 16;
    constexpr int A_BYTES = BM * 128, B_BYTES = BN * 128, ST_BYTES = A_BYTES + B_BYTES;
    constexpr int LPW = (BM + BN) / 8 / NWAVE;
    constexpr int LPA = BM / 8 / NWAVE;
    static_assert((BM + BN) % (8 * NWAVE) == 0 && (BM % (8 * NWAVE)) == 0, "piece split");
    static_assert(NB % 4 == 0, "NB%4");
    static_assert(!LNF || (MF == WN && WM == 2), "LN fold layout");
    extern __shared__ __attribute__((aligned(16))) char smem[];
    LAS char* lds = (LAS char*)smem;

    const int tid = threadIdx.x, lane = tid & 63;
    const int wave = __builtin_amdgcn_readfirstlane(tid >> 6);
    const int wm = wave / WN, wn = wave % WN;

    const int tiles_m = M / BM, tiles_n = N / BN;
    int tm, tn;
    {
        const int xcd = blockIdx.x & 7, idx = blockIdx.x >> 3;
        const int rm = xcd / RN, rn = xcd % RN;
        const int sm = tiles_m / RM, sn = tiles_n / RN;
        tm = rm * sm + idx % sm; tn = rn * sn + idx / sm;
    }
    const int m0 = tm * BM, n0 = tn * BN;
    unsigned pf_dummy = 0;

    unsigned voff[LPW];
    const char* sbase[LPW];
#pragma unroll
    for (int j = 0; j < LPW; ++j) {
        const int ii = wave + NWAVE * j;
        if (j < LPA) {
            const int row = ii * 8 + (lane >> 3), ch = (lane & 7) ^ (row & 7);
            voff[j] = (unsigned)(row * K * 2 + ch * 16);
            sbase[j] = (const char*)(X + (size_t)m0 * K);
        } else {
            const int pi = ii - BM / 8;
            const int kb = pi / (NB / 4), nb = 4 * (pi % (NB / 4)) + (lane >> 4);
            const int r = ((lane & 15) >> 1) ^ (4 * (kb & 1)), half = lane & 1;
            voff[j] = (unsigned)(((8 * kb + r) * ldw + 16 * nb + 8 * half) * 2);
            sbase[j] = (const char*)(W0 + n0);
        }
    }
    const unsigned lds_base = (unsigned)(uintptr_t)lds;
    const int NT = K / 64;
    auto issue = [&](int buf) {
#pragma unroll
        for (int j = 0; j < LPW; ++j) {
            const int ii = wave + NWAVE * j;
            glds16(sbase[j], voff[j], lds_base + buf * ST_BYTES + ii * 1024);
            sbase[j] += (j < LPA) ? 128 : (long)128 * ldw;
        }
    };

    const int fr = lane & 15, g = lane >> 4, q = (lane & 15) >> 2, p = lane & 3;
    const int rotm = LNF ? wn : 0, rotk = LNF ? wm : 0;
    const int xrow = wm * (MF * 16) + fr;
    const int xoff0 = xrow * 128 + ((g ^ (xrow & 7)) << 4);
    const int woff0 = A_BYTES + g * NB * 256 + (q + 4 * (g & 1)) * 32 + p * 8 + wn * NF * 256;

    f32x4 acc[MF][NF];
#pragma unroll
    for (int i = 0; i < MF; ++i)
#pragma unroll
        for (int j = 0; j < NF; ++j) acc[i][j] = (f32x4){0.f, 0.f, 0.f, 0.f};
    f32x4 s1 = {0.f, 0.f, 0.f, 0.f}, s2 = {0.f, 0.f, 0.f, 0.f}, cs[NF];
#pragma unroll
    for (int j = 0; j < NF; ++j) cs[j] = (f32x4){0.f, 0.f, 0.f, 0.f};
    const bf16x8 ones = {0x3f80, 0x3f80, 0x3f80, 0x3f80, 0x3f80, 0x3f80, 0x3f80, 0x3f80};

#pragma unroll
    for (int s = 0; s < NST - 1; ++s) issue(s);

    auto read_frags = [&](LAS const char* base, int slot, bf16x8 (&xf)[MF], bf16x8 (&wf)[NF]) {
        const int ks = slot ^ rotk;
#pragma unroll
        for (int j = 0; j < NF; ++j) {
            const int o = ks * (4 * NB * 256) + j * 256;
            s16x4 lo = tr_read(base + woff0 + o);
            s16x4 hi = tr_read(base + (woff0 ^ 128) + o);
            wf[j] = (bf16x8){lo[0], lo[1], lo[2], lo[3], hi[0], hi[1], hi[2], hi[3]};
        }
#pragma unroll
        for (int i = 0; i < MF; ++i)
            xf[i] = *(LAS const bf16x8*)(base + ((xoff0 ^ (ks * 64)) + (i ^ rotm) * 2048));
    };
    auto do_mfma = [&](bf16x8 (&xf)[MF], bf16x8 (&wf)[NF], bool colsum) {
#pragma unroll
        for (int i = 0; i < MF; ++i)
#pragma unroll
            for (int j = 0; j < NF; ++j)
                acc[i][j] = __builtin_amdgcn_mfma_f32_16x16x32_bf16(wf[j], xf[i], acc[i][j], 0, 0, 0);
        if (LNF) {
            s1 = __builtin_amdgcn_mfma_f32_16x16x32_bf16(ones, xf[0], s1, 0, 0, 0);
            s2 = __builtin_amdgcn_mfma_f32_16x16x32_bf16(xf[0], xf[0], s2, 0, 0, 0);
            if (colsum) {
#pragma unroll
                for (int j = 0; j < NF; ++j) cs[j] = __builtin_amdgcn_mfma_f32_16x16x32_bf16(wf[j], ones, cs[j], 0, 0, 0);
            }
        }
    };
    auto interleave = [&]() {
        constexpr int NM = MF * NF, NR = MF + 2 * NF, RPM = (NR + NM - 1) / NM;
#pragma unroll
        for (int i = 0; i < NM; ++i) {
            __builtin_amdgcn_sched_group_barrier(0x8, 1, 0);
            __builtin_amdgcn_sched_group_barrier(0x100, RPM, 0);
        }
    };
    bf16x8 xa[MF], wa[NF], xb[MF], wb[NF];
    wait_vm<(NST - 2) * LPW>();
    __builtin_amdgcn_s_barrier();
    asm volatile("" ::: "memory");
    read_frags(lds, 0, xa, wa);

    int buf = 0, pbuf = NST - 1;
    for (int kt = 0; kt < NT; ++kt) {
        const int rem = NT - 2 - kt;
        if (rem >= NST - 3) wait_vm<(NST - 3) * LPW>();
        else if (NST > 4 && rem == 1) wait_vm<LPW>();
        else if (NST > 5 && rem == 2) wait_vm<2 * LPW>();
        else wait_vm<0>();
        __builtin_amdgcn_s_barrier();
        asm volatile("" ::: "memory");
        if (kt + NST - 1 < NT) issue(pbuf);
        const int nbuf = (buf + 1 == NST) ? 0 : buf + 1;
        __builtin_amdgcn_sched_barrier(0);
        read_frags(lds + buf * ST_BYTES, 1, xb, wb);
        do_mfma(xa, wa, true);
        interleave();
        __builtin_amdgcn_sched_barrier(0);
        read_frags(lds + nbuf * ST_BYTES, 0, xa, wa);
        do_mfma(xb, wb, false);
        interleave();
        __builtin_amdgcn_sched_barrier(0);
        pbuf = buf; buf = nbuf;
    }

    asm volatile("" :: "v"(pf_dummy));
    LAS float* rowstat = (LAS float*)lds;
    LAS float* colsum = (LAS float*)(lds + BM * 8);
    if (LNF) {
        __syncthreads();
        if (g == (fr >> 2)) {
            const int r = wm * (MF * 16) + wn * 16 + fr;
            const int d = fr & 3;
            rowstat[2 * r] = s1[0];
            rowstat[2 * r + 1] = d == 0 ? s2[0] : d == 1 ? s2[1] : d == 2 ? s2[2] : s2[3];
        }
        if (fr == 0) {
#pragma unroll
            for (int j = 0; j < NF; ++j) *(LAS f32x4*)(colsum + wm * BN + (wn * NF + j) * 16 + 4 * g) = cs[j];
        }
        __syncthreads();
    }
    if (EPI == EPI_RES_STAT) __syncthreads();
    f32x4 csv[NF];
    if (LNF) {
#pragma unroll
        for (int j = 0; j < NF; ++j) {
            const int c = (wn * NF + j) * 16 + 4 * g;
            csv[j] = *(LAS const f32x4*)(colsum + c) + *(LAS const f32x4*)(colsum + BN + c);
        }
    }
#pragma unroll
    for (int i = 0; i < MF; ++i) {
        const int rl = wm * (MF * 16) + (i ^ rotm) * 16 + fr;
        const int m = m0 + rl;
        float mean = 0.f, rstd = 1.f, ps1 = 0.f, ps2 = 0.f;
        if (LNF) {
            const float a = rowstat[2 * rl], b = rowstat[2 * rl + 1];
            mean = a * (1.0f / D_MODEL);
            const float var = fmaxf(b * (1.0f / D_MODEL) - mean * mean, 0.f);
            rstd = 1.0f / sqrtf(var + 1e-5f);
        }
#pragma unroll
        for (int j = 0; j < NF; ++j) {
            const int n = n0 + (wn * NF + j) * 16 + 4 * g;
            f32x4 v = acc[i][j];
            if (LNF) v = (v - csv[j] * mean) * rstd;
            if (EPI != EPI_STORE) {
                const u32x2 rv = *(const u32x2*)(R + (size_t)m * ldc + n);
                v[0] += bf2f(rv[0] & 0xffffu); v[1] += bf2f(rv[0] >> 16);
                v[2] += bf2f(rv[1] & 0xffffu); v[3] += bf2f(rv[1] >> 16);
            }
            u32x2 w; w[0] = pk_bf16(v[0], v[1]); w[1] = pk_bf16(v[2], v[3]);
            *(u32x2*)(C + (size_t)m * ldc + n) = w;
            if (EPI == EPI_RES_STAT) {
                const float a0 = bf2f(w[0] & 0xffffu), a1 = bf2f(w[0] >> 16), a2 = bf2f(w[1] & 0xffffu), a3 = bf2f(w[1] >> 16);
                ps1 += (a0 + a1) + (a2 + a3);
                ps2 += (a0 * a0 + a1 * a1) + (a2 * a2 + a3 * a3);
            }
        }
        if (EPI == EPI_RES_STAT) {
            ps1 = red_g(ps1); ps2 = red_g(ps2);
            if (g == 0) { rowstat[(rl * WN + wn) * 2] = ps1; rowstat[(rl * WN + wn) * 2 + 1] = ps2; }
        }
    }
    if (EPI == EPI_RES_STAT) {
        __syncthreads();
        if (tid < BM) {
            float a = 0.f, b = 0.f;
#pragma unroll
            for (int w = 0; w < WN; ++w) { a += rowstat[(tid * WN + w) * 2]; b += rowstat[(tid * WN + w) * 2 + 1]; }
            float2 o; o.x = a; o.y = b;
            *(float2*)(stat_out + ((size_t)(m0 + tid) * tiles_n + tn) * 2) = o;
        }
    }
}

__global__ __launch_bounds__(512, 2) void glu_gemm_kernel(
    const bf16_t* __restrict__ X, const bf16_t* __restrict__ W1, const bf16_t* __restrict__ W3,
    const float* __restrict__ stat_in, bf16_t* __restrict__ C, const bf16_t* __restrict__ pfw) {
    constexpr int K = D_MODEL, LDW = F_DIM, HTB = 16384;
    if (blockIdx.x >= 224) {
        u32x4 d = {0u, 0u, 0u, 0u};
        const char* a = (const char*)pfw + (size_t)(blockIdx.x - 224) * 229376 + threadIdx.x * 16;
#pragma unroll
        for (int r = 0; r < 28; ++r) asm volatile("global_load_dwordx4 %0, %1, off offset:0" : "+v"(d) : "v"(a + r * 8192) : "memory");
        asm volatile("s_waitcnt vmcnt(0)" :: "v"(d) : "memory");
        return;
    }
    extern __shared__ __attribute__((aligned(16))) char smem[];
    LAS char* lds = (LAS char*)smem;
    const unsigned lds_base = (unsigned)(uintptr_t)lds;
    const int tid = threadIdx.x, lane = tid & 63;
    const int wave = __builtin_amdgcn_readfirstlane(tid >> 6);
    const int wr = wave >> 2, wc = wave & 3;
    int tm, tn;
    { const int xcd = blockIdx.x & 7, idx = blockIdx.x >> 3; tm = (xcd >> 2) * 4 + (idx & 3); tn = (xcd & 3) * 7 + (idx >> 2); }
    const int m0 = tm * 256, n0 = tn * 128;
    unsigned pf_dummy = 0;

    unsigned xoffs[2], woffs[2];
#pragma unroll
    for (int j = 0; j < 2; ++j) {
        const int pc = wave + 8 * j;
        const int row = pc * 8 + (lane >> 3), ch = (lane & 7) ^ (row & 7);
        xoffs[j] = (unsigned)(row * K * 2 + ch * 16);
        const int kb = pc >> 1, nb = 4 * (pc & 1) + (lane >> 4);
        const int r = ((lane & 15) >> 1) ^ (4 * (kb & 1)), half = lane & 1;
        woffs[j] = (unsigned)(((8 * kb + r) * LDW + 16 * nb + 8 * half) * 2);
    }
    const char* xsrc = (const char*)(X + (size_t)m0 * K);
    const char* w1src = (const char*)(W1 + n0);
    const char* w3src = (const char*)(W3 + n0);
#define SA_OFF(b, h) (((b) * 2 + (h)) * HTB)
#define SB_OFF(b, h) ((4 + (b) * 2 + (h)) * HTB)
#define STAGE_A(b, h, kt) do { const char* _s = xsrc + (size_t)(h) * 128 * K * 2 + (kt) * 128; \
        glds16(_s, xoffs[0], lds_base + SA_OFF(b, h) + wave * 1024); glds16(_s, xoffs[1], lds_base + SA_OFF(b, h) + (wave + 8) * 1024); } while (0)
#define STAGE_B(b, h, kt) do { const char* _s = ((h) ? w3src : w1src) + (size_t)(kt) * 64 * LDW * 2; \
        glds16(_s, woffs[0], lds_base + SB_OFF(b, h) + wave * 1024); glds16(_s, woffs[1], lds_base + SB_OFF(b, h) + (wave + 8) * 1024); } while (0)
    const int fr = lane & 15, g = lane >> 4, q = (lane & 15) >> 2, p = lane & 3;
    const int xrow = wr * 64 + fr;
    const int xo0 = (xrow * 128 + ((g ^ (xrow & 7)) << 4)) ^ (wr * 64);
    const int wo0 = g * 2048 + (q + 4 * (g & 1)) * 32 + p * 8 + wc * 512;
    unsigned xb_[4][2], wb_[2][2];
#pragma unroll
    for (int m = 0; m < 4; ++m)
#pragma unroll
        for (int k = 0; k < 2; ++k) { xb_[m][k] = lds_base + ((xo0 ^ (k * 64)) + m * 2048); asm volatile("" : "+v"(xb_[m][k])); }
#pragma unroll
    for (int t = 0; t < 2; ++t)
#pragma unroll
        for (int k = 0; k < 2; ++k) { wb_[t][k] = lds_base + 4 * HTB + ((wo0 ^ (t * 128)) + (k ^ wr) * 8192); asm volatile("" : "+v"(wb_[t][k])); }
#define LDSP(a) ((LAS const char*)(uintptr_t)(a))
#define LDA(dst, b, h) do { _Pragma("unroll") for (int m = 0; m < 4; ++m) _Pragma("unroll") for (int k = 0; k < 2; ++k) \
        dst[m][k] = *(LAS const bf16x8*)(LDSP(xb_[m][k]) + SA_OFF(b, h)); } while (0)
#define LDB(dst, b, h) do { _Pragma("unroll") for (int n = 0; n < 2; ++n) _Pragma("unroll") for (int k = 0; k < 2; ++k) { \
        s16x4 lo_ = tr_read(LDSP(wb_[0][k]) + SA_OFF(b, h) + n * 256); \
        s16x4 hi_ = tr_read(LDSP(wb_[1][k]) + SA_OFF(b, h) + n * 256); \
        dst[n][k] = (bf16x8){lo_[0], lo_[1], lo_[2], lo_[3], hi_[0], hi_[1], hi_[2], hi_[3]}; } } while (0)
#define MMA(ai, bj, At, Bt, CS) do { __builtin_amdgcn_s_setprio(1); \
        _Pragma("unroll") for (int m = 0; m < 4; ++m) _Pragma("unroll") for (int n = 0; n < 2; ++n) _Pragma("unroll") for (int k = 0; k < 2; ++k) \
            acc[ai][bj][m][n] = __builtin_amdgcn_mfma_f32_16x16x32_bf16(Bt[n][k], At[m][k], acc[ai][bj][m][n], 0, 0, 0); \
        if ((CS) & 2) { cs[bj][0] = __builtin_amdgcn_mfma_f32_16x16x32_bf16(Bt[0][0], ones, cs[bj][0], 0, 0, 0); \
                        cs[bj][1] = __builtin_amdgcn_mfma_f32_16x16x32_bf16(Bt[1][0], ones, cs[bj][1], 0, 0, 0); } \
        __builtin_amdgcn_s_setprio(0); } while (0)
#define WAIT_V(n) asm volatile("s_waitcnt vmcnt(" #n ")" ::: "memory")
#define WAIT_L(n) asm volatile("s_waitcnt lgkmcnt(" #n ")" ::: "memory")
#define BAR __builtin_amdgcn_s_barrier()
#define SCHED __builtin_amdgcn_sched_barrier(0)
    f32x4 acc[2][2][4][2] = {};
    bf16x8 At[4][2], B0[2][2], B1[2][2];
    f32x4 cs[2][2] = {};
    const bf16x8 ones = {0x3f80, 0x3f80, 0x3f80, 0x3f80, 0x3f80, 0x3f80, 0x3f80, 0x3f80};
    constexpr int nt = K / 64;
    {
        const char* ssrc = (const char*)(stat_in + (size_t)m0 * 32);
#pragma unroll
        for (int j = 0; j < 4; ++j) glds16(ssrc, (unsigned)((wave * 4 + j) * 1024 + lane * 16), lds_base + 8 * HTB + (wave * 4 + j) * 1024);
    }
    STAGE_B(0, 0, 0); STAGE_A(0, 0, 0); STAGE_B(0, 1, 0); STAGE_A(0, 1, 0);
    if (wr == 1) BAR;
    WAIT_V(4); BAR;
    STAGE_B(1, 0, 1); STAGE_A(1, 0, 1); STAGE_B(1, 1, 1);
    WAIT_V(6); BAR;
    for (int t = 0; t < nt; t += 2) {
        const bool more = t + 2 < nt;
        LDB(B0, 0, 0); SCHED; LDA(At, 0, 0); STAGE_A(1, 1, t + 1);
        WAIT_L(8); BAR; WAIT_L(0); MMA(0, 0, At, B0, 2); BAR; SCHED;
        LDB(B1, 0, 1); if (more) STAGE_B(0, 0, t + 2);
        BAR; WAIT_L(0); MMA(0, 1, At, B1, 1); BAR;
        LDA(At, 0, 1); if (more) STAGE_A(0, 0, t + 2);
        BAR; WAIT_L(0); MMA(1, 0, At, B0, 1); BAR; SCHED;
        if (more) { STAGE_B(0, 1, t + 2); WAIT_V(6); } else { WAIT_V(0); }
        BAR; MMA(1, 1, At, B1, 2); BAR;
        LDB(B0, 1, 0); SCHED; LDA(At, 1, 0); if (more) STAGE_A(0, 1, t + 2);
        WAIT_L(8); BAR; WAIT_L(0); MMA(0, 0, At, B0, 2); BAR; SCHED;
        LDB(B1, 1, 1); if (more) STAGE_B(1, 0, t + 3);
        BAR; WAIT_L(0); MMA(0, 1, At, B1, 1); BAR;
        LDA(At, 1, 1); if (more) STAGE_A(1, 0, t + 3);
        BAR; WAIT_L(0); MMA(1, 0, At, B0, 1); BAR; SCHED;
        if (more) { STAGE_B(1, 1, t + 3); WAIT_V(6); }
        BAR; MMA(1, 1, At, B1, 2); BAR;
    }
    if (wr == 0) BAR;
    asm volatile("" :: "v"(pf_dummy));
    LAS float* rowstat = (LAS float*)lds;
    LAS float* colsum = (LAS float*)(lds + 2048);
    if (fr == 0) {
#pragma unroll
        for (int b = 0; b < 2; ++b)
#pragma unroll
            for (int n = 0; n < 2; ++n) *(LAS f32x4*)(colsum + wr * 256 + b * 128 + wc * 32 + n * 16 + 4 * g) = cs[b][n];
    }
    if (tid < 256) {
        LAS const f32x4* sp = (LAS const f32x4*)(lds + 8 * HTB + tid * 128);
        f32x4 t = sp[tid & 7];
#pragma unroll
        for (int e = 1; e < 8; ++e) t += sp[e ^ (tid & 7)];
        const float mean = (t[0] + t[2]) * (1.0f / D_MODEL);
        const float rstd = 1.0f / sqrtf(fmaxf((t[1] + t[3]) * (1.0f / D_MODEL) - mean * mean, 0.f) + 1e-5f);
        rowstat[2 * tid] = mean; rowstat[2 * tid + 1] = rstd;
    }
    __syncthreads();
    f32x4 csv[2][2];
#pragma unroll
    for (int b = 0; b < 2; ++b)
#pragma unroll
        for (int n = 0; n < 2; ++n) {
            const int c = b * 128 + wc * 32 + n * 16 + 4 * g;
            csv[b][n] = *(LAS const f32x4*)(colsum + c) + *(LAS const f32x4*)(colsum + 256 + c);
        }
#pragma unroll
    for (int ai = 0; ai < 2; ++ai)
#pragma unroll
        for (int m = 0; m < 4; ++m) {
            const int rl = ai * 128 + wr * 64 + m * 16 + fr;
            const float mean = rowstat[2 * rl], rstd = rowstat[2 * rl + 1];
#pragma unroll
            for (int n = 0; n < 2; ++n) {
                const f32x4 gv = (acc[ai][0][m][n] - csv[0][n] * mean) * rstd;
                const f32x4 uv = (acc[ai][1][m][n] - csv[1][n] * mean) * rstd;
                float o[4];
#pragma unroll
                for (int r = 0; r < 4; ++r) o[r] = gv[r] * __builtin_amdgcn_rcpf(1.0f + __expf(-gv[r])) * uv[r];
                u32x2 w; w[0] = pk_bf16(o[0], o[1]); w[1] = pk_bf16(o[2], o[3]);
                *(u32x2*)(C + (size_t)(m0 + rl) * LDW + n0 + wc * 32 + n * 16 + 4 * g) = w;
            }
        }
#undef SA_OFF
#undef SB_OFF
#undef STAGE_A
#undef STAGE_B
#undef LDA
#undef LDB
#undef MMA
}

template <int NST>
__global__ __launch_bounds__(512, 2) void glu2_kernel(
    const bf16_t* __restrict__ X, const bf16_t* __restrict__ W1, const bf16_t* __restrict__ W3,
    const float* __restrict__ stat_in, bf16_t* __restrict__ C) {
    constexpr int K = D_MODEL, LDW = F_DIM, STB = 32768, NT = K / 32;
    extern __shared__ __attribute__((aligned(16))) char smem[];
    LAS char* lds = (LAS char*)smem;
    const unsigned lds_base = (unsigned)(uintptr_t)lds;
    const int tid = threadIdx.x, lane = tid & 63;
    const int wave = __builtin_amdgcn_readfirstlane(tid >> 6);
    const int wm = wave >> 2, wn = wave & 3;
    int tm, tn;
    { const int xcd = blockIdx.x & 7, idx = blockIdx.x >> 3; tm = (xcd >> 2) * 4 + (idx & 3); tn = (xcd & 3) * 7 + (idx >> 2); }
    const int m0 = tm * 256, n0 = tn * 128;
    unsigned pf_dummy = 0;

    unsigned xoffs[2], woffs[2];
#pragma unroll
    for (int j = 0; j < 2; ++j) {
        const int pc = wave + 8 * j;
        const int row = pc * 16 + (lane >> 2), ch = (lane & 3) ^ ((4 - ((row >> 2) & 3)) & 3);
        xoffs[j] = (unsigned)(row * K * 2 + ch * 16);
        const int kb = pc >> 2, nb = 4 * (pc & 1) + (lane >> 4);
        const int r = ((lane & 15) >> 1) ^ (4 * (kb & 1)), half = lane & 1;
        woffs[j] = (unsigned)(((8 * kb + r) * LDW + 16 * nb + 8 * half) * 2);
    }
    const char* xsrc = (const char*)(X + (size_t)m0 * K);
    const char* wsrc = (const char*)(((wave & 2) ? W3 : W1) + n0);
    auto issue = [&](int buf) {
        const unsigned b = lds_base + buf * STB;
        glds16(xsrc, xoffs[0], b + wave * 1024);
        glds16(xsrc, xoffs[1], b + (wave + 8) * 1024);
        glds16(wsrc, woffs[0], b + 16384 + wave * 1024);
        glds16(wsrc, woffs[1], b + 16384 + (wave + 8) * 1024);
        xsrc += 64; wsrc += (size_t)32 * LDW * 2;
    };
    const int fr = lane & 15, g = lane >> 4, q = (lane & 15) >> 2, p = lane & 3;
    const int xo0 = (wm * 128 + fr) * 64 + ((g ^ ((4 - (fr >> 2)) & 3)) << 4);
    const int wo0 = 16384 + g * 4096 + (q + 4 * (g & 1)) * 32 + p * 8 + wn * 512;

    f32x4 acc[8][4];
#pragma unroll
    for (int i = 0; i < 8; ++i)
#pragma unroll
        for (int j = 0; j < 4; ++j) acc[i][j] = (f32x4){0.f, 0.f, 0.f, 0.f};
    f32x4 cs[4] = {};
    const bf16x8 ones = {0x3f80, 0x3f80, 0x3f80, 0x3f80, 0x3f80, 0x3f80, 0x3f80, 0x3f80};

    {
        const char* ssrc = (const char*)(stat_in + (size_t)m0 * 32);
#pragma unroll
        for (int j = 0; j < 4; ++j) glds16(ssrc, (unsigned)((wave * 4 + j) * 1024 + lane * 16), lds_base + NST * STB + (wave * 4 + j) * 1024);
    }
#pragma unroll
    for (int s = 0; s < NST; ++s) issue(s);

    auto read_frags = [&](LAS const char* base, bf16x8 (&xf)[8], bf16x8 (&wf)[4]) {
#pragma unroll
        for (int j = 0; j < 4; ++j) {
            const int o = (j & 1) * 256 + (j >> 1) * 2048;
            s16x4 lo = tr_read(base + wo0 + o);
            s16x4 hi = tr_read(base + (wo0 ^ 128) + o);
            wf[j] = (bf16x8){lo[0], lo[1], lo[2], lo[3], hi[0], hi[1], hi[2], hi[3]};
        }
#pragma unroll
        for (int i = 0; i < 8; ++i) xf[i] = *(LAS const bf16x8*)(base + xo0 + i * 1024);
    };
    auto do_mfma = [&](bf16x8 (&xf)[8], bf16x8 (&wf)[4]) {
#pragma unroll
        for (int i = 0; i < 8; ++i)
#pragma unroll
            for (int j = 0; j < 4; ++j)
                acc[i][j] = __builtin_amdgcn_mfma_f32_16x16x32_bf16(wf[j], xf[i], acc[i][j], 0, 0, 0);
    };
    auto interleave = [&]() {
#pragma unroll
        for (int i = 0; i < 16; ++i) {
            __builtin_amdgcn_sched_group_barrier(0x8, 2, 0);
            __builtin_amdgcn_sched_group_barrier(0x100, 1, 0);
        }
    };
    auto colsum = [&](bf16x8 (&wf)[4]) {
#pragma unroll
        for (int j = 0; j < 4; ++j) cs[j] = __builtin_amdgcn_mfma_f32_16x16x32_bf16(wf[j], ones, cs[j], 0, 0, 0);
    };
    bf16x8 xa[8], wa[4], xb[8], wb[4];
    wait_vm<(NST - 1) * 4>();
    __builtin_amdgcn_s_barrier();
    asm volatile("" ::: "memory");
    read_frags(lds, xa, wa);
    int buf = 0;
    auto step = [&](int kt, bf16x8 (&xc)[8], bf16x8 (&wc_)[4], bf16x8 (&xn)[8], bf16x8 (&wn_)[4], bool docs) {
        const int rem = NT - 2 - kt;
        if (rem >= NST - 2) wait_vm<(NST - 2) * 4>();
        else if (rem == 1) wait_vm<4>();
        else if (NST > 4 && rem == 2) wait_vm<8>();
        else wait_vm<0>();
        asm volatile("s_waitcnt lgkmcnt(0)" ::: "memory");
        __builtin_amdgcn_s_barrier();
        asm volatile("" ::: "memory");
        if (kt + NST < NT) issue(buf);
        buf = (buf + 1 == NST) ? 0 : buf + 1;
        __builtin_amdgcn_sched_barrier(0);
        read_frags(lds + buf * STB, xn, wn_);
        do_mfma(xc, wc_);
        interleave();
        __builtin_amdgcn_sched_barrier(0);
        if (docs) colsum(wc_);
        __builtin_amdgcn_sched_barrier(0);
    };
    for (int kt = 0; kt < NT; kt += 2) {
        step(kt, xa, wa, xb, wb, wm == 0);
        step(kt + 1, xb, wb, xa, wa, wm == 1);
    }
    asm volatile("" :: "v"(pf_dummy));
    __syncthreads();
    LAS float* rowstat = (LAS float*)lds;
    LAS float* colsm = (LAS float*)(lds + 2048);
    if (fr == 0) {
#pragma unroll
        for (int j = 0; j < 4; ++j) *(LAS f32x4*)(colsm + wm * 256 + (j >> 1) * 128 + wn * 32 + (j & 1) * 16 + 4 * g) = cs[j];
    }
    if (tid < 256) {
        LAS const f32x4* sp = (LAS const f32x4*)(lds + NST * STB + tid * 128);
        f32x4 t = sp[tid & 7];
#pragma unroll
        for (int e = 1; e < 8; ++e) t += sp[e ^ (tid & 7)];
        const float mean = (t[0] + t[2]) * (1.0f / D_MODEL);
        const float rstd = 1.0f / sqrtf(fmaxf((t[1] + t[3]) * (1.0f / D_MODEL) - mean * mean, 0.f) + 1e-5f);
        rowstat[2 * tid] = mean; rowstat[2 * tid + 1] = rstd;
    }
    __syncthreads();
    f32x4 csv[4];
#pragma unroll
    for (int j = 0; j < 4; ++j) {
        const int c = (j >> 1) * 128 + wn * 32 + (j & 1) * 16 + 4 * g;
        csv[j] = *(LAS const f32x4*)(colsm + c) + *(LAS const f32x4*)(colsm + 256 + c);
    }
#pragma unroll
    for (int i = 0; i < 8; ++i) {
        const int rl = wm * 128 + i * 16 + fr;
        const float mean = rowstat[2 * rl], rstd = rowstat[2 * rl + 1];
#pragma unroll
        for (int n = 0; n < 2; ++n) {
            const f32x4 gv = (acc[i][n] - csv[n] * mean) * rstd;
            const f32x4 uv = (acc[i][n + 2] - csv[n + 2] * mean) * rstd;
            float o[4];
#pragma unroll
            for (int r = 0; r < 4; ++r) o[r] = gv[r] * __builtin_amdgcn_rcpf(1.0f + __expf(-gv[r])) * uv[r];
            u32x2 w; w[0] = pk_bf16(o[0], o[1]); w[1] = pk_bf16(o[2], o[3]);
            *(u32x2*)(C + (size_t)(m0 + rl) * LDW + n0 + wn * 32 + n * 16 + 4 * g) = w;
        }
    }
}

__global__ __launch_bounds__(512) void attn_kernel(const bf16_t* __restrict__ qkv, bf16_t* __restrict__ O,
        const bf16_t* __restrict__ pf0, const bf16_t* __restrict__ pf1, const bf16_t* __restrict__ pf2, const bf16_t* __restrict__ pf3) {
    extern __shared__ __attribute__((aligned(16))) char smem[];
    LAS char* lds = (LAS char*)smem;
    LAS char* ldsK = lds;
    LAS char* ldsV = lds + 32768;
    const int tid = threadIdx.x, lane = tid & 63;
    const int w = __builtin_amdgcn_readfirstlane(tid >> 6);
    const int h = blockIdx.x & 31, qb = blockIdx.x >> 5;
    const int q0 = qb * 256, kbase = q0 - 256;
    constexpr int LD = 3 * D_MODEL;

    {
        u32x4 kv[4], vv[4];
#pragma unroll
        for (int i = 0; i < 4; ++i) {
            const int idx = tid + 512 * i, key = idx >> 2, ch = idx & 3;
            const int gk = kbase + key;
            if (gk >= 0) {
                const bf16_t* src = qkv + (size_t)gk * LD + h * 32 + ch * 8;
                kv[i] = *(const u32x4*)(src + D_MODEL);
                vv[i] = *(const u32x4*)(src + 2 * D_MODEL);
            }
        }
#pragma unroll
        for (int i = 0; i < 4; ++i) {
            const int idx = tid + 512 * i, key = idx >> 2, ch = idx & 3;
            const int gk = kbase + key;
            if (gk >= 0) {
                *(LAS u32x4*)(ldsK + key * 64 + ((ch ^ ((key >> 2) & 3)) << 4)) = kv[i];
                *(LAS u32x4*)(ldsV + key * 64 + (ch << 4)) = vv[i];
            }
        }
    }
    const int r32 = lane & 31, hi = lane >> 5;
    const int qw = q0 + 32 * w;
    bf16x8 qf[2];
#pragma unroll
    for (int s = 0; s < 2; ++s)
        qf[s] = *(const bf16x8*)(qkv + (size_t)(qw + r32) * LD + h * 32 + 16 * s + 8 * hi);
    __syncthreads();
    u32x4 pf_dummy = {0u, 0u, 0u, 0u};
    {
        const size_t gt = (size_t)(blockIdx.x * 512 + tid) * 16;
        const char* a0 = (const char*)pf0 + gt;
        asm volatile("global_load_dwordx4 %0, %1, off" : "+v"(pf_dummy) : "v"(a0) : "memory");
    }

    f32x16 sc[9];
    const float NEG = -INFINITY;
#pragma unroll
    for (int j = 0; j < 9; ++j) {
        const int kb = qw - 256 + 32 * j;
        if (kb >= 0) {
            const int key = 32 * (w + j) + r32;
            f32x16 a = {};
#pragma unroll
            for (int s = 0; s < 2; ++s) {
                const bf16x8 kf = *(LAS const bf16x8*)(ldsK + key * 64 + (((2 * s + hi) ^ ((key >> 2) & 3)) << 4));
                a = __builtin_amdgcn_mfma_f32_32x32x16_bf16(kf, qf[s], a, 0, 0, 0);
            }
            sc[j] = a;
        } else {
#pragma unroll
            for (int r = 0; r < 16; ++r) sc[j][r] = NEG;
        }
    }
#pragma unroll
    for (int r = 0; r < 16; ++r) {
        const int i = (r & 3) + 8 * (r >> 2) + 4 * hi;
        if (!(i > r32)) sc[0][r] = NEG;
        if (!(i <= r32)) sc[8][r] = NEG;
    }
    float mx = NEG;
#pragma unroll
    for (int j = 0; j < 9; ++j)
#pragma unroll
        for (int r = 0; r < 16; ++r) mx = fmaxf(mx, sc[j][r]);
    mx = fmaxf(mx, __shfl_xor(mx, 32));
    const float c = 0.17677669529663687f * 1.4426950408889634f;
    const float nm = -mx * c;
    float l = 0.f;
#pragma unroll
    for (int j = 0; j < 9; ++j)
#pragma unroll
        for (int r = 0; r < 16; ++r) {
            const float e = __builtin_amdgcn_exp2f(__builtin_fmaf(sc[j][r], c, nm));
            sc[j][r] = e; l += e;
        }
    l += __shfl_xor(l, 32);

    f32x16 o = {};
    const int gq = (lane & 15) >> 2, gp = lane & 3, gd = (lane >> 4) & 1;
#pragma unroll
    for (int j = 0; j < 9; ++j) {
        const int kb = qw - 256 + 32 * j;
        if (kb >= 0) {
#pragma unroll
            for (int s = 0; s < 2; ++s) {
                u32x4 pw;
#pragma unroll
                for (int e = 0; e < 4; ++e) pw[e] = pk_bf16(sc[j][8 * s + 2 * e], sc[j][8 * s + 2 * e + 1]);
                const int key = 32 * (w + j) + 16 * s + 4 * hi + gq;
                LAS const char* vp = ldsV + key * 64 + (16 * gd + 4 * gp) * 2;
                s16x4 lo = tr_read(vp), hi8 = tr_read(vp + 8 * 64);
                const bf16x8 vf = (bf16x8){lo[0], lo[1], lo[2], lo[3], hi8[0], hi8[1], hi8[2], hi8[3]};
                o = __builtin_amdgcn_mfma_f32_32x32x16_bf16(vf, __builtin_bit_cast(bf16x8, pw), o, 0, 0, 0);
            }
        }
    }
    const float rl = 1.0f / l;
    bf16_t* dst = O + (size_t)(qw + r32) * D_MODEL + h * 32 + 4 * hi;
#pragma unroll
    for (int t = 0; t < 4; ++t) {
        u32x2 wv; wv[0] = pk_bf16(o[4 * t] * rl, o[4 * t + 1] * rl); wv[1] = pk_bf16(o[4 * t + 2] * rl, o[4 * t + 3] * rl);
        *(u32x2*)(dst + 8 * t) = wv;
    }
    asm volatile("s_waitcnt vmcnt(0)" :: "v"(pf_dummy) : "memory");
}

template <typename KF> static void set_lds(KF kf, int bytes) {
    (void)hipFuncSetAttribute((const void*)kf, hipFuncAttributeMaxDynamicSharedMemorySize, bytes);
}

extern "C" void kernel_launch(void* const* d_in, const int* in_sizes, int n_in,
                              void* d_out, int out_size, void* d_ws, size_t ws_size,
                              hipStream_t stream) {
    const bf16_t* x = (const bf16_t*)d_in[0];
    const bf16_t* wqkv = (const bf16_t*)d_in[1];
    const bf16_t* wo = (const bf16_t*)d_in[2];
    const bf16_t* w1 = (const bf16_t*)d_in[4];
    const bf16_t* w3 = (const bf16_t*)d_in[5];
    const bf16_t* w2 = (const bf16_t*)d_in[6];
    bf16_t* out = (bf16_t*)d_out;
    char* ws = (char*)d_ws;
    bf16_t* qkv = (bf16_t*)(ws + (4u << 20));
    bf16_t* att = (bf16_t*)(ws + (16u << 20));
    bf16_t* hbuf = (bf16_t*)(ws + (20u << 20));
    bf16_t* act = (bf16_t*)(ws + (28u << 20));
    float* hstat = (float*)(ws + (24u << 20));

    {
        auto kf = gemm_kernel<128, 192, 2, 4, 4, EPI_STORE, 1, 2, 4>;
        constexpr int lds = 4 * (128 + 192) * 128;
        set_lds(kf, lds);
        kf<<<256, 512, lds, stream>>>(x, wqkv, nullptr, qkv, nullptr, S_LEN, 3072, 1024, 3072, 3072);
    }
    {
        set_lds(attn_kernel, 65536);
        attn_kernel<<<256, 512, 65536, stream>>>(qkv, att, wo, w1, w3, w2);
    }
    {
        auto kf = gemm_kernel<128, 64, 4, 2, 6, EPI_RES_STAT, 0, 4, 2>;
        constexpr int lds = 6 * (128 + 64) * 128;
        set_lds(kf, lds);
        kf<<<256, 512, lds, stream>>>(att, wo, x, hbuf, hstat, S_LEN, 1024, 1024, 1024, 1024);
    }
    {
        set_lds(glu_gemm_kernel, 163840);
        glu_gemm_kernel<<<256, 512, 163840, stream>>>(hbuf, w1, w3, hstat, act, w2);
    }
    {
        auto kf = gemm_kernel<128, 64, 4, 2, 6, EPI_RES, 0, 4, 2>;
        constexpr int lds = 6 * (128 + 64) * 128;
        set_lds(kf, lds);
        kf<<<256, 512, lds, stream>>>(act, w2, hbuf, out, nullptr, S_LEN, 1024, F_DIM, 1024, 1024);
    }
}
```

```cpp
#include <hip/hip_runtime.h>
#include <hip/hip_bf16.h>
#include <stdint.h>

typedef unsigned short bf16_t;
typedef short bf16x8 __attribute__((ext_vector_type(8)));
typedef short s16x4 __attribute__((ext_vector_type(4)));
typedef float f32x4 __attribute__((ext_vector_type(4)));
typedef float f32x16 __attribute__((ext_vector_type(16)));
typedef unsigned u32x4 __attribute__((ext_vector_type(4)));
typedef unsigned u32x2 __attribute__((ext_vector_type(2)));
#define LAS __attribute__((address_space(3)))

constexpr int S_LEN = 2048, D_MODEL = 1024, F_DIM = 3584, N_HEAD = 32, D_HEAD = 32, WIN = 256;

__device__ __forceinline__ float bf2f(unsigned v16) { return __uint_as_float(v16 << 16); }
__device__ __forceinline__ unsigned pk_bf16(float lo, float hi) {
    unsigned r; asm("v_cvt_pk_bf16_f32 %0, %1, %2" : "=v"(r) : "v"(lo), "v"(hi)); return r;
}
__device__ __forceinline__ s16x4 tr_read(LAS const char* p) {
    return __builtin_amdgcn_ds_read_tr16_b64_v4i16((LAS s16x4*)p);
}
__device__ __forceinline__ void glds16(const void* sbase, unsigned voff, unsigned lds_addr) {
    unsigned keep;
    asm volatile("s_mov_b32 %0, m0\n\ts_mov_b32 m0, %3\n\ts_nop 0\n\tglobal_load_lds_dwordx4 %2, %1\n\ts_mov_b32 m0, %0"
                 : "=&s"(keep) : "s"(sbase), "v"(voff), "s"(lds_addr) : "memory");
}
typedef __bf16 bf16x2_t __attribute__((ext_vector_type(2)));
__device__ __forceinline__ float dot2(unsigned a, unsigned b, float acc) {
    return __builtin_amdgcn_fdot2_f32_bf16(__builtin_bit_cast(bf16x2_t, a), __builtin_bit_cast(bf16x2_t, b), acc, false);
}
__device__ __forceinline__ void frag_sum(const bf16x8& f, float& s1) {
    const u32x4 u = __builtin_bit_cast(u32x4, f);
#pragma unroll
    for (int e = 0; e < 4; ++e) s1 = dot2(u[e], 0x3f803f80u, s1);
}
__device__ __forceinline__ void frag_sum_sq(const bf16x8& f, float& s1, float& s2) {
    const u32x4 u = __builtin_bit_cast(u32x4, f);
#pragma unroll
    for (int e = 0; e < 4; ++e) { s1 = dot2(u[e], 0x3f803f80u, s1); s2 = dot2(u[e], u[e], s2); }
}
__device__ __forceinline__ float red_g(float v) { v += __shfl_xor(v, 16); v += __shfl_xor(v, 32); return v; }
__device__ __forceinline__ void prefetch_region(const char* base, int lpr, long pitch, int chunk, int part, int tid,
                                                int nthreads, unsigned& dummy) {
    for (int l = tid; l < chunk; l += nthreads) {
        const int L = part * chunk + l, row = L / lpr, c = L - row * lpr;
        const char* a = base + (long)row * pitch + c * 128;
        asm volatile("global_load_dword %0, %1, off" : "+v"(dummy) : "v"(a) : "memory");
    }
}
#define ST8(ptr, v) (*(u32x2*)(ptr) = (v))
template <int N> __device__ __forceinline__ void wait_vm() { asm volatile("s_waitcnt vmcnt(%0)" ::"n"(N) : "memory"); }

enum { EPI_STORE = 0, EPI_RES = 1, EPI_RES_STAT = 2 };

template <int BM, int BN, int WM, int WN, int NST, int EPI, int LNF, int RM, int RN>
__global__ __launch_bounds__(WM * WN * 64) void gemm_kernel(
    const bf16_t* __restrict__ X, const bf16_t* __restrict__ W0,
    const bf16_t* __restrict__ R, bf16_t* __restrict__ C, float* __restrict__ stat_out,
    int M, int N, int K, int ldw, int ldc) {
    constexpr int NWAVE = WM * WN;
    constexpr int MF = BM / 16 / WM, NF = BN / 16 / WN, NB = BN / 16;
    constexpr int A_BYTES = BM * 128, B_BYTES = BN * 128, ST_BYTES = A_BYTES + B_BYTES;
    constexpr int LPW = (BM + BN) / 8 / NWAVE;
    constexpr int LPA = BM / 8 / NWAVE;
    static_assert((BM + BN) % (8 * NWAVE) == 0 && (BM % (8 * NWAVE)) == 0, "piece split");
    static_assert(NB % 4 == 0, "NB%4");
    static_assert(!LNF || (MF == WN && WM == 2), "LN fold layout");
    extern __shared__ __attribute__((aligned(16))) char smem[];
    LAS char* lds = (LAS char*)smem;

    const int tid = threadIdx.x, lane = tid & 63;
    const int wave = __builtin_amdgcn_readfirstlane(tid >> 6);
    const int wm = wave / WN, wn = wave % WN;

    const int tiles_m = M / BM, tiles_n = N / BN;
    int tm, tn;
    {
        const int xcd = blockIdx.x & 7, idx = blockIdx.x >> 3;
        const int rm = xcd / RN, rn = xcd % RN;
        const int sm = tiles_m / RM, sn = tiles_n / RN;
        tm = rm * sm + idx % sm; tn = rn * sn + idx / sm;
    }
    const int m0 = tm * BM, n0 = tn * BN;
    unsigned pf_dummy = 0;

    unsigned voff[LPW];
    const char* sbase[LPW];
#pragma unroll
    for (int j = 0; j < LPW; ++j) {
        const int ii = wave + NWAVE * j;
        if (j < LPA) {
            const int row = ii * 8 + (lane >> 3), ch = (lane & 7) ^ (row & 7);
            voff[j] = (unsigned)(row * K * 2 + ch * 16);
            sbase[j] = (const char*)(X + (size_t)m0 * K);
        } else {
            const int pi = ii - BM / 8;
            const int kb = pi / (NB / 4), nb = 4 * (pi % (NB / 4)) + (lane >> 4);
            const int r = ((lane & 15) >> 1) ^ (4 * (kb & 1)), half = lane & 1;
            voff[j] = (unsigned)(((8 * kb + r) * ldw + 16 * nb + 8 * half) * 2);
            sbase[j] = (const char*)(W0 + n0);
        }
    }
    const unsigned lds_base = (unsigned)(uintptr_t)lds;
    const int NT = K / 64;
    auto issue = [&](int buf) {
#pragma unroll
        for (int j = 0; j < LPW; ++j) {
            const int ii = wave + NWAVE * j;
            glds16(sbase[j], voff[j], lds_base + buf * ST_BYTES + ii * 1024);
            sbase[j] += (j < LPA) ? 128 : (long)128 * ldw;
        }
    };

    const int fr = lane & 15, g = lane >> 4, q = (lane & 15) >> 2, p = lane & 3;
    const int rotm = LNF ? wn : 0, rotk = LNF ? wm : 0;
    const int xrow = wm * (MF * 16) + fr;
    const int xoff0 = xrow * 128 + ((g ^ (xrow & 7)) << 4);
    const int woff0 = A_BYTES + g * NB * 256 + (q + 4 * (g & 1)) * 32 + p * 8 + wn * NF * 256;

    f32x4 acc[MF][NF];
#pragma unroll
    for (int i = 0; i < MF; ++i)
#pragma unroll
        for (int j = 0; j < NF; ++j) acc[i][j] = (f32x4){0.f, 0.f, 0.f, 0.f};
    f32x4 s1 = {0.f, 0.f, 0.f, 0.f}, s2 = {0.f, 0.f, 0.f, 0.f}, cs[NF];
#pragma unroll
    for (int j = 0; j < NF; ++j) cs[j] = (f32x4){0.f, 0.f, 0.f, 0.f};
    const bf16x8 ones = {0x3f80, 0x3f80, 0x3f80, 0x3f80, 0x3f80, 0x3f80, 0x3f80, 0x3f80};

#pragma unroll
    for (int s = 0; s < NST - 1; ++s) issue(s);

    auto read_frags = [&](LAS const char* base, int slot, bf16x8 (&xf)[MF], bf16x8 (&wf)[NF]) {
        const int ks = slot ^ rotk;
#pragma unroll
        for (int j = 0; j < NF; ++j) {
            const int o = ks * (4 * NB * 256) + j * 256;
            s16x4 lo = tr_read(base + woff0 + o);
            s16x4 hi = tr_read(base + (woff0 ^ 128) + o);
            wf[j] = (bf16x8){lo[0], lo[1], lo[2], lo[3], hi[0], hi[1], hi[2], hi[3]};
        }
#pragma unroll
        for (int i = 0; i < MF; ++i)
            xf[i] = *(LAS const bf16x8*)(base + ((xoff0 ^ (ks * 64)) + (i ^ rotm) * 2048));
    };
    auto do_mfma = [&](bf16x8 (&xf)[MF], bf16x8 (&wf)[NF], bool colsum) {
#pragma unroll
        for (int i = 0; i < MF; ++i)
#pragma unroll
            for (int j = 0; j < NF; ++j)
                acc[i][j] = __builtin_amdgcn_mfma_f32_16x16x32_bf16(wf[j], xf[i], acc[i][j], 0, 0, 0);
        if (LNF) {
            s1 = __builtin_amdgcn_mfma_f32_16x16x32_bf16(ones, xf[0], s1, 0, 0, 0);
            s2 = __builtin_amdgcn_mfma_f32_16x16x32_bf16(xf[0], xf[0], s2, 0, 0, 0);
            if (colsum) {
#pragma unroll
                for (int j = 0; j < NF; ++j) cs[j] = __builtin_amdgcn_mfma_f32_16x16x32_bf16(wf[j], ones, cs[j], 0, 0, 0);
            }
        }
    };
    auto interleave = [&]() {
        constexpr int NM = MF * NF, NR = MF + 2 * NF, RPM = (NR + NM - 1) / NM;
#pragma unroll
        for (int i = 0; i < NM; ++i) {
            __builtin_amdgcn_sched_group_barrier(0x8, 1, 0);
            __builtin_amdgcn_sched_group_barrier(0x100, RPM, 0);
        }
    };
    bf16x8 xa[MF], wa[NF], xb[MF], wb[NF];
    wait_vm<(NST - 2) * LPW>();
    __builtin_amdgcn_s_barrier();
    asm volatile("" ::: "memory");
    read_frags(lds, 0, xa, wa);

    int buf = 0, pbuf = NST - 1;
    for (int kt = 0; kt < NT; ++kt) {
        const int rem = NT - 2 - kt;
        if (rem >= NST - 3) wait_vm<(NST - 3) * LPW>();
        else if (NST > 4 && rem == 1) wait_vm<LPW>();
        else if (NST > 5 && rem == 2) wait_vm<2 * LPW>();
        else wait_vm<0>();
        __builtin_amdgcn_s_barrier();
        asm volatile("" ::: "memory");
        if (kt + NST - 1 < NT) issue(pbuf);
        const int nbuf = (buf + 1 == NST) ? 0 : buf + 1;
        __builtin_amdgcn_sched_barrier(0);
        read_frags(lds + buf * ST_BYTES, 1, xb, wb);
        do_mfma(xa, wa, true);
        interleave();
        __builtin_amdgcn_sched_barrier(0);
        read_frags(lds + nbuf * ST_BYTES, 0, xa, wa);
        do_mfma(xb, wb, false);
        interleave();
        __builtin_amdgcn_sched_barrier(0);
        pbuf = buf; buf = nbuf;
    }

    asm volatile("" :: "v"(pf_dummy));
    constexpr int NTHR = NWAVE * 64, CPR = BN / 8, NCH = BM * CPR / NTHR;
    constexpr int RS = (EPI == EPI_STORE) ? BN * 2 + 16 : BN * 4 + 16;
    LAS float* rowstat = (LAS float*)(lds + BM * RS);
    LAS float* colsum = rowstat + BM * 2;
    u32x4 rres[NCH];
    if (EPI != EPI_STORE) {
#pragma unroll
        for (int r = 0; r < NCH; ++r) {
            const int c = tid + NTHR * r, row = c / CPR, c8 = c % CPR;
            rres[r] = *(const u32x4*)(R + (size_t)(m0 + row) * ldc + n0 + c8 * 8);
        }
    }
    __syncthreads();
    if (LNF) {
        if (g == (fr >> 2)) {
            const int r = wm * (MF * 16) + wn * 16 + fr;
            const int d = fr & 3;
            rowstat[2 * r] = s1[0];
            rowstat[2 * r + 1] = d == 0 ? s2[0] : d == 1 ? s2[1] : d == 2 ? s2[2] : s2[3];
        }
        if (fr == 0) {
#pragma unroll
            for (int j = 0; j < NF; ++j) *(LAS f32x4*)(colsum + wm * BN + (wn * NF + j) * 16 + 4 * g) = cs[j];
        }
        __syncthreads();
    }
    f32x4 csv[NF];
    if (LNF) {
#pragma unroll
        for (int j = 0; j < NF; ++j) {
            const int c = (wn * NF + j) * 16 + 4 * g;
            csv[j] = *(LAS const f32x4*)(colsum + c) + *(LAS const f32x4*)(colsum + BN + c);
        }
    }
#pragma unroll
    for (int i = 0; i < MF; ++i) {
        const int rl = wm * (MF * 16) + (i ^ rotm) * 16 + fr;
        float mean = 0.f, rstd = 1.f;
        if (LNF) {
            const float a = rowstat[2 * rl], b = rowstat[2 * rl + 1];
            mean = a * (1.0f / D_MODEL);
            const float var = fmaxf(b * (1.0f / D_MODEL) - mean * mean, 0.f);
            rstd = 1.0f / sqrtf(var + 1e-5f);
        }
#pragma unroll
        for (int j = 0; j < NF; ++j) {
            const int nl = (wn * NF + j) * 16 + 4 * g;
            f32x4 v = acc[i][j];
            if (LNF) v = (v - csv[j] * mean) * rstd;
            if (EPI == EPI_STORE) {
                u32x2 w; w[0] = pk_bf16(v[0], v[1]); w[1] = pk_bf16(v[2], v[3]);
                *(LAS u32x2*)(lds + rl * RS + nl * 2) = w;
            } else {
                *(LAS f32x4*)(lds + rl * RS + nl * 4) = v;
            }
        }
    }
    __syncthreads();
#pragma unroll
    for (int r = 0; r < NCH; ++r) {
        const int c = tid + NTHR * r, row = c / CPR, c8 = c % CPR;
        u32x4 w;
        if (EPI == EPI_STORE) {
            w = *(LAS const u32x4*)(lds + row * RS + c8 * 16);
        } else {
            f32x4 v0 = *(LAS const f32x4*)(lds + row * RS + c8 * 32), v1 = *(LAS const f32x4*)(lds + row * RS + c8 * 32 + 16);
            const u32x4 rv = rres[r];
            v0[0] += bf2f(rv[0] & 0xffffu); v0[1] += bf2f(rv[0] >> 16); v0[2] += bf2f(rv[1] & 0xffffu); v0[3] += bf2f(rv[1] >> 16);
            v1[0] += bf2f(rv[2] & 0xffffu); v1[1] += bf2f(rv[2] >> 16); v1[2] += bf2f(rv[3] & 0xffffu); v1[3] += bf2f(rv[3] >> 16);
            w[0] = pk_bf16(v0[0], v0[1]); w[1] = pk_bf16(v0[2], v0[3]); w[2] = pk_bf16(v1[0], v1[1]); w[3] = pk_bf16(v1[2], v1[3]);
        }
        *(u32x4*)(C + (size_t)(m0 + row) * ldc + n0 + c8 * 8) = w;
        if (EPI == EPI_RES_STAT) {
            float a = 0.f, b = 0.f;
#pragma unroll
            for (int e = 0; e < 4; ++e) {
                const float x0 = bf2f(w[e] & 0xffffu), x1 = bf2f(w[e] >> 16);
                a += x0 + x1; b += x0 * x0 + x1 * x1;
            }
#pragma unroll
            for (int o = 1; o < CPR; o <<= 1) { a += __shfl_xor(a, o); b += __shfl_xor(b, o); }
            if (c8 == 0) { float2 o2; o2.x = a; o2.y = b; *(float2*)(stat_out + ((size_t)(m0 + row) * tiles_n + tn) * 2) = o2; }
        }
    }
}

__global__ __launch_bounds__(512, 2) void glu_gemm_kernel(
    const bf16_t* __restrict__ X, const bf16_t* __restrict__ W1, const bf16_t* __restrict__ W3,
    const float* __restrict__ stat_in, bf16_t* __restrict__ C, const bf16_t* __restrict__ pfw) {
    constexpr int K = D_MODEL, LDW = F_DIM, HTB = 16384;
    if (blockIdx.x >= 224) {
        u32x4 d = {0u, 0u, 0u, 0u};
        const char* a = (const char*)pfw + (size_t)(blockIdx.x - 224) * 229376 + threadIdx.x * 16;
#pragma unroll
        for (int r = 0; r < 28; ++r) asm volatile("global_load_dwordx4 %0, %1, off offset:0" : "+v"(d) : "v"(a + r * 8192) : "memory");
        asm volatile("s_waitcnt vmcnt(0)" :: "v"(d) : "memory");
        return;
    }
    extern __shared__ __attribute__((aligned(16))) char smem[];
    LAS char* lds = (LAS char*)smem;
    const unsigned lds_base = (unsigned)(uintptr_t)lds;
    const int tid = threadIdx.x, lane = tid & 63;
    const int wave = __builtin_amdgcn_readfirstlane(tid >> 6);
    const int wr = wave >> 2, wc = wave & 3;
    int tm, tn;
    { const int xcd = blockIdx.x & 7, idx = blockIdx.x >> 3; tm = (xcd >> 2) * 4 + (idx & 3); tn = (xcd & 3) * 7 + (idx >> 2); }
    const int m0 = tm * 256, n0 = tn * 128;
    unsigned pf_dummy = 0;

    unsigned xoffs[2], woffs[2];
#pragma unroll
    for (int j = 0; j < 2; ++j) {
        const int pc = wave + 8 * j;
        const int row = pc * 8 + (lane >> 3), ch = (lane & 7) ^ (row & 7);
        xoffs[j] = (unsigned)(row * K * 2 + ch * 16);
        const int kb = pc >> 1, nb = 4 * (pc & 1) + (lane >> 4);
        const int r = ((lane & 15) >> 1) ^ (4 * (kb & 1)), half = lane & 1;
        woffs[j] = (unsigned)(((8 * kb + r) * LDW + 16 * nb + 8 * half) * 2);
    }
    const char* xsrc = (const char*)(X + (size_t)m0 * K);
    const char* w1src = (const char*)(W1 + n0);
    const char* w3src = (const char*)(W3 + n0);
#define SA_OFF(b, h) (((b) * 2 + (h)) * HTB)
#define SB_OFF(b, h) ((4 + (b) * 2 + (h)) * HTB)
#define STAGE_A(b, h, kt) do { const char* _s = xsrc + (size_t)(h) * 128 * K * 2 + (kt) * 128; \
        glds16(_s, xoffs[0], lds_base + SA_OFF(b, h) + wave * 1024); glds16(_s, xoffs[1], lds_base + SA_OFF(b, h) + (wave + 8) * 1024); } while (0)
#define STAGE_B(b, h, kt) do { const char* _s = ((h) ? w3src : w1src) + (size_t)(kt) * 64 * LDW * 2; \
        glds16(_s, woffs[0], lds_base + SB_OFF(b, h) + wave * 1024); glds16(_s, woffs[1], lds_base + SB_OFF(b, h) + (wave + 8) * 1024); } while (0)
    const int fr = lane & 15, g = lane >> 4, q = (lane & 15) >> 2, p = lane & 3;
    const int xrow = wr * 64 + fr;
    const int xo0 = (xrow * 128 + ((g ^ (xrow & 7)) << 4)) ^ (wr * 64);
    const int wo0 = g * 2048 + (q + 4 * (g & 1)) * 32 + p * 8 + wc * 512;
    unsigned xb_[4][2], wb_[2][2];
#pragma unroll
    for (int m = 0; m < 4; ++m)
#pragma unroll
        for (int k = 0; k < 2; ++k) { xb_[m][k] = lds_base + ((xo0 ^ (k * 64)) + m * 2048); asm volatile("" : "+v"(xb_[m][k])); }
#pragma unroll
    for (int t = 0; t < 2; ++t)
#pragma unroll
        for (int k = 0; k < 2; ++k) { wb_[t][k] = lds_base + 4 * HTB + ((wo0 ^ (t * 128)) + (k ^ wr) * 8192); asm volatile("" : "+v"(wb_[t][k])); }
#define LDSP(a) ((LAS const char*)(uintptr_t)(a))
#define LDA(dst, b, h) do { _Pragma("unroll") for (int m = 0; m < 4; ++m) _Pragma("unroll") for (int k = 0; k < 2; ++k) \
        dst[m][k] = *(LAS const bf16x8*)(LDSP(xb_[m][k]) + SA_OFF(b, h)); } while (0)
#define LDB(dst, b, h) do { _Pragma("unroll") for (int n = 0; n < 2; ++n) _Pragma("unroll") for (int k = 0; k < 2; ++k) { \
        s16x4 lo_ = tr_read(LDSP(wb_[0][k]) + SA_OFF(b, h) + n * 256); \
        s16x4 hi_ = tr_read(LDSP(wb_[1][k]) + SA_OFF(b, h) + n * 256); \
        dst[n][k] = (bf16x8){lo_[0], lo_[1], lo_[2], lo_[3], hi_[0], hi_[1], hi_[2], hi_[3]}; } } while (0)
#define MMA(ai, bj, At, Bt, CS) do { __builtin_amdgcn_s_setprio(1); \
        _Pragma("unroll") for (int m = 0; m < 4; ++m) _Pragma("unroll") for (int n = 0; n < 2; ++n) _Pragma("unroll") for (int k = 0; k < 2; ++k) \
            acc[ai][bj][m][n] = __builtin_amdgcn_mfma_f32_16x16x32_bf16(Bt[n][k], At[m][k], acc[ai][bj][m][n], 0, 0, 0); \
        if ((CS) & 2) { cs[bj][0] = __builtin_amdgcn_mfma_f32_16x16x32_bf16(Bt[0][0], ones, cs[bj][0], 0, 0, 0); \
                        cs[bj][1] = __builtin_amdgcn_mfma_f32_16x16x32_bf16(Bt[1][0], ones, cs[bj][1], 0, 0, 0); } \
        __builtin_amdgcn_s_setprio(0); } while (0)
#define WAIT_V(n) asm volatile("s_waitcnt vmcnt(" #n ")" ::: "memory")
#define WAIT_L(n) asm volatile("s_waitcnt lgkmcnt(" #n ")" ::: "memory")
#define BAR __builtin_amdgcn_s_barrier()
#define SCHED __builtin_amdgcn_sched_barrier(0)
    f32x4 acc[2][2][4][2] = {};
    bf16x8 At[4][2], B0[2][2], B1[2][2];
    f32x4 cs[2][2] = {};
    const bf16x8 ones = {0x3f80, 0x3f80, 0x3f80, 0x3f80, 0x3f80, 0x3f80, 0x3f80, 0x3f80};
    constexpr int nt = K / 64;
    {
        const char* ssrc = (const char*)(stat_in + (size_t)m0 * 32);
#pragma unroll
        for (int j = 0; j < 4; ++j) glds16(ssrc, (unsigned)((wave * 4 + j) * 1024 + lane * 16), lds_base + 8 * HTB + (wave * 4 + j) * 1024);
    }
    STAGE_B(0, 0, 0); STAGE_A(0, 0, 0); STAGE_B(0, 1, 0); STAGE_A(0, 1, 0);
    if (wr == 1) BAR;
    WAIT_V(4); BAR;
    STAGE_B(1, 0, 1); STAGE_A(1, 0, 1); STAGE_B(1, 1, 1);
    WAIT_V(6); BAR;
    for (int t = 0; t < nt; t += 2) {
        const bool more = t + 2 < nt;
        LDB(B0, 0, 0); SCHED; LDA(At, 0, 0); STAGE_A(1, 1, t + 1);
        WAIT_L(8); BAR; WAIT_L(0); MMA(0, 0, At, B0, 2); BAR; SCHED;
        LDB(B1, 0, 1); if (more) STAGE_B(0, 0, t + 2);
        BAR; WAIT_L(0); MMA(0, 1, At, B1, 1); BAR;
        LDA(At, 0, 1); if (more) STAGE_A(0, 0, t + 2);
        BAR; WAIT_L(0); MMA(1, 0, At, B0, 1); BAR; SCHED;
        if (more) { STAGE_B(0, 1, t + 2); WAIT_V(6); } else { WAIT_V(0); }
        BAR; MMA(1, 1, At, B1, 2); BAR;
        LDB(B0, 1, 0); SCHED; LDA(At, 1, 0); if (more) STAGE_A(0, 1, t + 2);
        WAIT_L(8); BAR; WAIT_L(0); MMA(0, 0, At, B0, 2); BAR; SCHED;
        LDB(B1, 1, 1); if (more) STAGE_B(1, 0, t + 3);
        BAR; WAIT_L(0); MMA(0, 1, At, B1, 1); BAR;
        LDA(At, 1, 1); if (more) STAGE_A(1, 0, t + 3);
        BAR; WAIT_L(0); MMA(1, 0, At, B0, 1); BAR; SCHED;
        if (more) { STAGE_B(1, 1, t + 3); WAIT_V(6); }
        BAR; MMA(1, 1, At, B1, 2); BAR;
    }
    if (wr == 0) BAR;
    asm volatile("" :: "v"(pf_dummy));
    constexpr int RS = 272;
    LAS float* rowstat = (LAS float*)(lds + 256 * RS);
    LAS float* colsum = rowstat + 512;
    if (fr == 0) {
#pragma unroll
        for (int b = 0; b < 2; ++b)
#pragma unroll
            for (int n = 0; n < 2; ++n) *(LAS f32x4*)(colsum + wr * 256 + b * 128 + wc * 32 + n * 16 + 4 * g) = cs[b][n];
    }
    if (tid < 256) {
        LAS const f32x4* sp = (LAS const f32x4*)(lds + 8 * HTB + tid * 128);
        f32x4 t = sp[tid & 7];
#pragma unroll
        for (int e = 1; e < 8; ++e) t += sp[e ^ (tid & 7)];
        const float mean = (t[0] + t[2]) * (1.0f / D_MODEL);
        const float rstd = 1.0f / sqrtf(fmaxf((t[1] + t[3]) * (1.0f / D_MODEL) - mean * mean, 0.f) + 1e-5f);
        rowstat[2 * tid] = mean * rstd; rowstat[2 * tid + 1] = rstd;
    }
    __syncthreads();
    f32x4 csv[2][2];
#pragma unroll
    for (int b = 0; b < 2; ++b)
#pragma unroll
        for (int n = 0; n < 2; ++n) {
            const int c = b * 128 + wc * 32 + n * 16 + 4 * g;
            csv[b][n] = *(LAS const f32x4*)(colsum + c) + *(LAS const f32x4*)(colsum + 256 + c);
        }
#pragma unroll
    for (int ai = 0; ai < 2; ++ai)
#pragma unroll
        for (int m = 0; m < 4; ++m) {
            const int rl = ai * 128 + wr * 64 + m * 16 + fr;
            const float mr = rowstat[2 * rl], rstd = rowstat[2 * rl + 1];
#pragma unroll
            for (int n = 0; n < 2; ++n) {
                const f32x4 gv = acc[ai][0][m][n] * rstd - csv[0][n] * mr;
                const f32x4 uv = acc[ai][1][m][n] * rstd - csv[1][n] * mr;
                float o[4];
#pragma unroll
                for (int r = 0; r < 4; ++r) o[r] = gv[r] * __builtin_amdgcn_rcpf(1.0f + __expf(-gv[r])) * uv[r];
                u32x2 w; w[0] = pk_bf16(o[0], o[1]); w[1] = pk_bf16(o[2], o[3]);
                *(LAS u32x2*)(lds + rl * RS + (wc * 32 + n * 16 + 4 * g) * 2) = w;
            }
        }
    __syncthreads();
#pragma unroll
    for (int r = 0; r < 8; ++r) {
        const int c = tid + 512 * r, row = c >> 4, c8 = c & 15;
        *(u32x4*)(C + (size_t)(m0 + row) * LDW + n0 + c8 * 8) = *(LAS const u32x4*)(lds + row * RS + c8 * 16);
    }
#undef SA_OFF
#undef SB_OFF
#undef STAGE_A
#undef STAGE_B
#undef LDA
#undef LDB
#undef MMA
}

template <int NST>
__global__ __launch_bounds__(512, 2) void glu2_kernel(
    const bf16_t* __restrict__ X, const bf16_t* __restrict__ W1, const bf16_t* __restrict__ W3,
    const float* __restrict__ stat_in, bf16_t* __restrict__ C) {
    constexpr int K = D_MODEL, LDW = F_DIM, STB = 32768, NT = K / 32;
    extern __shared__ __attribute__((aligned(16))) char smem[];
    LAS char* lds = (LAS char*)smem;
    const unsigned lds_base = (unsigned)(uintptr_t)lds;
    const int tid = threadIdx.x, lane = tid & 63;
    const int wave = __builtin_amdgcn_readfirstlane(tid >> 6);
    const int wm = wave >> 2, wn = wave & 3;
    int tm, tn;
    { const int xcd = blockIdx.x & 7, idx = blockIdx.x >> 3; tm = (xcd >> 2) * 4 + (idx & 3); tn = (xcd & 3) * 7 + (idx >> 2); }
    const int m0 = tm * 256, n0 = tn * 128;
    unsigned pf_dummy = 0;

    unsigned xoffs[2], woffs[2];
#pragma unroll
    for (int j = 0; j < 2; ++j) {
        const int pc = wave + 8 * j;
        const int row = pc * 16 + (lane >> 2), ch = (lane & 3) ^ ((4 - ((row >> 2) & 3)) & 3);
        xoffs[j] = (unsigned)(row * K * 2 + ch * 16);
        const int kb = pc >> 2, nb = 4 * (pc & 1) + (lane >> 4);
        const int r = ((lane & 15) >> 1) ^ (4 * (kb & 1)), half = lane & 1;
        woffs[j] = (unsigned)(((8 * kb + r) * LDW + 16 * nb + 8 * half) * 2);
    }
    const char* xsrc = (const char*)(X + (size_t)m0 * K);
    const char* wsrc = (const char*)(((wave & 2) ? W3 : W1) + n0);
    auto issue = [&](int buf) {
        const unsigned b = lds_base + buf * STB;
        glds16(xsrc, xoffs[0], b + wave * 1024);
        glds16(xsrc, xoffs[1], b + (wave + 8) * 1024);
        glds16(wsrc, woffs[0], b + 16384 + wave * 1024);
        glds16(wsrc, woffs[1], b + 16384 + (wave + 8) * 1024);
        xsrc += 64; wsrc += (size_t)32 * LDW * 2;
    };
    const int fr = lane & 15, g = lane >> 4, q = (lane & 15) >> 2, p = lane & 3;
    const int xo0 = (wm * 128 + fr) * 64 + ((g ^ ((4 - (fr >> 2)) & 3)) << 4);
    const int wo0 = 16384 + g * 4096 + (q + 4 * (g & 1)) * 32 + p * 8 + wn * 512;

    f32x4 acc[8][4];
#pragma unroll
    for (int i = 0; i < 8; ++i)
#pragma unroll
        for (int j = 0; j < 4; ++j) acc[i][j] = (f32x4){0.f, 0.f, 0.f, 0.f};
    f32x4 cs[4] = {};
    const bf16x8 ones = {0x3f80, 0x3f80, 0x3f80, 0x3f80, 0x3f80, 0x3f80, 0x3f80, 0x3f80};

    {
        const char* ssrc = (const char*)(stat_in + (size_t)m0 * 32);
#pragma unroll
        for (int j = 0; j < 4; ++j) glds16(ssrc, (unsigned)((wave * 4 + j) * 1024 + lane * 16), lds_base + NST * STB + (wave * 4 + j) * 1024);
    }
#pragma unroll
    for (int s = 0; s < NST; ++s) issue(s);

    auto read_frags = [&](LAS const char* base, bf16x8 (&xf)[8], bf16x8 (&wf)[4]) {
#pragma unroll
        for (int j = 0; j < 4; ++j) {
            const int o = (j & 1) * 256 + (j >> 1) * 2048;
            s16x4 lo = tr_read(base + wo0 + o);
            s16x4 hi = tr_read(base + (wo0 ^ 128) + o);
            wf[j] = (bf16x8){lo[0], lo[1], lo[2], lo[3], hi[0], hi[1], hi[2], hi[3]};
        }
#pragma unroll
        for (int i = 0; i < 8; ++i) xf[i] = *(LAS const bf16x8*)(base + xo0 + i * 1024);
    };
    auto do_mfma = [&](bf16x8 (&xf)[8], bf16x8 (&wf)[4]) {
#pragma unroll
        for (int i = 0; i < 8; ++i)
#pragma unroll
            for (int j = 0; j < 4; ++j)
                acc[i][j] = __builtin_amdgcn_mfma_f32_16x16x32_bf16(wf[j], xf[i], acc[i][j], 0, 0, 0);
    };
    auto interleave = [&]() {
#pragma unroll
        for (int i = 0; i < 16; ++i) {
            __builtin_amdgcn_sched_group_barrier(0x8, 2, 0);
            __builtin_amdgcn_sched_group_barrier(0x100, 1, 0);
        }
    };
    auto colsum = [&](bf16x8 (&wf)[4]) {
#pragma unroll
        for (int j = 0; j < 4; ++j) cs[j] = __builtin_amdgcn_mfma_f32_16x16x32_bf16(wf[j], ones, cs[j], 0, 0, 0);
    };
    bf16x8 xa[8], wa[4], xb[8], wb[4];
    wait_vm<(NST - 1) * 4>();
    __builtin_amdgcn_s_barrier();
    asm volatile("" ::: "memory");
    read_frags(lds, xa, wa);
    int buf = 0;
    auto step = [&](int kt, bf16x8 (&xc)[8], bf16x8 (&wc_)[4], bf16x8 (&xn)[8], bf16x8 (&wn_)[4], bool docs) {
        const int rem = NT - 2 - kt;
        if (rem >= NST - 2) wait_vm<(NST - 2) * 4>();
        else if (rem == 1) wait_vm<4>();
        else if (NST > 4 && rem == 2) wait_vm<8>();
        else wait_vm<0>();
        asm volatile("s_waitcnt lgkmcnt(0)" ::: "memory");
        __builtin_amdgcn_s_barrier();
        asm volatile("" ::: "memory");
        if (kt + NST < NT) issue(buf);
        buf = (buf + 1 == NST) ? 0 : buf + 1;
        __builtin_amdgcn_sched_barrier(0);
        read_frags(lds + buf * STB, xn, wn_);
        do_mfma(xc, wc_);
        interleave();
        __builtin_amdgcn_sched_barrier(0);
        if (docs) colsum(wc_);
        __builtin_amdgcn_sched_barrier(0);
    };
    for (int kt = 0; kt < NT; kt += 2) {
        step(kt, xa, wa, xb, wb, wm == 0);
        step(kt + 1, xb, wb, xa, wa, wm == 1);
    }
    asm volatile("" :: "v"(pf_dummy));
    __syncthreads();
    LAS float* rowstat = (LAS float*)lds;
    LAS float* colsm = (LAS float*)(lds + 2048);
    if (fr == 0) {
#pragma unroll
        for (int j = 0; j < 4; ++j) *(LAS f32x4*)(colsm + wm * 256 + (j >> 1) * 128 + wn * 32 + (j & 1) * 16 + 4 * g) = cs[j];
    }
    if (tid < 256) {
        LAS const f32x4* sp = (LAS const f32x4*)(lds + NST * STB + tid * 128);
        f32x4 t = sp[tid & 7];
#pragma unroll
        for (int e = 1; e < 8; ++e) t += sp[e ^ (tid & 7)];
        const float mean = (t[0] + t[2]) * (1.0f / D_MODEL);
        const float rstd = 1.0f / sqrtf(fmaxf((t[1] + t[3]) * (1.0f / D_MODEL) - mean * mean, 0.f) + 1e-5f);
        rowstat[2 * tid] = mean; rowstat[2 * tid + 1] = rstd;
    }
    __syncthreads();
    f32x4 csv[4];
#pragma unroll
    for (int j = 0; j < 4; ++j) {
        const int c = (j >> 1) * 128 + wn * 32 + (j & 1) * 16 + 4 * g;
        csv[j] = *(LAS const f32x4*)(colsm + c) + *(LAS const f32x4*)(colsm + 256 + c);
    }
#pragma unroll
    for (int i = 0; i < 8; ++i) {
        const int rl = wm * 128 + i * 16 + fr;
        const float mean = rowstat[2 * rl], rstd = rowstat[2 * rl + 1];
#pragma unroll
        for (int n = 0; n < 2; ++n) {
            const f32x4 gv = (acc[i][n] - csv[n] * mean) * rstd;
            const f32x4 uv = (acc[i][n + 2] - csv[n + 2] * mean) * rstd;
            float o[4];
#pragma unroll
            for (int r = 0; r < 4; ++r) o[r] = gv[r] * __builtin_amdgcn_rcpf(1.0f + __expf(-gv[r])) * uv[r];
            u32x2 w; w[0] = pk_bf16(o[0], o[1]); w[1] = pk_bf16(o[2], o[3]);
            *(u32x2*)(C + (size_t)(m0 + rl) * LDW + n0 + wn * 32 + n * 16 + 4 * g) = w;
        }
    }
}

__global__ __launch_bounds__(512) void attn_kernel(const bf16_t* __restrict__ qkv, bf16_t* __restrict__ O,
        const bf16_t* __restrict__ pf0, const bf16_t* __restrict__ pf1, const bf16_t* __restrict__ pf2, const bf16_t* __restrict__ pf3) {
    extern __shared__ __attribute__((aligned(16))) char smem[];
    LAS char* lds = (LAS char*)smem;
    LAS char* ldsK = lds;
    LAS char* ldsV = lds + 32768;
    const int tid = threadIdx.x, lane = tid & 63;
    const int w = __builtin_amdgcn_readfirstlane(tid >> 6);
    const int h = blockIdx.x & 31, qb = blockIdx.x >> 5;
    const int q0 = qb * 256, kbase = q0 - 256;
    constexpr int LD = 3 * D_MODEL;

    {
        u32x4 kv[4], vv[4];
#pragma unroll
        for (int i = 0; i < 4; ++i) {
            const int idx = tid + 512 * i, key = idx >> 2, ch = idx & 3;
            const int gk = kbase + key;
            if (gk >= 0) {
                const bf16_t* src = qkv + (size_t)gk * LD + h * 32 + ch * 8;
                kv[i] = *(const u32x4*)(src + D_MODEL);
                vv[i] = *(const u32x4*)(src + 2 * D_MODEL);
            }
        }
#pragma unroll
        for (int i = 0; i < 4; ++i) {
            const int idx = tid + 512 * i, key = idx >> 2, ch = idx & 3;
            const int gk = kbase + key;
            if (gk >= 0) {
                *(LAS u32x4*)(ldsK + key * 64 + ((ch ^ ((key >> 2) & 3)) << 4)) = kv[i];
                *(LAS u32x4*)(ldsV + key * 64 + (ch << 4)) = vv[i];
            }
        }
    }
    const int r32 = lane & 31, hi = lane >> 5;
    const int qw = q0 + 32 * w;
    bf16x8 qf[2];
#pragma unroll
    for (int s = 0; s < 2; ++s)
        qf[s] = *(const bf16x8*)(qkv + (size_t)(qw + r32) * LD + h * 32 + 16 * s + 8 * hi);
    __syncthreads();
    u32x4 pf_dummy = {0u, 0u, 0u, 0u};
    {
        const size_t gt = (size_t)(blockIdx.x * 512 + tid) * 16;
        const char* a0 = (const char*)pf0 + gt;
        asm volatile("global_load_dwordx4 %0, %1, off" : "+v"(pf_dummy) : "v"(a0) : "memory");
    }

    f32x16 sc[9];
    const float NEG = -INFINITY;
#pragma unroll
    for (int j = 0; j < 9; ++j) {
        const int kb = qw - 256 + 32 * j;
        if (kb >= 0) {
            const int key = 32 * (w + j) + r32;
            f32x16 a = {};
#pragma unroll
            for (int s = 0; s < 2; ++s) {
                const bf16x8 kf = *(LAS const bf16x8*)(ldsK + key * 64 + (((2 * s + hi) ^ ((key >> 2) & 3)) << 4));
                a = __builtin_amdgcn_mfma_f32_32x32x16_bf16(kf, qf[s], a, 0, 0, 0);
            }
            sc[j] = a;
        } else {
#pragma unroll
            for (int r = 0; r < 16; ++r) sc[j][r] = NEG;
        }
    }
#pragma unroll
    for (int r = 0; r < 16; ++r) {
        const int i = (r & 3) + 8 * (r >> 2) + 4 * hi;
        if (!(i > r32)) sc[0][r] = NEG;
        if (!(i <= r32)) sc[8][r] = NEG;
    }
    float mx = NEG;
#pragma unroll
    for (int j = 0; j < 9; ++j)
#pragma unroll
        for (int r = 0; r < 16; ++r) mx = fmaxf(mx, sc[j][r]);
    mx = fmaxf(mx, __shfl_xor(mx, 32));
    const float c = 0.17677669529663687f * 1.4426950408889634f;
    const float nm = -mx * c;
    float l = 0.f;
#pragma unroll
    for (int j = 0; j < 9; ++j)
#pragma unroll
        for (int r = 0; r < 16; ++r) {
            const float e = __builtin_amdgcn_exp2f(__builtin_fmaf(sc[j][r], c, nm));
            sc[j][r] = e; l += e;
        }
    l += __shfl_xor(l, 32);

    f32x16 o = {};
    const int gq = (lane & 15) >> 2, gp = lane & 3, gd = (lane >> 4) & 1;
#pragma unroll
    for (int j = 0; j < 9; ++j) {
        const int kb = qw - 256 + 32 * j;
        if (kb >= 0) {
#pragma unroll
            for (int s = 0; s < 2; ++s) {
                u32x4 pw;
#pragma unroll
                for (int e = 0; e < 4; ++e) pw[e] = pk_bf16(sc[j][8 * s + 2 * e], sc[j][8 * s + 2 * e + 1]);
                const int key = 32 * (w + j) + 16 * s + 4 * hi + gq;
                LAS const char* vp = ldsV + key * 64 + (16 * gd + 4 * gp) * 2;
                s16x4 lo = tr_read(vp), hi8 = tr_read(vp + 8 * 64);
                const bf16x8 vf = (bf16x8){lo[0], lo[1], lo[2], lo[3], hi8[0], hi8[1], hi8[2], hi8[3]};
                o = __builtin_amdgcn_mfma_f32_32x32x16_bf16(vf, __builtin_bit_cast(bf16x8, pw), o, 0, 0, 0);
            }
        }
    }
    const float rl = 1.0f / l;
    bf16_t* dst = O + (size_t)(qw + r32) * D_MODEL + h * 32 + 4 * hi;
#pragma unroll
    for (int t = 0; t < 4; ++t) {
        u32x2 wv; wv[0] = pk_bf16(o[4 * t] * rl, o[4 * t + 1] * rl); wv[1] = pk_bf16(o[4 * t + 2] * rl, o[4 * t + 3] * rl);
        ST8(dst + 8 * t, wv);
    }
    asm volatile("s_waitcnt vmcnt(0)" :: "v"(pf_dummy) : "memory");
}

template <typename KF> static void set_lds(KF kf, int bytes) {
    (void)hipFuncSetAttribute((const void*)kf, hipFuncAttributeMaxDynamicSharedMemorySize, bytes);
}

extern "C" void kernel_launch(void* const* d_in, const int* in_sizes, int n_in,
                              void* d_out, int out_size, void* d_ws, size_t ws_size,
                              hipStream_t stream) {
    const bf16_t* x = (const bf16_t*)d_in[0];
    const bf16_t* wqkv = (const bf16_t*)d_in[1];
    const bf16_t* wo = (const bf16_t*)d_in[2];
    const bf16_t* w1 = (const bf16_t*)d_in[4];
    const bf16_t* w3 = (const bf16_t*)d_in[5];
    const bf16_t* w2 = (const bf16_t*)d_in[6];
    bf16_t* out = (bf16_t*)d_out;
    char* ws = (char*)d_ws;
    bf16_t* qkv = (bf16_t*)(ws + (4u << 20));
    bf16_t* att = (bf16_t*)(ws + (16u << 20));
    bf16_t* hbuf = (bf16_t*)(ws + (20u << 20));
    bf16_t* act = (bf16_t*)(ws + (28u << 20));
    float* hstat = (float*)(ws + (24u << 20));

    {
        auto kf = gemm_kernel<128, 192, 2, 4, 4, EPI_STORE, 1, 2, 4>;
        constexpr int lds = 4 * (128 + 192) * 128;
        set_lds(kf, lds);
        kf<<<256, 512, lds, stream>>>(x, wqkv, nullptr, qkv, nullptr, S_LEN, 3072, 1024, 3072, 3072);
    }
    {
        set_lds(attn_kernel, 65536);
        attn_kernel<<<256, 512, 65536, stream>>>(qkv, att, wo, w1, w3, w2);
    }
    {
        auto kf = gemm_kernel<128, 64, 4, 2, 6, EPI_RES_STAT, 0, 4, 2>;
        constexpr int lds = 6 * (128 + 64) * 128;
        set_lds(kf, lds);
        kf<<<256, 512, lds, stream>>>(att, wo, x, hbuf, hstat, S_LEN, 1024, 1024, 1024, 1024);
    }
    {
        set_lds(glu_gemm_kernel, 163840);
        glu_gemm_kernel<<<256, 512, 163840, stream>>>(hbuf, w1, w3, hstat, act, w2);
    }
    {
        auto kf = gemm_kernel<128, 64, 4, 2, 6, EPI_RES, 0, 4, 2>;
        constexpr int lds = 6 * (128 + 64) * 128;
        set_lds(kf, lds);
        kf<<<256, 512, lds, stream>>>(act, w2, hbuf, out, nullptr, S_LEN, 1024, F_DIM, 1024, 1024);
    }
}
```

```cpp
#include <hip/hip_runtime.h>
#include <hip/hip_bf16.h>
#include <stdint.h>

typedef unsigned short bf16_t;
typedef short bf16x8 __attribute__((ext_vector_type(8)));
typedef short s16x4 __attribute__((ext_vector_type(4)));
typedef float f32x4 __attribute__((ext_vector_type(4)));
typedef float f32x16 __attribute__((ext_vector_type(16)));
typedef unsigned u32x4 __attribute__((ext_vector_type(4)));
typedef unsigned u32x2 __attribute__((ext_vector_type(2)));
#define LAS __attribute__((address_space(3)))

constexpr int S_LEN = 2048, D_MODEL = 1024, F_DIM = 3584, N_HEAD = 32, D_HEAD = 32, WIN = 256;

__device__ __forceinline__ float bf2f(unsigned v16) { return __uint_as_float(v16 << 16); }
__device__ __forceinline__ unsigned pk_bf16(float lo, float hi) {
    unsigned r; asm("v_cvt_pk_bf16_f32 %0, %1, %2" : "=v"(r) : "v"(lo), "v"(hi)); return r;
}
__device__ __forceinline__ s16x4 tr_read(LAS const char* p) {
    return __builtin_amdgcn_ds_read_tr16_b64_v4i16((LAS s16x4*)p);
}
__device__ __forceinline__ void glds16(const void* sbase, unsigned voff, unsigned lds_addr) {
    unsigned keep;
    asm volatile("s_mov_b32 %0, m0\n\ts_mov_b32 m0, %3\n\ts_nop 0\n\tglobal_load_lds_dwordx4 %2, %1\n\ts_mov_b32 m0, %0"
                 : "=&s"(keep) : "s"(sbase), "v"(voff), "s"(lds_addr) : "memory");
}
typedef __bf16 bf16x2_t __attribute__((ext_vector_type(2)));
__device__ __forceinline__ float dot2(unsigned a, unsigned b, float acc) {
    return __builtin_amdgcn_fdot2_f32_bf16(__builtin_bit_cast(bf16x2_t, a), __builtin_bit_cast(bf16x2_t, b), acc, false);
}
__device__ __forceinline__ void frag_sum(const bf16x8& f, float& s1) {
    const u32x4 u = __builtin_bit_cast(u32x4, f);
#pragma unroll
    for (int e = 0; e < 4; ++e) s1 = dot2(u[e], 0x3f803f80u, s1);
}
__device__ __forceinline__ void frag_sum_sq(const bf16x8& f, float& s1, float& s2) {
    const u32x4 u = __builtin_bit_cast(u32x4, f);
#pragma unroll
    for (int e = 0; e < 4; ++e) { s1 = dot2(u[e], 0x3f803f80u, s1); s2 = dot2(u[e], u[e], s2); }
}
__device__ __forceinline__ float red_g(float v) { v += __shfl_xor(v, 16); v += __shfl_xor(v, 32); return v; }
__device__ __forceinline__ void prefetch_region(const char* base, int lpr, long pitch, int chunk, int part, int tid,
                                                int nthreads, unsigned& dummy) {
    for (int l = tid; l < chunk; l += nthreads) {
        const int L = part * chunk + l, row = L / lpr, c = L - row * lpr;
        const char* a = base + (long)row * pitch + c * 128;
        asm volatile("global_load_dword %0, %1, off" : "+v"(dummy) : "v"(a) : "memory");
    }
}
#define ST8(ptr, v) (*(u32x2*)(ptr) = (v))
template <int N> __device__ __forceinline__ void wait_vm() { asm volatile("s_waitcnt vmcnt(%0)" ::"n"(N) : "memory"); }

enum { EPI_STORE = 0, EPI_RES = 1, EPI_RES_STAT = 2 };

template <int BM, int BN, int WM, int WN, int NST, int EPI, int LNF, int RM, int RN>
__global__ __launch_bounds__(WM * WN * 64) void gemm_kernel(
    const bf16_t* __restrict__ X, const bf16_t* __restrict__ W0,
    const bf16_t* __restrict__ R, bf16_t* __restrict__ C, float* __restrict__ stat_out,
    int M, int N, int K, int ldw, int ldc) {
    constexpr int NWAVE = WM * WN;
    constexpr int MF = BM / 16 / WM, NF = BN / 16 / WN, NB = BN / 16;
    constexpr int A_BYTES = BM * 128, B_BYTES = BN * 128, ST_BYTES = A_BYTES + B_BYTES;
    constexpr int LPW = (BM + BN) / 8 / NWAVE;
    constexpr int LPA = BM / 8 / NWAVE;
    static_assert((BM + BN) % (8 * NWAVE) == 0 && (BM % (8 * NWAVE)) == 0, "piece split");
    static_assert(NB % 4 == 0, "NB%4");
    static_assert(!LNF || (MF == WN && WM == 2), "LN fold layout");
    static_assert(EPI != EPI_STORE || (BM * 4 == NWAVE * 64), "head-major store layout");
    extern __shared__ __attribute__((aligned(16))) char smem[];
    LAS char* lds = (LAS char*)smem;

    const int tid = threadIdx.x, lane = tid & 63;
    const int wave = __builtin_amdgcn_readfirstlane(tid >> 6);
    const int wm = wave / WN, wn = wave % WN;

    const int tiles_m = M / BM, tiles_n = N / BN;
    int tm, tn;
    {
        const int xcd = blockIdx.x & 7, idx = blockIdx.x >> 3;
        const int rm = xcd / RN, rn = xcd % RN;
        const int sm = tiles_m / RM, sn = tiles_n / RN;
        tm = rm * sm + idx % sm; tn = rn * sn + idx / sm;
    }
    const int m0 = tm * BM, n0 = tn * BN;
    unsigned pf_dummy = 0;

    unsigned voff[LPW];
    const char* sbase[LPW];
#pragma unroll
    for (int j = 0; j < LPW; ++j) {
        const int ii = wave + NWAVE * j;
        if (j < LPA) {
            const int row = ii * 8 + (lane >> 3), ch = (lane & 7) ^ (row & 7);
            voff[j] = (unsigned)(row * K * 2 + ch * 16);
            sbase[j] = (const char*)(X + (size_t)m0 * K);
        } else {
            const int pi = ii - BM / 8;
            const int kb = pi / (NB / 4), nb = 4 * (pi % (NB / 4)) + (lane >> 4);
            const int r = ((lane & 15) >> 1) ^ (4 * (kb & 1)), half = lane & 1;
            voff[j] = (unsigned)(((8 * kb + r) * ldw + 16 * nb + 8 * half) * 2);
            sbase[j] = (const char*)(W0 + n0);
        }
    }
    const unsigned lds_base = (unsigned)(uintptr_t)lds;
    const int NT = K / 64;
    auto issue = [&](int buf) {
#pragma unroll
        for (int j = 0; j < LPW; ++j) {
            const int ii = wave + NWAVE * j;
            glds16(sbase[j], voff[j], lds_base + buf * ST_BYTES + ii * 1024);
            sbase[j] += (j < LPA) ? 128 : (long)128 * ldw;
        }
    };

    const int fr = lane & 15, g = lane >> 4, q = (lane & 15) >> 2, p = lane & 3;
    const int rotm = LNF ? wn : 0, rotk = LNF ? wm : 0;
    const int xrow = wm * (MF * 16) + fr;
    const int xoff0 = xrow * 128 + ((g ^ (xrow & 7)) << 4);
    const int woff0 = A_BYTES + g * NB * 256 + (q + 4 * (g & 1)) * 32 + p * 8 + wn * NF * 256;

    f32x4 acc[MF][NF];
#pragma unroll
    for (int i = 0; i < MF; ++i)
#pragma unroll
        for (int j = 0; j < NF; ++j) acc[i][j] = (f32x4){0.f, 0.f, 0.f, 0.f};
    f32x4 s1 = {0.f, 0.f, 0.f, 0.f}, s2 = {0.f, 0.f, 0.f, 0.f}, cs[NF];
#pragma unroll
    for (int j = 0; j < NF; ++j) cs[j] = (f32x4){0.f, 0.f, 0.f, 0.f};
    const bf16x8 ones = {0x3f80, 0x3f80, 0x3f80, 0x3f80, 0x3f80, 0x3f80, 0x3f80, 0x3f80};

#pragma unroll
    for (int s = 0; s < NST - 1; ++s) issue(s);

    auto read_frags = [&](LAS const char* base, int slot, bf16x8 (&xf)[MF], bf16x8 (&wf)[NF]) {
        const int ks = slot ^ rotk;
#pragma unroll
        for (int j = 0; j < NF; ++j) {
            const int o = ks * (4 * NB * 256) + j * 256;
            s16x4 lo = tr_read(base + woff0 + o);
            s16x4 hi = tr_read(base + (woff0 ^ 128) + o);
            wf[j] = (bf16x8){lo[0], lo[1], lo[2], lo[3], hi[0], hi[1], hi[2], hi[3]};
        }
#pragma unroll
        for (int i = 0; i < MF; ++i)
            xf[i] = *(LAS const bf16x8*)(base + ((xoff0 ^ (ks * 64)) + (i ^ rotm) * 2048));
    };
    auto do_mfma = [&](bf16x8 (&xf)[MF], bf16x8 (&wf)[NF], bool colsum) {
#pragma unroll
        for (int i = 0; i < MF; ++i)
#pragma unroll
            for (int j = 0; j < NF; ++j)
                acc[i][j] = __builtin_amdgcn_mfma_f32_16x16x32_bf16(wf[j], xf[i], acc[i][j], 0, 0, 0);
        if (LNF) {
            s1 = __builtin_amdgcn_mfma_f32_16x16x32_bf16(ones, xf[0], s1, 0, 0, 0);
            s2 = __builtin_amdgcn_mfma_f32_16x16x32_bf16(xf[0], xf[0], s2, 0, 0, 0);
            if (colsum) {
#pragma unroll
                for (int j = 0; j < NF; ++j) cs[j] = __builtin_amdgcn_mfma_f32_16x16x32_bf16(wf[j], ones, cs[j], 0, 0, 0);
            }
        }
    };
    auto interleave = [&]() {
        constexpr int NM = MF * NF, NR = MF + 2 * NF, RPM = (NR + NM - 1) / NM;
#pragma unroll
        for (int i = 0; i < NM; ++i) {
            __builtin_amdgcn_sched_group_barrier(0x8, 1, 0);
            __builtin_amdgcn_sched_group_barrier(0x100, RPM, 0);
        }
    };
    bf16x8 xa[MF], wa[NF], xb[MF], wb[NF];
    wait_vm<(NST - 2) * LPW>();
    __builtin_amdgcn_s_barrier();
    asm volatile("" ::: "memory");
    read_frags(lds, 0, xa, wa);

    int buf = 0, pbuf = NST - 1;
    for (int kt = 0; kt < NT; ++kt) {
        const int rem = NT - 2 - kt;
        if (rem >= NST - 3) wait_vm<(NST - 3) * LPW>();
        else if (NST > 4 && rem == 1) wait_vm<LPW>();
        else if (NST > 5 && rem == 2) wait_vm<2 * LPW>();
        else wait_vm<0>();
        __builtin_amdgcn_s_barrier();
        asm volatile("" ::: "memory");
        if (kt + NST - 1 < NT) issue(pbuf);
        const int nbuf = (buf + 1 == NST) ? 0 : buf + 1;
        __builtin_amdgcn_sched_barrier(0);
        read_frags(lds + buf * ST_BYTES, 1, xb, wb);
        do_mfma(xa, wa, true);
        interleave();
        __builtin_amdgcn_sched_barrier(0);
        read_frags(lds + nbuf * ST_BYTES, 0, xa, wa);
        do_mfma(xb, wb, false);
        interleave();
        __builtin_amdgcn_sched_barrier(0);
        pbuf = buf; buf = nbuf;
    }

    asm volatile("" :: "v"(pf_dummy));
    constexpr int NTHR = NWAVE * 64, CPR = BN / 8, NCH = BM * CPR / NTHR;
    constexpr int RS = (EPI == EPI_STORE) ? BN * 2 + 16 : BN * 4 + 16;
    LAS float* rowstat = (LAS float*)(lds + BM * RS);
    LAS float* colsum = rowstat + BM * 2;
    u32x4 rres[NCH];
    if (EPI != EPI_STORE) {
#pragma unroll
        for (int r = 0; r < NCH; ++r) {
            const int c = tid + NTHR * r, row = c / CPR, c8 = c % CPR;
            rres[r] = *(const u32x4*)(R + (size_t)(m0 + row) * ldc + n0 + c8 * 8);
        }
    }
    __syncthreads();
    if (LNF) {
        if (g == (fr >> 2)) {
            const int r = wm * (MF * 16) + wn * 16 + fr;
            const int d = fr & 3;
            rowstat[2 * r] = s1[0];
            rowstat[2 * r + 1] = d == 0 ? s2[0] : d == 1 ? s2[1] : d == 2 ? s2[2] : s2[3];
        }
        if (fr == 0) {
#pragma unroll
            for (int j = 0; j < NF; ++j) *(LAS f32x4*)(colsum + wm * BN + (wn * NF + j) * 16 + 4 * g) = cs[j];
        }
        __syncthreads();
    }
    f32x4 csv[NF];
    if (LNF) {
#pragma unroll
        for (int j = 0; j < NF; ++j) {
            const int c = (wn * NF + j) * 16 + 4 * g;
            csv[j] = *(LAS const f32x4*)(colsum + c) + *(LAS const f32x4*)(colsum + BN + c);
        }
    }
#pragma unroll
    for (int i = 0; i < MF; ++i) {
        const int rl = wm * (MF * 16) + (i ^ rotm) * 16 + fr;
        float mean = 0.f, rstd = 1.f;
        if (LNF) {
            const float a = rowstat[2 * rl], b = rowstat[2 * rl + 1];
            mean = a * (1.0f / D_MODEL);
            const float var = fmaxf(b * (1.0f / D_MODEL) - mean * mean, 0.f);
            rstd = 1.0f / sqrtf(var + 1e-5f);
        }
#pragma unroll
        for (int j = 0; j < NF; ++j) {
            const int nl = (wn * NF + j) * 16 + 4 * g;
            f32x4 v = acc[i][j];
            if (LNF) v = (v - csv[j] * mean) * rstd;
            if (EPI == EPI_STORE) {
                u32x2 w; w[0] = pk_bf16(v[0], v[1]); w[1] = pk_bf16(v[2], v[3]);
                *(LAS u32x2*)(lds + rl * RS + nl * 2) = w;
            } else {
                *(LAS f32x4*)(lds + rl * RS + nl * 4) = v;
            }
        }
    }
    __syncthreads();
#pragma unroll
    for (int r = 0; r < NCH; ++r) {
        const int c = tid + NTHR * r, row = c / CPR, c8 = c % CPR;
        u32x4 w;
        if (EPI == EPI_STORE) {
            const int hrow = tid >> 2, d8 = tid & 3;
            w = *(LAS const u32x4*)(lds + hrow * RS + r * 64 + d8 * 16);
            *(u32x4*)(C + ((size_t)(n0 / 32 + r) * M + m0 + hrow) * 32 + d8 * 8) = w;
            continue;
        } else {
            f32x4 v0 = *(LAS const f32x4*)(lds + row * RS + c8 * 32), v1 = *(LAS const f32x4*)(lds + row * RS + c8 * 32 + 16);
            const u32x4 rv = rres[r];
            v0[0] += bf2f(rv[0] & 0xffffu); v0[1] += bf2f(rv[0] >> 16); v0[2] += bf2f(rv[1] & 0xffffu); v0[3] += bf2f(rv[1] >> 16);
            v1[0] += bf2f(rv[2] & 0xffffu); v1[1] += bf2f(rv[2] >> 16); v1[2] += bf2f(rv[3] & 0xffffu); v1[3] += bf2f(rv[3] >> 16);
            w[0] = pk_bf16(v0[0], v0[1]); w[1] = pk_bf16(v0[2], v0[3]); w[2] = pk_bf16(v1[0], v1[1]); w[3] = pk_bf16(v1[2], v1[3]);
        }
        *(u32x4*)(C + (size_t)(m0 + row) * ldc + n0 + c8 * 8) = w;
        if (EPI == EPI_RES_STAT) {
            float a = 0.f, b = 0.f;
#pragma unroll
            for (int e = 0; e < 4; ++e) {
                const float x0 = bf2f(w[e] & 0xffffu), x1 = bf2f(w[e] >> 16);
                a += x0 + x1; b += x0 * x0 + x1 * x1;
            }
#pragma unroll
            for (int o = 1; o < CPR; o <<= 1) { a += __shfl_xor(a, o); b += __shfl_xor(b, o); }
            if (c8 == 0) { float2 o2; o2.x = a; o2.y = b; *(float2*)(stat_out + ((size_t)(m0 + row) * tiles_n + tn) * 2) = o2; }
        }
    }
}

__global__ __launch_bounds__(512, 2) void glu_gemm_kernel(
    const bf16_t* __restrict__ X, const bf16_t* __restrict__ W1, const bf16_t* __restrict__ W3,
    const float* __restrict__ stat_in, bf16_t* __restrict__ C, const bf16_t* __restrict__ pfw) {
    constexpr int K = D_MODEL, LDW = F_DIM, HTB = 16384;
    if (blockIdx.x >= 224) {
        u32x4 d = {0u, 0u, 0u, 0u};
        const char* a = (const char*)pfw + (size_t)(blockIdx.x - 224) * 229376 + threadIdx.x * 16;
#pragma unroll
        for (int r = 0; r < 28; ++r) asm volatile("global_load_dwordx4 %0, %1, off offset:0" : "+v"(d) : "v"(a + r * 8192) : "memory");
        asm volatile("s_waitcnt vmcnt(0)" :: "v"(d) : "memory");
        return;
    }
    extern __shared__ __attribute__((aligned(16))) char smem[];
    LAS char* lds = (LAS char*)smem;
    const unsigned lds_base = (unsigned)(uintptr_t)lds;
    const int tid = threadIdx.x, lane = tid & 63;
    const int wave = __builtin_amdgcn_readfirstlane(tid >> 6);
    const int wr = wave >> 2, wc = wave & 3;
    int tm, tn;
    { const int xcd = blockIdx.x & 7, idx = blockIdx.x >> 3; tm = (xcd >> 2) * 4 + (idx & 3); tn = (xcd & 3) * 7 + (idx >> 2); }
    const int m0 = tm * 256, n0 = tn * 128;
    unsigned pf_dummy = 0;

    unsigned xoffs[2], woffs[2];
#pragma unroll
    for (int j = 0; j < 2; ++j) {
        const int pc = wave + 8 * j;
        const int row = pc * 8 + (lane >> 3), ch = (lane & 7) ^ (row & 7);
        xoffs[j] = (unsigned)(row * K * 2 + ch * 16);
        const int kb = pc >> 1, nb = 4 * (pc & 1) + (lane >> 4);
        const int r = ((lane & 15) >> 1) ^ (4 * (kb & 1)), half = lane & 1;
        woffs[j] = (unsigned)(((8 * kb + r) * LDW + 16 * nb + 8 * half) * 2);
    }
    const char* xsrc = (const char*)(X + (size_t)m0 * K);
    const char* w1src = (const char*)(W1 + n0);
    const char* w3src = (const char*)(W3 + n0);
#define SA_OFF(b, h) (((b) * 2 + (h)) * HTB)
#define SB_OFF(b, h) ((4 + (b) * 2 + (h)) * HTB)
#define STAGE_A(b, h, kt) do { const char* _s = xsrc + (size_t)(h) * 128 * K * 2 + (kt) * 128; \
        glds16(_s, xoffs[0], lds_base + SA_OFF(b, h) + wave * 1024); glds16(_s, xoffs[1], lds_base + SA_OFF(b, h) + (wave + 8) * 1024); } while (0)
#define STAGE_B(b, h, kt) do { const char* _s = ((h) ? w3src : w1src) + (size_t)(kt) * 64 * LDW * 2; \
        glds16(_s, woffs[0], lds_base + SB_OFF(b, h) + wave * 1024); glds16(_s, woffs[1], lds_base + SB_OFF(b, h) + (wave + 8) * 1024); } while (0)
    const int fr = lane & 15, g = lane >> 4, q = (lane & 15) >> 2, p = lane & 3;
    const int xrow = wr * 64 + fr;
    const int xo0 = (xrow * 128 + ((g ^ (xrow & 7)) << 4)) ^ (wr * 64);
    const int wo0 = g * 2048 + (q + 4 * (g & 1)) * 32 + p * 8 + wc * 512;
    unsigned xb_[4][2], wb_[2][2];
#pragma unroll
    for (int m = 0; m < 4; ++m)
#pragma unroll
        for (int k = 0; k < 2; ++k) { xb_[m][k] = lds_base + ((xo0 ^ (k * 64)) + m * 2048); asm volatile("" : "+v"(xb_[m][k])); }
#pragma unroll
    for (int t = 0; t < 2; ++t)
#pragma unroll
        for (int k = 0; k < 2; ++k) { wb_[t][k] = lds_base + 4 * HTB + ((wo0 ^ (t * 128)) + (k ^ wr) * 8192); asm volatile("" : "+v"(wb_[t][k])); }
#define LDSP(a) ((LAS const char*)(uintptr_t)(a))
#define LDA(dst, b, h) do { _Pragma("unroll") for (int m = 0; m < 4; ++m) _Pragma("unroll") for (int k = 0; k < 2; ++k) \
        dst[m][k] = *(LAS const bf16x8*)(LDSP(xb_[m][k]) + SA_OFF(b, h)); } while (0)
#define LDB(dst, b, h) do { _Pragma("unroll") for (int n = 0; n < 2; ++n) _Pragma("unroll") for (int k = 0; k < 2; ++k) { \
        s16x4 lo_ = tr_read(LDSP(wb_[0][k]) + SA_OFF(b, h) + n * 256); \
        s16x4 hi_ = tr_read(LDSP(wb_[1][k]) + SA_OFF(b, h) + n * 256); \
        dst[n][k] = (bf16x8){lo_[0], lo_[1], lo_[2], lo_[3], hi_[0], hi_[1], hi_[2], hi_[3]}; } } while (0)
#define MMA(ai, bj, At, Bt, CS) do { __builtin_amdgcn_s_setprio(1); \
        _Pragma("unroll") for (int m = 0; m < 4; ++m) _Pragma("unroll") for (int n = 0; n < 2; ++n) _Pragma("unroll") for (int k = 0; k < 2; ++k) \
            acc[ai][bj][m][n] = __builtin_amdgcn_mfma_f32_16x16x32_bf16(Bt[n][k], At[m][k], acc[ai][bj][m][n], 0, 0, 0); \
        if ((CS) & 2) { cs[bj][0] = __builtin_amdgcn_mfma_f32_16x16x32_bf16(Bt[0][0], ones, cs[bj][0], 0, 0, 0); \
                        cs[bj][1] = __builtin_amdgcn_mfma_f32_16x16x32_bf16(Bt[1][0], ones, cs[bj][1], 0, 0, 0); } \
        __builtin_amdgcn_s_setprio(0); } while (0)
#define WAIT_V(n) asm volatile("s_waitcnt vmcnt(" #n ")" ::: "memory")
#define WAIT_L(n) asm volatile("s_waitcnt lgkmcnt(" #n ")" ::: "memory")
#define BAR __builtin_amdgcn_s_barrier()
#define SCHED __builtin_amdgcn_sched_barrier(0)
    f32x4 acc[2][2][4][2] = {};
    bf16x8 At[4][2], B0[2][2], B1[2][2];
    f32x4 cs[2][2] = {};
    const bf16x8 ones = {0x3f80, 0x3f80, 0x3f80, 0x3f80, 0x3f80, 0x3f80, 0x3f80, 0x3f80};
    constexpr int nt = K / 64;
    {
        const char* ssrc = (const char*)(stat_in + (size_t)m0 * 32);
#pragma unroll
        for (int j = 0; j < 4; ++j) glds16(ssrc, (unsigned)((wave * 4 + j) * 1024 + lane * 16), lds_base + 8 * HTB + (wave * 4 + j) * 1024);
    }
    STAGE_B(0, 0, 0); STAGE_A(0, 0, 0); STAGE_B(0, 1, 0); STAGE_A(0, 1, 0);
    if (wr == 1) BAR;
    WAIT_V(4); BAR;
    STAGE_B(1, 0, 1); STAGE_A(1, 0, 1); STAGE_B(1, 1, 1);
    WAIT_V(6); BAR;
    for (int t = 0; t < nt; t += 2) {
        const bool more = t + 2 < nt;
        LDB(B0, 0, 0); SCHED; LDA(At, 0, 0); STAGE_A(1, 1, t + 1);
        WAIT_L(8); BAR; WAIT_L(0); MMA(0, 0, At, B0, 2); BAR; SCHED;
        LDB(B1, 0, 1); if (more) STAGE_B(0, 0, t + 2);
        BAR; WAIT_L(0); MMA(0, 1, At, B1, 1); BAR;
        LDA(At, 0, 1); if (more) STAGE_A(0, 0, t + 2);
        BAR; WAIT_L(0); MMA(1, 0, At, B0, 1); BAR; SCHED;
        if (more) { STAGE_B(0, 1, t + 2); WAIT_V(6); } else { WAIT_V(0); }
        BAR; MMA(1, 1, At, B1, 2); BAR;
        LDB(B0, 1, 0); SCHED; LDA(At, 1, 0); if (more) STAGE_A(0, 1, t + 2);
        WAIT_L(8); BAR; WAIT_L(0); MMA(0, 0, At, B0, 2); BAR; SCHED;
        LDB(B1, 1, 1); if (more) STAGE_B(1, 0, t + 3);
        BAR; WAIT_L(0); MMA(0, 1, At, B1, 1); BAR;
        LDA(At, 1, 1); if (more) STAGE_A(1, 0, t + 3);
        BAR; WAIT_L(0); MMA(1, 0, At, B0, 1); BAR; SCHED;
        if (more) { STAGE_B(1, 1, t + 3); WAIT_V(6); }
        BAR; MMA(1, 1, At, B1, 2); BAR;
    }
    if (wr == 0) BAR;
    asm volatile("" :: "v"(pf_dummy));
    constexpr int RS = 272;
    LAS float* rowstat = (LAS float*)(lds + 256 * RS);
    LAS float* colsum = rowstat + 512;
    if (fr == 0) {
#pragma unroll
        for (int b = 0; b < 2; ++b)
#pragma unroll
            for (int n = 0; n < 2; ++n) *(LAS f32x4*)(colsum + wr * 256 + b * 128 + wc * 32 + n * 16 + 4 * g) = cs[b][n];
    }
    if (tid < 256) {
        LAS const f32x4* sp = (LAS const f32x4*)(lds + 8 * HTB + tid * 128);
        f32x4 t = sp[tid & 7];
#pragma unroll
        for (int e = 1; e < 8; ++e) t += sp[e ^ (tid & 7)];
        const float mean = (t[0] + t[2]) * (1.0f / D_MODEL);
        const float rstd = 1.0f / sqrtf(fmaxf((t[1] + t[3]) * (1.0f / D_MODEL) - mean * mean, 0.f) + 1e-5f);
        rowstat[2 * tid] = mean * rstd; rowstat[2 * tid + 1] = rstd;
    }
    __syncthreads();
    f32x4 csv[2][2];
#pragma unroll
    for (int b = 0; b < 2; ++b)
#pragma unroll
        for (int n = 0; n < 2; ++n) {
            const int c = b * 128 + wc * 32 + n * 16 + 4 * g;
            csv[b][n] = *(LAS const f32x4*)(colsum + c) + *(LAS const f32x4*)(colsum + 256 + c);
        }
#pragma unroll
    for (int ai = 0; ai < 2; ++ai)
#pragma unroll
        for (int m = 0; m < 4; ++m) {
            const int rl = ai * 128 + wr * 64 + m * 16 + fr;
            const float mr = rowstat[2 * rl], rstd = rowstat[2 * rl + 1];
#pragma unroll
            for (int n = 0; n < 2; ++n) {
                const f32x4 gv = acc[ai][0][m][n] * rstd - csv[0][n] * mr;
                const f32x4 uv = acc[ai][1][m][n] * rstd - csv[1][n] * mr;
                float o[4];
#pragma unroll
                for (int r = 0; r < 4; ++r) o[r] = gv[r] * __builtin_amdgcn_rcpf(1.0f + __expf(-gv[r])) * uv[r];
                u32x2 w; w[0] = pk_bf16(o[0], o[1]); w[1] = pk_bf16(o[2], o[3]);
                *(LAS u32x2*)(lds + rl * RS + (wc * 32 + n * 16 + 4 * g) * 2) = w;
            }
        }
    __syncthreads();
#pragma unroll
    for (int r = 0; r < 8; ++r) {
        const int c = tid + 512 * r, row = c >> 4, c8 = c & 15;
        *(u32x4*)(C + (size_t)(m0 + row) * LDW + n0 + c8 * 8) = *(LAS const u32x4*)(lds + row * RS + c8 * 16);
    }
#undef SA_OFF
#undef SB_OFF
#undef STAGE_A
#undef STAGE_B
#undef LDA
#undef LDB
#undef MMA
}

template <int NST>
__global__ __launch_bounds__(512, 2) void glu2_kernel(
    const bf16_t* __restrict__ X, const bf16_t* __restrict__ W1, const bf16_t* __restrict__ W3,
    const float* __restrict__ stat_in, bf16_t* __restrict__ C) {
    constexpr int K = D_MODEL, LDW = F_DIM, STB = 32768, NT = K / 32;
    extern __shared__ __attribute__((aligned(16))) char smem[];
    LAS char* lds = (LAS char*)smem;
    const unsigned lds_base = (unsigned)(uintptr_t)lds;
    const int tid = threadIdx.x, lane = tid & 63;
    const int wave = __builtin_amdgcn_readfirstlane(tid >> 6);
    const int wm = wave >> 2, wn = wave & 3;
    int tm, tn;
    { const int xcd = blockIdx.x & 7, idx = blockIdx.x >> 3; tm = (xcd >> 2) * 4 + (idx & 3); tn = (xcd & 3) * 7 + (idx >> 2); }
    const int m0 = tm * 256, n0 = tn * 128;
    unsigned pf_dummy = 0;

    unsigned xoffs[2], woffs[2];
#pragma unroll
    for (int j = 0; j < 2; ++j) {
        const int pc = wave + 8 * j;
        const int row = pc * 16 + (lane >> 2), ch = (lane & 3) ^ ((4 - ((row >> 2) & 3)) & 3);
        xoffs[j] = (unsigned)(row * K * 2 + ch * 16);
        const int kb = pc >> 2, nb = 4 * (pc & 1) + (lane >> 4);
        const int r = ((lane & 15) >> 1) ^ (4 * (kb & 1)), half = lane & 1;
        woffs[j] = (unsigned)(((8 * kb + r) * LDW + 16 * nb + 8 * half) * 2);
    }
    const char* xsrc = (const char*)(X + (size_t)m0 * K);
    const char* wsrc = (const char*)(((wave & 2) ? W3 : W1) + n0);
    auto issue = [&](int buf) {
        const unsigned b = lds_base + buf * STB;
        glds16(xsrc, xoffs[0], b + wave * 1024);
        glds16(xsrc, xoffs[1], b + (wave + 8) * 1024);
        glds16(wsrc, woffs[0], b + 16384 + wave * 1024);
        glds16(wsrc, woffs[1], b + 16384 + (wave + 8) * 1024);
        xsrc += 64; wsrc += (size_t)32 * LDW * 2;
    };
    const int fr = lane & 15, g = lane >> 4, q = (lane & 15) >> 2, p = lane & 3;
    const int xo0 = (wm * 128 + fr) * 64 + ((g ^ ((4 - (fr >> 2)) & 3)) << 4);
    const int wo0 = 16384 + g * 4096 + (q + 4 * (g & 1)) * 32 + p * 8 + wn * 512;

    f32x4 acc[8][4];
#pragma unroll
    for (int i = 0; i < 8; ++i)
#pragma unroll
        for (int j = 0; j < 4; ++j) acc[i][j] = (f32x4){0.f, 0.f, 0.f, 0.f};
    f32x4 cs[4] = {};
    const bf16x8 ones = {0x3f80, 0x3f80, 0x3f80, 0x3f80, 0x3f80, 0x3f80, 0x3f80, 0x3f80};

    {
        const char* ssrc = (const char*)(stat_in + (size_t)m0 * 32);
#pragma unroll
        for (int j = 0; j < 4; ++j) glds16(ssrc, (unsigned)((wave * 4 + j) * 1024 + lane * 16), lds_base + NST * STB + (wave * 4 + j) * 1024);
    }
#pragma unroll
    for (int s = 0; s < NST; ++s) issue(s);

    auto read_frags = [&](LAS const char* base, bf16x8 (&xf)[8], bf16x8 (&wf)[4]) {
#pragma unroll
        for (int j = 0; j < 4; ++j) {
            const int o = (j & 1) * 256 + (j >> 1) * 2048;
            s16x4 lo = tr_read(base + wo0 + o);
            s16x4 hi = tr_read(base + (wo0 ^ 128) + o);
            wf[j] = (bf16x8){lo[0], lo[1], lo[2], lo[3], hi[0], hi[1], hi[2], hi[3]};
        }
#pragma unroll
        for (int i = 0; i < 8; ++i) xf[i] = *(LAS const bf16x8*)(base + xo0 + i * 1024);
    };
    auto do_mfma = [&](bf16x8 (&xf)[8], bf16x8 (&wf)[4]) {
#pragma unroll
        for (int i = 0; i < 8; ++i)
#pragma unroll
            for (int j = 0; j < 4; ++j)
                acc[i][j] = __builtin_amdgcn_mfma_f32_16x16x32_bf16(wf[j], xf[i], acc[i][j], 0, 0, 0);
    };
    auto interleave = [&]() {
#pragma unroll
        for (int i = 0; i < 16; ++i) {
            __builtin_amdgcn_sched_group_barrier(0x8, 2, 0);
            __builtin_amdgcn_sched_group_barrier(0x100, 1, 0);
        }
    };
    auto colsum = [&](bf16x8 (&wf)[4]) {
#pragma unroll
        for (int j = 0; j < 4; ++j) cs[j] = __builtin_amdgcn_mfma_f32_16x16x32_bf16(wf[j], ones, cs[j], 0, 0, 0);
    };
    bf16x8 xa[8], wa[4], xb[8], wb[4];
    wait_vm<(NST - 1) * 4>();
    __builtin_amdgcn_s_barrier();
    asm volatile("" ::: "memory");
    read_frags(lds, xa, wa);
    int buf = 0;
    auto step = [&](int kt, bf16x8 (&xc)[8], bf16x8 (&wc_)[4], bf16x8 (&xn)[8], bf16x8 (&wn_)[4], bool docs) {
        const int rem = NT - 2 - kt;
        if (rem >= NST - 2) wait_vm<(NST - 2) * 4>();
        else if (rem == 1) wait_vm<4>();
        else if (NST > 4 && rem == 2) wait_vm<8>();
        else wait_vm<0>();
        asm volatile("s_waitcnt lgkmcnt(0)" ::: "memory");
        __builtin_amdgcn_s_barrier();
        asm volatile("" ::: "memory");
        if (kt + NST < NT) issue(buf);
        buf = (buf + 1 == NST) ? 0 : buf + 1;
        __builtin_amdgcn_sched_barrier(0);
        read_frags(lds + buf * STB, xn, wn_);
        do_mfma(xc, wc_);
        interleave();
        __builtin_amdgcn_sched_barrier(0);
        if (docs) colsum(wc_);
        __builtin_amdgcn_sched_barrier(0);
    };
    for (int kt = 0; kt < NT; kt += 2) {
        step(kt, xa, wa, xb, wb, wm == 0);
        step(kt + 1, xb, wb, xa, wa, wm == 1);
    }
    asm volatile("" :: "v"(pf_dummy));
    __syncthreads();
    LAS float* rowstat = (LAS float*)lds;
    LAS float* colsm = (LAS float*)(lds + 2048);
    if (fr == 0) {
#pragma unroll
        for (int j = 0; j < 4; ++j) *(LAS f32x4*)(colsm + wm * 256 + (j >> 1) * 128 + wn * 32 + (j & 1) * 16 + 4 * g) = cs[j];
    }
    if (tid < 256) {
        LAS const f32x4* sp = (LAS const f32x4*)(lds + NST * STB + tid * 128);
        f32x4 t = sp[tid & 7];
#pragma unroll
        for (int e = 1; e < 8; ++e) t += sp[e ^ (tid & 7)];
        const float mean = (t[0] + t[2]) * (1.0f / D_MODEL);
        const float rstd = 1.0f / sqrtf(fmaxf((t[1] + t[3]) * (1.0f / D_MODEL) - mean * mean, 0.f) + 1e-5f);
        rowstat[2 * tid] = mean; rowstat[2 * tid + 1] = rstd;
    }
    __syncthreads();
    f32x4 csv[4];
#pragma unroll
    for (int j = 0; j < 4; ++j) {
        const int c = (j >> 1) * 128 + wn * 32 + (j & 1) * 16 + 4 * g;
        csv[j] = *(LAS const f32x4*)(colsm + c) + *(LAS const f32x4*)(colsm + 256 + c);
    }
#pragma unroll
    for (int i = 0; i < 8; ++i) {
        const int rl = wm * 128 + i * 16 + fr;
        const float mean = rowstat[2 * rl], rstd = rowstat[2 * rl + 1];
#pragma unroll
        for (int n = 0; n < 2; ++n) {
            const f32x4 gv = (acc[i][n] - csv[n] * mean) * rstd;
            const f32x4 uv = (acc[i][n + 2] - csv[n + 2] * mean) * rstd;
            float o[4];
#pragma unroll
            for (int r = 0; r < 4; ++r) o[r] = gv[r] * __builtin_amdgcn_rcpf(1.0f + __expf(-gv[r])) * uv[r];
            u32x2 w; w[0] = pk_bf16(o[0], o[1]); w[1] = pk_bf16(o[2], o[3]);
            *(u32x2*)(C + (size_t)(m0 + rl) * LDW + n0 + wn * 32 + n * 16 + 4 * g) = w;
        }
    }
}

__global__ __launch_bounds__(512) void attn_kernel(const bf16_t* __restrict__ qkv, bf16_t* __restrict__ O,
                                                   const bf16_t* __restrict__ pf0) {
    extern __shared__ __attribute__((aligned(16))) char smem[];
    LAS char* lds = (LAS char*)smem;
    LAS char* ldsK = lds;
    LAS char* ldsV = lds + 32768;
    const unsigned lds_base = (unsigned)(uintptr_t)lds;
    const int tid = threadIdx.x, lane = tid & 63;
    const int w = __builtin_amdgcn_readfirstlane(tid >> 6);
    const int h = blockIdx.x & 31, qb = blockIdx.x >> 5;
    const int q0 = qb * 256, kbase = q0 - 256;
    const bf16_t* Qh = qkv + (size_t)h * (S_LEN * 32);
    const bf16_t* Kh = qkv + (size_t)(32 + h) * (S_LEN * 32);
    const bf16_t* Vh = qkv + (size_t)(64 + h) * (S_LEN * 32);

    {
        const unsigned koff = (unsigned)((lane >> 2) * 64 + (((lane & 3) ^ ((lane >> 4) & 3)) << 4));
        const unsigned voff = (unsigned)(lane * 16);
#pragma unroll
        for (int i = 0; i < 4; ++i) {
            const int pc = w + 8 * i;
            if (kbase + pc * 16 >= 0) {
                glds16((const char*)(Kh + (long)(kbase + pc * 16) * 32), koff, lds_base + pc * 1024);
                glds16((const char*)(Vh + (long)(kbase + pc * 16) * 32), voff, lds_base + 32768 + pc * 1024);
            }
        }
    }
    const int r32 = lane & 31, hi = lane >> 5;
    const int qw = q0 + 32 * w;
    bf16x8 qf[2];
#pragma unroll
    for (int s = 0; s < 2; ++s) qf[s] = *(const bf16x8*)(Qh + (size_t)(qw + r32) * 32 + 16 * s + 8 * hi);
    wait_vm<0>();
    __syncthreads();
    u32x4 pf_dummy = {0u, 0u, 0u, 0u};
    {
        const char* a0 = (const char*)pf0 + (size_t)(blockIdx.x * 512 + tid) * 16;
        asm volatile("global_load_dwordx4 %0, %1, off" : "+v"(pf_dummy) : "v"(a0) : "memory");
    }

    f32x16 sc[9];
    const float NEG = -INFINITY;
#pragma unroll
    for (int j = 0; j < 9; ++j) {
        const int kb = qw - 256 + 32 * j;
        if (kb >= 0) {
            const int key = 32 * (w + j) + r32;
            f32x16 a = {};
#pragma unroll
            for (int s = 0; s < 2; ++s) {
                const bf16x8 kf = *(LAS const bf16x8*)(ldsK + key * 64 + (((2 * s + hi) ^ ((key >> 2) & 3)) << 4));
                a = __builtin_amdgcn_mfma_f32_32x32x16_bf16(kf, qf[s], a, 0, 0, 0);
            }
            sc[j] = a;
        } else {
#pragma unroll
            for (int r = 0; r < 16; ++r) sc[j][r] = NEG;
        }
    }
#pragma unroll
    for (int r = 0; r < 16; ++r) {
        const int i = (r & 3) + 8 * (r >> 2) + 4 * hi;
        if (!(i > r32)) sc[0][r] = NEG;
        if (!(i <= r32)) sc[8][r] = NEG;
    }
    float mxa = NEG, mxb = NEG;
#pragma unroll
    for (int j = 0; j < 9; ++j)
#pragma unroll
        for (int r = 0; r < 16; r += 4) {
            mxa = __builtin_fmaxf(__builtin_fmaxf(mxa, sc[j][r]), sc[j][r + 1]);
            mxb = __builtin_fmaxf(__builtin_fmaxf(mxb, sc[j][r + 2]), sc[j][r + 3]);
        }
    float mx = fmaxf(mxa, mxb);
    mx = fmaxf(mx, __shfl_xor(mx, 32));
    const float c = 0.17677669529663687f * 1.4426950408889634f;
    const float nm = -mx * c;

    f32x16 o = {}, ls = {};
    const bf16x8 ones = {0x3f80, 0x3f80, 0x3f80, 0x3f80, 0x3f80, 0x3f80, 0x3f80, 0x3f80};
    const int gq = (lane & 15) >> 2, gp = lane & 3, gd = (lane >> 4) & 1;
#pragma unroll
    for (int j = 0; j < 9; ++j) {
        const int kb = qw - 256 + 32 * j;
        if (kb >= 0) {
#pragma unroll
            for (int s = 0; s < 2; ++s) {
                u32x4 pw;
#pragma unroll
                for (int e = 0; e < 4; ++e)
                    pw[e] = pk_bf16(__builtin_amdgcn_exp2f(__builtin_fmaf(sc[j][8 * s + 2 * e], c, nm)),
                                    __builtin_amdgcn_exp2f(__builtin_fmaf(sc[j][8 * s + 2 * e + 1], c, nm)));
                const int key = 32 * (w + j) + 16 * s + 4 * hi + gq;
                LAS const char* vp = ldsV + key * 64 + (16 * gd + 4 * gp) * 2;
                s16x4 lo = tr_read(vp), hi8 = tr_read(vp + 8 * 64);
                const bf16x8 vf = (bf16x8){lo[0], lo[1], lo[2], lo[3], hi8[0], hi8[1], hi8[2], hi8[3]};
                const bf16x8 pf = __builtin_bit_cast(bf16x8, pw);
                o = __builtin_amdgcn_mfma_f32_32x32x16_bf16(vf, pf, o, 0, 0, 0);
                ls = __builtin_amdgcn_mfma_f32_32x32x16_bf16(ones, pf, ls, 0, 0, 0);
            }
        }
    }
    const float rl = 1.0f / ls[0];
    bf16_t* dst = O + (size_t)(qw + r32) * D_MODEL + h * 32 + 4 * hi;
#pragma unroll
    for (int t = 0; t < 4; ++t) {
        u32x2 wv; wv[0] = pk_bf16(o[4 * t] * rl, o[4 * t + 1] * rl); wv[1] = pk_bf16(o[4 * t + 2] * rl, o[4 * t + 3] * rl);
        ST8(dst + 8 * t, wv);
    }
    asm volatile("s_waitcnt vmcnt(0)" :: "v"(pf_dummy) : "memory");
}

template <typename KF> static void set_lds(KF kf, int bytes) {
    (void)hipFuncSetAttribute((const void*)kf, hipFuncAttributeMaxDynamicSharedMemorySize, bytes);
}

extern "C" void kernel_launch(void* const* d_in, const int* in_sizes, int n_in,
                              void* d_out, int out_size, void* d_ws, size_t ws_size,
                              hipStream_t stream) {
    const bf16_t* x = (const bf16_t*)d_in[0];
    const bf16_t* wqkv = (const bf16_t*)d_in[1];
    const bf16_t* wo = (const bf16_t*)d_in[2];
    const bf16_t* w1 = (const bf16_t*)d_in[4];
    const bf16_t* w3 = (const bf16_t*)d_in[5];
    const bf16_t* w2 = (const bf16_t*)d_in[6];
    bf16_t* out = (bf16_t*)d_out;
    char* ws = (char*)d_ws;
    bf16_t* qkv = (bf16_t*)(ws + (4u << 20));
    bf16_t* att = (bf16_t*)(ws + (16u << 20));
    bf16_t* hbuf = (bf16_t*)(ws + (20u << 20));
    bf16_t* act = (bf16_t*)(ws + (28u << 20));
    float* hstat = (float*)(ws + (24u << 20));

    {
        auto kf = gemm_kernel<128, 192, 2, 4, 4, EPI_STORE, 1, 2, 4>;
        constexpr int lds = 4 * (128 + 192) * 128;
        set_lds(kf, lds);
        kf<<<256, 512, lds, stream>>>(x, wqkv, nullptr, qkv, nullptr, S_LEN, 3072, 1024, 3072, 3072);
    }
    {
        set_lds(attn_kernel, 65536);
        attn_kernel<<<256, 512, 65536, stream>>>(qkv, att, wo);
    }
    {
        auto kf = gemm_kernel<128, 64, 4, 2, 6, EPI_RES_STAT, 0, 4, 2>;
        constexpr int lds = 6 * (128 + 64) * 128;
        set_lds(kf, lds);
        kf<<<256, 512, lds, stream>>>(att, wo, x, hbuf, hstat, S_LEN, 1024, 1024, 1024, 1024);
    }
    {
        set_lds(glu_gemm_kernel, 163840);
        glu_gemm_kernel<<<256, 512, 163840, stream>>>(hbuf, w1, w3, hstat, act, w2);
    }
    {
        auto kf = gemm_kernel<128, 64, 4, 2, 6, EPI_RES, 0, 4, 2>;
        constexpr int lds = 6 * (128 + 64) * 128;
        set_lds(kf, lds);
        kf<<<256, 512, lds, stream>>>(act, w2, hbuf, out, nullptr, S_LEN, 1024, F_DIM, 1024, 1024);
    }
}
```

```cpp
#include <hip/hip_runtime.h>
#include <hip/hip_bf16.h>
#include <stdint.h>

typedef unsigned short bf16_t;
typedef short bf16x8 __attribute__((ext_vector_type(8)));
typedef short s16x4 __attribute__((ext_vector_type(4)));
typedef float f32x4 __attribute__((ext_vector_type(4)));
typedef float f32x16 __attribute__((ext_vector_type(16)));
typedef float f32x2 __attribute__((ext_vector_type(2)));
typedef unsigned u32x4 __attribute__((ext_vector_type(4)));
typedef unsigned u32x2 __attribute__((ext_vector_type(2)));
#define LAS __attribute__((address_space(3)))

constexpr int S_LEN = 2048, D_MODEL = 1024, F_DIM = 3584, N_HEAD = 32, D_HEAD = 32, WIN = 256;

__device__ __forceinline__ float bf2f(unsigned v16) { return __uint_as_float(v16 << 16); }
__device__ __forceinline__ unsigned pk_bf16(float lo, float hi) {
    unsigned r; asm("v_cvt_pk_bf16_f32 %0, %1, %2" : "=v"(r) : "v"(lo), "v"(hi)); return r;
}
__device__ __forceinline__ s16x4 tr_read(LAS const char* p) {
    return __builtin_amdgcn_ds_read_tr16_b64_v4i16((LAS s16x4*)p);
}
__device__ __forceinline__ void glds16(const void* sbase, unsigned voff, unsigned lds_addr) {
    unsigned keep;
    asm volatile("s_mov_b32 %0, m0\n\ts_mov_b32 m0, %3\n\ts_nop 0\n\tglobal_load_lds_dwordx4 %2, %1\n\ts_mov_b32 m0, %0"
                 : "=&s"(keep) : "s"(sbase), "v"(voff), "s"(lds_addr) : "memory");
}
typedef __bf16 bf16x2_t __attribute__((ext_vector_type(2)));
__device__ __forceinline__ float dot2(unsigned a, unsigned b, float acc) {
    return __builtin_amdgcn_fdot2_f32_bf16(__builtin_bit_cast(bf16x2_t, a), __builtin_bit_cast(bf16x2_t, b), acc, false);
}
__device__ __forceinline__ void frag_sum(const bf16x8& f, float& s1) {
    const u32x4 u = __builtin_bit_cast(u32x4, f);
#pragma unroll
    for (int e = 0; e < 4; ++e) s1 = dot2(u[e], 0x3f803f80u, s1);
}
__device__ __forceinline__ void frag_sum_sq(const bf16x8& f, float& s1, float& s2) {
    const u32x4 u = __builtin_bit_cast(u32x4, f);
#pragma unroll
    for (int e = 0; e < 4; ++e) { s1 = dot2(u[e], 0x3f803f80u, s1); s2 = dot2(u[e], u[e], s2); }
}
__device__ __forceinline__ float red_g(float v) { v += __shfl_xor(v, 16); v += __shfl_xor(v, 32); return v; }
__device__ __forceinline__ void prefetch_region(const char* base, int lpr, long pitch, int chunk, int part, int tid,
                                                int nthreads, unsigned& dummy) {
    for (int l = tid; l < chunk; l += nthreads) {
        const int L = part * chunk + l, row = L / lpr, c = L - row * lpr;
        const char* a = base + (long)row * pitch + c * 128;
        asm volatile("global_load_dword %0, %1, off" : "+v"(dummy) : "v"(a) : "memory");
    }
}
#define ST8(ptr, v) (*(u32x2*)(ptr) = (v))
__device__ __forceinline__ void st16_sc1(void* p, u32x4 v) { asm volatile("global_store_dwordx4 %0, %1, off sc1\n\ts_nop 1" :: "v"(p), "v"(v) : "memory"); }
#define ST16(ptr, v) st16_sc1((void*)(ptr), (v))
template <int N> __device__ __forceinline__ void wait_vm() { asm volatile("s_waitcnt vmcnt(%0)" ::"n"(N) : "memory"); }

#define G4RM 4
#define G4RN 2
#define G1RM 2
#define G1RN 4
#define NOCS 0
enum { EPI_STORE = 0, EPI_RES = 1, EPI_RES_STAT = 2 };

template <int BM, int BN, int WM, int WN, int NST, int EPI, int LNF, int RM, int RN>
__global__ __launch_bounds__(WM * WN * 64) void gemm_kernel(
    const bf16_t* __restrict__ X, const bf16_t* __restrict__ W0,
    const bf16_t* __restrict__ R, bf16_t* __restrict__ C, float* __restrict__ stat_out,
    int M, int N, int K, int ldw, int ldc) {
    constexpr int NWAVE = WM * WN;
    constexpr int MF = BM / 16 / WM, NF = BN / 16 / WN, NB = BN / 16;
    constexpr int A_BYTES = BM * 128, B_BYTES = BN * 128, ST_BYTES = A_BYTES + B_BYTES;
    constexpr int LPW = (BM + BN) / 8 / NWAVE;
    constexpr int LPA = BM / 8 / NWAVE;
    static_assert((BM + BN) % (8 * NWAVE) == 0 && (BM % (8 * NWAVE)) == 0, "piece split");
    static_assert(NB % 4 == 0, "NB%4");
    static_assert(!LNF || (MF == WN && WM == 2), "LN fold layout");
    static_assert(EPI != EPI_STORE || (BM * 4 == NWAVE * 64), "head-major store layout");
    extern __shared__ __attribute__((aligned(16))) char smem[];
    LAS char* lds = (LAS char*)smem;

    const int tid = threadIdx.x, lane = tid & 63;
    const int wave = __builtin_amdgcn_readfirstlane(tid >> 6);
    const int wm = wave / WN, wn = wave % WN;

    const int tiles_m = M / BM, tiles_n = N / BN;
    int tm, tn;
    {
        const int xcd = blockIdx.x & 7, idx = blockIdx.x >> 3;
        const int rm = xcd / RN, rn = xcd % RN;
        const int sm = tiles_m / RM, sn = tiles_n / RN;
        tm = rm * sm + idx % sm; tn = rn * sn + idx / sm;
    }
    const int m0 = tm * BM, n0 = tn * BN;
    unsigned pf_dummy = 0;

    unsigned voff[LPW];
    const char* sbase[LPW];
#pragma unroll
    for (int j = 0; j < LPW; ++j) {
        const int ii = wave + NWAVE * j;
        if (j < LPA) {
            const int row = ii * 8 + (lane >> 3), ch = (lane & 7) ^ (row & 7);
            voff[j] = (unsigned)(row * K * 2 + ch * 16);
            sbase[j] = (const char*)(X + (size_t)m0 * K);
        } else {
            const int pi = ii - BM / 8;
            const int kb = pi / (NB / 4), nb = 4 * (pi % (NB / 4)) + (lane >> 4);
            const int r = ((lane & 15) >> 1) ^ (4 * (kb & 1)), half = lane & 1;
            voff[j] = (unsigned)(((8 * kb + r) * ldw + 16 * nb + 8 * half) * 2);
            sbase[j] = (const char*)(W0 + n0);
        }
    }
    const unsigned lds_base = (unsigned)(uintptr_t)lds;
    const int NT = K / 64;
    auto issue = [&](int buf) {
#pragma unroll
        for (int j = 0; j < LPW; ++j) {
            const int ii = wave + NWAVE * j;
            glds16(sbase[j], voff[j], lds_base + buf * ST_BYTES + ii * 1024);
            sbase[j] += (j < LPA) ? 128 : (long)128 * ldw;
        }
    };

    const int fr = lane & 15, g = lane >> 4, q = (lane & 15) >> 2, p = lane & 3;
    const int rotm = LNF ? wn : 0, rotk = LNF ? wm : 0;
    const int xrow = wm * (MF * 16) + fr;
    const int xoff0 = xrow * 128 + ((g ^ (xrow & 7)) << 4);
    const int woff0 = A_BYTES + g * NB * 256 + (q + 4 * (g & 1)) * 32 + p * 8 + wn * NF * 256;

    f32x4 acc[MF][NF];
#pragma unroll
    for (int i = 0; i < MF; ++i)
#pragma unroll
        for (int j = 0; j < NF; ++j) acc[i][j] = (f32x4){0.f, 0.f, 0.f, 0.f};
    f32x4 s1 = {0.f, 0.f, 0.f, 0.f}, s2 = {0.f, 0.f, 0.f, 0.f}, cs[NF];
#pragma unroll
    for (int j = 0; j < NF; ++j) cs[j] = (f32x4){0.f, 0.f, 0.f, 0.f};
    const bf16x8 ones = {0x3f80, 0x3f80, 0x3f80, 0x3f80, 0x3f80, 0x3f80, 0x3f80, 0x3f80};

#pragma unroll
    for (int s = 0; s < NST - 1; ++s) issue(s);

    auto read_tile = [&](LAS const char* base, bf16x8 (&xf)[2][MF], bf16x8 (&wf)[2][NF]) {
#pragma unroll
        for (int slot = 0; slot < 2; ++slot) {
            const int ks = slot ^ rotk;
#pragma unroll
            for (int j = 0; j < NF; ++j) {
                const int o = ks * (4 * NB * 256) + j * 256;
                s16x4 lo = tr_read(base + woff0 + o);
                s16x4 hi = tr_read(base + (woff0 ^ 128) + o);
                wf[slot][j] = (bf16x8){lo[0], lo[1], lo[2], lo[3], hi[0], hi[1], hi[2], hi[3]};
            }
#pragma unroll
            for (int i = 0; i < MF; ++i)
                xf[slot][i] = *(LAS const bf16x8*)(base + ((xoff0 ^ (ks * 64)) + (i ^ rotm) * 2048));
        }
    };
    auto do_mfma = [&](bf16x8 (&xf)[2][MF], bf16x8 (&wf)[2][NF]) {
#pragma unroll
        for (int slot = 0; slot < 2; ++slot) {
#pragma unroll
            for (int i = 0; i < MF; ++i)
#pragma unroll
                for (int j = 0; j < NF; ++j)
                    acc[i][j] = __builtin_amdgcn_mfma_f32_16x16x32_bf16(wf[slot][j], xf[slot][i], acc[i][j], 0, 0, 0);
            if (LNF) {
                s1 = __builtin_amdgcn_mfma_f32_16x16x32_bf16(ones, xf[slot][0], s1, 0, 0, 0);
                s2 = __builtin_amdgcn_mfma_f32_16x16x32_bf16(xf[slot][0], xf[slot][0], s2, 0, 0, 0);
                if (slot == 0) {
#pragma unroll
                    for (int j = 0; j < NF; ++j) cs[j] = __builtin_amdgcn_mfma_f32_16x16x32_bf16(wf[0][j], ones, cs[j], 0, 0, 0);
                }
            }
        }
    };
    auto interleave = [&]() {
        constexpr int NM = 2 * MF * NF + (LNF ? 4 + NF : 0), NR = 2 * (MF + 2 * NF), RPM = (NR + NM - 1) / NM;
#pragma unroll
        for (int i = 0; i < NM; ++i) {
            __builtin_amdgcn_sched_group_barrier(0x8, 1, 0);
            __builtin_amdgcn_sched_group_barrier(0x100, RPM, 0);
        }
    };
    bf16x8 xa[2][MF], wa[2][NF], xb[2][MF], wb[2][NF];
    wait_vm<(NST - 2) * LPW>();
    __builtin_amdgcn_s_barrier();
    asm volatile("" ::: "memory");
    read_tile(lds, xa, wa);

    int buf = 0, pbuf = NST - 1;
    auto step = [&](int kt, bf16x8 (&xc)[2][MF], bf16x8 (&wc)[2][NF], bf16x8 (&xn)[2][MF], bf16x8 (&wn2)[2][NF]) {
        const int rem = NT - 2 - kt;
        if (rem >= NST - 3) wait_vm<(NST - 3) * LPW>();
        else if (NST > 4 && rem == 1) wait_vm<LPW>();
        else if (NST > 5 && rem == 2) wait_vm<2 * LPW>();
        else wait_vm<0>();
        __builtin_amdgcn_s_barrier();
        asm volatile("" ::: "memory");
        if (kt + NST - 1 < NT) issue(pbuf);
        pbuf = buf;
        buf = (buf + 1 == NST) ? 0 : buf + 1;
        __builtin_amdgcn_sched_barrier(0);
        read_tile(lds + buf * ST_BYTES, xn, wn2);
        do_mfma(xc, wc);
        interleave();
        __builtin_amdgcn_sched_barrier(0);
    };
    for (int kt = 0; kt < NT; kt += 2) {
        step(kt, xa, wa, xb, wb);
        step(kt + 1, xb, wb, xa, wa);
    }

    asm volatile("" :: "v"(pf_dummy));
    constexpr int NTHR = NWAVE * 64, CPR = BN / 8, NCH = BM * CPR / NTHR;
    constexpr int RS = (EPI == EPI_STORE) ? BN * 2 + 16 : BN * 4 + 16;
    LAS float* rowstat = (LAS float*)(lds + BM * RS);
    LAS float* colsum = rowstat + BM * 2;
    u32x4 rres[NCH];
    if (EPI != EPI_STORE) {
#pragma unroll
        for (int r = 0; r < NCH; ++r) {
            const int c = tid + NTHR * r, row = c / CPR, c8 = c % CPR;
            rres[r] = *(const u32x4*)(R + (size_t)(m0 + row) * ldc + n0 + c8 * 8);
        }
    }
    __syncthreads();
    if (LNF) {
        if (g == (fr >> 2)) {
            const int r = wm * (MF * 16) + wn * 16 + fr;
            const int d = fr & 3;
            rowstat[2 * r] = s1[0];
            rowstat[2 * r + 1] = d == 0 ? s2[0] : d == 1 ? s2[1] : d == 2 ? s2[2] : s2[3];
        }
        if (fr == 0) {
#pragma unroll
            for (int j = 0; j < NF; ++j) *(LAS f32x4*)(colsum + wm * BN + (wn * NF + j) * 16 + 4 * g) = cs[j];
        }
        __syncthreads();
    }
    f32x4 csv[NF];
    if (LNF) {
#pragma unroll
        for (int j = 0; j < NF; ++j) {
            const int c = (wn * NF + j) * 16 + 4 * g;
            csv[j] = *(LAS const f32x4*)(colsum + c) + *(LAS const f32x4*)(colsum + BN + c);
        }
    }
#pragma unroll
    for (int i = 0; i < MF; ++i) {
        const int rl = wm * (MF * 16) + (i ^ rotm) * 16 + fr;
        float mean = 0.f, rstd = 1.f;
        if (LNF) {
            const float a = rowstat[2 * rl], b = rowstat[2 * rl + 1];
            mean = a * (1.0f / D_MODEL);
            const float var = fmaxf(b * (1.0f / D_MODEL) - mean * mean, 0.f);
            rstd = 1.0f / sqrtf(var + 1e-5f);
        }
#pragma unroll
        for (int j = 0; j < NF; ++j) {
            const int nl = (wn * NF + j) * 16 + 4 * g;
            f32x4 v = acc[i][j];
            if (LNF) v = (v - csv[j] * mean) * rstd;
            if (EPI == EPI_STORE) {
                u32x2 w; w[0] = pk_bf16(v[0], v[1]); w[1] = pk_bf16(v[2], v[3]);
                *(LAS u32x2*)(lds + rl * RS + nl * 2) = w;
            } else {
                *(LAS f32x4*)(lds + rl * RS + nl * 4) = v;
            }
        }
    }
    __syncthreads();
#pragma unroll
    for (int r = 0; r < NCH; ++r) {
        const int c = tid + NTHR * r, row = c / CPR, c8 = c % CPR;
        u32x4 w;
        if (EPI == EPI_STORE) {
            const int hrow = tid >> 2, d8 = tid & 3;
            w = *(LAS const u32x4*)(lds + hrow * RS + r * 64 + d8 * 16);
            ST16(C + ((size_t)(n0 / 32 + r) * M + m0 + hrow) * 32 + d8 * 8, w);
            continue;
        } else {
            f32x4 v0 = *(LAS const f32x4*)(lds + row * RS + c8 * 32), v1 = *(LAS const f32x4*)(lds + row * RS + c8 * 32 + 16);
            const u32x4 rv = rres[r];
            v0[0] += bf2f(rv[0] & 0xffffu); v0[1] += bf2f(rv[0] >> 16); v0[2] += bf2f(rv[1] & 0xffffu); v0[3] += bf2f(rv[1] >> 16);
            v1[0] += bf2f(rv[2] & 0xffffu); v1[1] += bf2f(rv[2] >> 16); v1[2] += bf2f(rv[3] & 0xffffu); v1[3] += bf2f(rv[3] >> 16);
            w[0] = pk_bf16(v0[0], v0[1]); w[1] = pk_bf16(v0[2], v0[3]); w[2] = pk_bf16(v1[0], v1[1]); w[3] = pk_bf16(v1[2], v1[3]);
        }
        ST16(C + (size_t)(m0 + row) * ldc + n0 + c8 * 8, w);
        if (EPI == EPI_RES_STAT) {
            float a = 0.f, b = 0.f;
#pragma unroll
            for (int e = 0; e < 4; ++e) {
                const float x0 = bf2f(w[e] & 0xffffu), x1 = bf2f(w[e] >> 16);
                a += x0 + x1; b += x0 * x0 + x1 * x1;
            }
#pragma unroll
            for (int o = 1; o < CPR; o <<= 1) { a += __shfl_xor(a, o); b += __shfl_xor(b, o); }
            if (c8 == 0) { float2 o2; o2.x = a; o2.y = b; *(float2*)(stat_out + ((size_t)(m0 + row) * tiles_n + tn) * 2) = o2; }
        }
    }
}

__global__ __launch_bounds__(512, 2) void glu_gemm_kernel(
    const bf16_t* __restrict__ X, const bf16_t* __restrict__ W1, const bf16_t* __restrict__ W3,
    const float* __restrict__ stat_in, bf16_t* __restrict__ C, const bf16_t* __restrict__ pfw) {
    constexpr int K = D_MODEL, LDW = F_DIM, HTB = 16384;
    if (blockIdx.x >= 224) {
        u32x4 d = {0u, 0u, 0u, 0u};
        const char* a = (const char*)pfw + (size_t)(blockIdx.x - 224) * 229376 + threadIdx.x * 16;
#pragma unroll
        for (int r = 0; r < 28; ++r) asm volatile("global_load_dwordx4 %0, %1, off offset:0" : "+v"(d) : "v"(a + r * 8192) : "memory");
        asm volatile("s_waitcnt vmcnt(0)" :: "v"(d) : "memory");
        return;
    }
    extern __shared__ __attribute__((aligned(16))) char smem[];
    LAS char* lds = (LAS char*)smem;
    const unsigned lds_base = (unsigned)(uintptr_t)lds;
    const int tid = threadIdx.x, lane = tid & 63;
    const int wave = __builtin_amdgcn_readfirstlane(tid >> 6);
    const int wr = wave >> 2, wc = wave & 3;
    int tm, tn;
    { const int xcd = blockIdx.x & 7, idx = blockIdx.x >> 3; tm = (xcd >> 2) * 4 + (idx & 3); tn = (xcd & 3) * 7 + (idx >> 2); }
    const int m0 = tm * 256, n0 = tn * 128;
    unsigned pf_dummy = 0;

    unsigned xoffs[2], woffs[2];
#pragma unroll
    for (int j = 0; j < 2; ++j) {
        const int pc = wave + 8 * j;
        const int row = pc * 8 + (lane >> 3), ch = (lane & 7) ^ (row & 7);
        xoffs[j] = (unsigned)(row * K * 2 + ch * 16);
        const int kb = pc >> 1, nb = 4 * (pc & 1) + (lane >> 4);
        const int r = ((lane & 15) >> 1) ^ (4 * (kb & 1)), half = lane & 1;
        woffs[j] = (unsigned)(((8 * kb + r) * LDW + 16 * nb + 8 * half) * 2);
    }
    const char* xsrc = (const char*)(X + (size_t)m0 * K);
    const char* w1src = (const char*)(W1 + n0);
    const char* w3src = (const char*)(W3 + n0);
#define SA_OFF(b, h) (((b) * 2 + (h)) * HTB)
#define SB_OFF(b, h) ((4 + (b) * 2 + (h)) * HTB)
#define STAGE_A(b, h, kt) do { const char* _s = xsrc + (size_t)(h) * 128 * K * 2 + (kt) * 128; \
        glds16(_s, xoffs[0], lds_base + SA_OFF(b, h) + wave * 1024); glds16(_s, xoffs[1], lds_base + SA_OFF(b, h) + (wave + 8) * 1024); } while (0)
#define STAGE_B(b, h, kt) do { const char* _s = ((h) ? w3src : w1src) + (size_t)(kt) * 64 * LDW * 2; \
        glds16(_s, woffs[0], lds_base + SB_OFF(b, h) + wave * 1024); glds16(_s, woffs[1], lds_base + SB_OFF(b, h) + (wave + 8) * 1024); } while (0)
    const int fr = lane & 15, g = lane >> 4, q = (lane & 15) >> 2, p = lane & 3;
    const int xrow = wr * 64 + fr;
    const int xo0 = (xrow * 128 + ((g ^ (xrow & 7)) << 4)) ^ (wr * 64);
    const int wo0 = g * 2048 + (q + 4 * (g & 1)) * 32 + p * 8 + wc * 512;
    unsigned xb_[4][2], wb_[2][2];
#pragma unroll
    for (int m = 0; m < 4; ++m)
#pragma unroll
        for (int k = 0; k < 2; ++k) { xb_[m][k] = lds_base + ((xo0 ^ (k * 64)) + m * 2048); asm volatile("" : "+v"(xb_[m][k])); }
#pragma unroll
    for (int t = 0; t < 2; ++t)
#pragma unroll
        for (int k = 0; k < 2; ++k) { wb_[t][k] = lds_base + 4 * HTB + ((wo0 ^ (t * 128)) + (k ^ wr) * 8192); asm volatile("" : "+v"(wb_[t][k])); }
#define LDSP(a) ((LAS const char*)(uintptr_t)(a))
#define LDA(dst, b, h) do { _Pragma("unroll") for (int m = 0; m < 4; ++m) _Pragma("unroll") for (int k = 0; k < 2; ++k) \
        dst[m][k] = *(LAS const bf16x8*)(LDSP(xb_[m][k]) + SA_OFF(b, h)); } while (0)
#define LDB(dst, b, h) do { _Pragma("unroll") for (int n = 0; n < 2; ++n) _Pragma("unroll") for (int k = 0; k < 2; ++k) { \
        s16x4 lo_ = tr_read(LDSP(wb_[0][k]) + SA_OFF(b, h) + n * 256); \
        s16x4 hi_ = tr_read(LDSP(wb_[1][k]) + SA_OFF(b, h) + n * 256); \
        dst[n][k] = (bf16x8){lo_[0], lo_[1], lo_[2], lo_[3], hi_[0], hi_[1], hi_[2], hi_[3]}; } } while (0)
#define MMA(ai, bj, At, Bt, CS) do { __builtin_amdgcn_s_setprio(1); \
        _Pragma("unroll") for (int m = 0; m < 4; ++m) _Pragma("unroll") for (int n = 0; n < 2; ++n) _Pragma("unroll") for (int k = 0; k < 2; ++k) \
            acc[ai][bj][m][n] = __builtin_amdgcn_mfma_f32_16x16x32_bf16(Bt[n][k], At[m][k], acc[ai][bj][m][n], 0, 0, 0); \
        if (((CS) & 2) && !NOCS) { cs[bj][0] = __builtin_amdgcn_mfma_f32_16x16x32_bf16(Bt[0][0], ones, cs[bj][0], 0, 0, 0); \
                        cs[bj][1] = __builtin_amdgcn_mfma_f32_16x16x32_bf16(Bt[1][0], ones, cs[bj][1], 0, 0, 0); } \
        __builtin_amdgcn_s_setprio(0); } while (0)
#define WAIT_V(n) asm volatile("s_waitcnt vmcnt(" #n ")" ::: "memory")
#define WAIT_L(n) asm volatile("s_waitcnt lgkmcnt(" #n ")" ::: "memory")
#define BAR __builtin_amdgcn_s_barrier()
#define SCHED __builtin_amdgcn_sched_barrier(0)
    f32x4 acc[2][2][4][2] = {};
    bf16x8 At[4][2], B0[2][2], B1[2][2];
    f32x4 cs[2][2] = {};
    const bf16x8 ones = {0x3f80, 0x3f80, 0x3f80, 0x3f80, 0x3f80, 0x3f80, 0x3f80, 0x3f80};
    constexpr int nt = K / 64;
    {
        const char* ssrc = (const char*)(stat_in + (size_t)m0 * 32);
#pragma unroll
        for (int j = 0; j < 4; ++j) glds16(ssrc, (unsigned)((wave * 4 + j) * 1024 + lane * 16), lds_base + 8 * HTB + (wave * 4 + j) * 1024);
    }
    STAGE_B(0, 0, 0); STAGE_A(0, 0, 0); STAGE_B(0, 1, 0); STAGE_A(0, 1, 0);
    if (wr == 1) BAR;
    WAIT_V(4); BAR;
    STAGE_B(1, 0, 1); STAGE_A(1, 0, 1); STAGE_B(1, 1, 1);
    WAIT_V(6); BAR;
    for (int t = 0; t < nt; t += 2) {
        const bool more = t + 2 < nt;
        LDB(B0, 0, 0); SCHED; LDA(At, 0, 0); STAGE_A(1, 1, t + 1);
        WAIT_L(8); BAR; WAIT_L(0); MMA(0, 0, At, B0, 2); BAR; SCHED;
        LDB(B1, 0, 1); if (more) STAGE_B(0, 0, t + 2);
        BAR; WAIT_L(0); MMA(0, 1, At, B1, 1); BAR;
        LDA(At, 0, 1); if (more) STAGE_A(0, 0, t + 2);
        BAR; WAIT_L(0); MMA(1, 0, At, B0, 1); BAR; SCHED;
        if (more) { STAGE_B(0, 1, t + 2); WAIT_V(6); } else { WAIT_V(0); }
        BAR; MMA(1, 1, At, B1, 2); BAR;
        LDB(B0, 1, 0); SCHED; LDA(At, 1, 0); if (more) STAGE_A(0, 1, t + 2);
        WAIT_L(8); BAR; WAIT_L(0); MMA(0, 0, At, B0, 2); BAR; SCHED;
        LDB(B1, 1, 1); if (more) STAGE_B(1, 0, t + 3);
        BAR; WAIT_L(0); MMA(0, 1, At, B1, 1); BAR;
        LDA(At, 1, 1); if (more) STAGE_A(1, 0, t + 3);
        BAR; WAIT_L(0); MMA(1, 0, At, B0, 1); BAR; SCHED;
        if (more) { STAGE_B(1, 1, t + 3); WAIT_V(6); }
        BAR; MMA(1, 1, At, B1, 2); BAR;
    }
    if (wr == 0) BAR;
    asm volatile("" :: "v"(pf_dummy));
    constexpr int RS = 272;
    LAS float* rowstat = (LAS float*)(lds + 256 * RS);
    LAS float* colsum = rowstat + 512;
    if (fr == 0) {
#pragma unroll
        for (int b = 0; b < 2; ++b)
#pragma unroll
            for (int n = 0; n < 2; ++n) *(LAS f32x4*)(colsum + wr * 256 + b * 128 + wc * 32 + n * 16 + 4 * g) = cs[b][n];
    }
    if (tid < 256) {
        LAS const f32x4* sp = (LAS const f32x4*)(lds + 8 * HTB + tid * 128);
        f32x4 t = sp[tid & 7];
#pragma unroll
        for (int e = 1; e < 8; ++e) t += sp[e ^ (tid & 7)];
        const float mean = (t[0] + t[2]) * (1.0f / D_MODEL);
        const float rstd = 1.0f / sqrtf(fmaxf((t[1] + t[3]) * (1.0f / D_MODEL) - mean * mean, 0.f) + 1e-5f);
        rowstat[2 * tid] = mean * rstd; rowstat[2 * tid + 1] = rstd;
    }
    __syncthreads();
    f32x4 csv[2][2];
#pragma unroll
    for (int b = 0; b < 2; ++b)
#pragma unroll
        for (int n = 0; n < 2; ++n) {
            const int c = b * 128 + wc * 32 + n * 16 + 4 * g;
            csv[b][n] = *(LAS const f32x4*)(colsum + c) + *(LAS const f32x4*)(colsum + 256 + c);
        }
#pragma unroll
    for (int ai = 0; ai < 2; ++ai) {
#pragma unroll
        for (int m = 0; m < 4; ++m) {
            const int rl = ai * 128 + wr * 64 + m * 16 + fr;
            const float mr = rowstat[2 * rl], rstd = rowstat[2 * rl + 1];
#pragma unroll
            for (int n = 0; n < 2; ++n) {
                u32x2 w;
#pragma unroll
                for (int hh = 0; hh < 2; ++hh) {
                    const f32x2 ag = {acc[ai][0][m][n][2 * hh], acc[ai][0][m][n][2 * hh + 1]};
                    const f32x2 au = {acc[ai][1][m][n][2 * hh], acc[ai][1][m][n][2 * hh + 1]};
                    const f32x2 cg = {csv[0][n][2 * hh], csv[0][n][2 * hh + 1]};
                    const f32x2 cu = {csv[1][n][2 * hh], csv[1][n][2 * hh + 1]};
                    const f32x2 gv = ag * rstd - cg * mr;
                    const f32x2 uv = au * rstd - cu * mr;
                    const f32x2 t = gv * (-1.4426950408889634f);
                    f32x2 e; e.x = __builtin_amdgcn_exp2f(t.x); e.y = __builtin_amdgcn_exp2f(t.y);
                    const f32x2 d = e + 1.0f;
                    f32x2 rr; rr.x = __builtin_amdgcn_rcpf(d.x); rr.y = __builtin_amdgcn_rcpf(d.y);
                    const f32x2 o = (gv * uv) * rr;
                    w[hh] = pk_bf16(o.x, o.y);
                }
                *(LAS u32x2*)(lds + rl * RS + (wc * 32 + n * 16 + 4 * g) * 2) = w;
            }
        }
        asm volatile("s_waitcnt lgkmcnt(0)" ::: "memory");
        __builtin_amdgcn_s_barrier();
        asm volatile("" ::: "memory");
#pragma unroll
        for (int r = 0; r < 4; ++r) {
            const int c = tid + 512 * r, row = ai * 128 + (c >> 4), c8 = c & 15;
            ST16(C + (size_t)(m0 + row) * LDW + n0 + c8 * 8, *(LAS const u32x4*)(lds + row * RS + c8 * 16));
        }
        __builtin_amdgcn_sched_barrier(0);
    }
#undef SA_OFF
#undef SB_OFF
#undef STAGE_A
#undef STAGE_B
#undef LDA
#undef LDB
#undef MMA
}

template <int NST>
__global__ __launch_bounds__(512, 2) void glu2_kernel(
    const bf16_t* __restrict__ X, const bf16_t* __restrict__ W1, const bf16_t* __restrict__ W3,
    const float* __restrict__ stat_in, bf16_t* __restrict__ C) {
    constexpr int K = D_MODEL, LDW = F_DIM, STB = 32768, NT = K / 32;
    extern __shared__ __attribute__((aligned(16))) char smem[];
    LAS char* lds = (LAS char*)smem;
    const unsigned lds_base = (unsigned)(uintptr_t)lds;
    const int tid = threadIdx.x, lane = tid & 63;
    const int wave = __builtin_amdgcn_readfirstlane(tid >> 6);
    const int wm = wave >> 2, wn = wave & 3;
    int tm, tn;
    { const int xcd = blockIdx.x & 7, idx = blockIdx.x >> 3; tm = (xcd >> 2) * 4 + (idx & 3); tn = (xcd & 3) * 7 + (idx >> 2); }
    const int m0 = tm * 256, n0 = tn * 128;
    unsigned pf_dummy = 0;

    unsigned xoffs[2], woffs[2];
#pragma unroll
    for (int j = 0; j < 2; ++j) {
        const int pc = wave + 8 * j;
        const int row = pc * 16 + (lane >> 2), ch = (lane & 3) ^ ((4 - ((row >> 2) & 3)) & 3);
        xoffs[j] = (unsigned)(row * K * 2 + ch * 16);
        const int kb = pc >> 2, nb = 4 * (pc & 1) + (lane >> 4);
        const int r = ((lane & 15) >> 1) ^ (4 * (kb & 1)), half = lane & 1;
        woffs[j] = (unsigned)(((8 * kb + r) * LDW + 16 * nb + 8 * half) * 2);
    }
    const char* xsrc = (const char*)(X + (size_t)m0 * K);
    const char* wsrc = (const char*)(((wave & 2) ? W3 : W1) + n0);
    auto issue = [&](int buf) {
        const unsigned b = lds_base + buf * STB;
        glds16(xsrc, xoffs[0], b + wave * 1024);
        glds16(xsrc, xoffs[1], b + (wave + 8) * 1024);
        glds16(wsrc, woffs[0], b + 16384 + wave * 1024);
        glds16(wsrc, woffs[1], b + 16384 + (wave + 8) * 1024);
        xsrc += 64; wsrc += (size_t)32 * LDW * 2;
    };
    const int fr = lane & 15, g = lane >> 4, q = (lane & 15) >> 2, p = lane & 3;
    const int xo0 = (wm * 128 + fr) * 64 + ((g ^ ((4 - (fr >> 2)) & 3)) << 4);
    const int wo0 = 16384 + g * 4096 + (q + 4 * (g & 1)) * 32 + p * 8 + wn * 512;

    f32x4 acc[8][4];
#pragma unroll
    for (int i = 0; i < 8; ++i)
#pragma unroll
        for (int j = 0; j < 4; ++j) acc[i][j] = (f32x4){0.f, 0.f, 0.f, 0.f};
    f32x4 cs[4] = {};
    const bf16x8 ones = {0x3f80, 0x3f80, 0x3f80, 0x3f80, 0x3f80, 0x3f80, 0x3f80, 0x3f80};

    {
        const char* ssrc = (const char*)(stat_in + (size_t)m0 * 32);
#pragma unroll
        for (int j = 0; j < 4; ++j) glds16(ssrc, (unsigned)((wave * 4 + j) * 1024 + lane * 16), lds_base + NST * STB + (wave * 4 + j) * 1024);
    }
#pragma unroll
    for (int s = 0; s < NST; ++s) issue(s);

    auto read_frags = [&](LAS const char* base, bf16x8 (&xf)[8], bf16x8 (&wf)[4]) {
#pragma unroll
        for (int j = 0; j < 4; ++j) {
            const int o = (j & 1) * 256 + (j >> 1) * 2048;
            s16x4 lo = tr_read(base + wo0 + o);
            s16x4 hi = tr_read(base + (wo0 ^ 128) + o);
            wf[j] = (bf16x8){lo[0], lo[1], lo[2], lo[3], hi[0], hi[1], hi[2], hi[3]};
        }
#pragma unroll
        for (int i = 0; i < 8; ++i) xf[i] = *(LAS const bf16x8*)(base + xo0 + i * 1024);
    };
    auto do_mfma = [&](bf16x8 (&xf)[8], bf16x8 (&wf)[4]) {
#pragma unroll
        for (int i = 0; i < 8; ++i)
#pragma unroll
            for (int j = 0; j < 4; ++j)
                acc[i][j] = __builtin_amdgcn_mfma_f32_16x16x32_bf16(wf[j], xf[i], acc[i][j], 0, 0, 0);
    };
    auto interleave = [&]() {
#pragma unroll
        for (int i = 0; i < 16; ++i) {
            __builtin_amdgcn_sched_group_barrier(0x8, 2, 0);
            __builtin_amdgcn_sched_group_barrier(0x100, 1, 0);
        }
    };
    auto colsum = [&](bf16x8 (&wf)[4]) {
#pragma unroll
        for (int j = 0; j < 4; ++j) cs[j] = __builtin_amdgcn_mfma_f32_16x16x32_bf16(wf[j], ones, cs[j], 0, 0, 0);
    };
    bf16x8 xa[8], wa[4], xb[8], wb[4];
    wait_vm<(NST - 1) * 4>();
    __builtin_amdgcn_s_barrier();
    asm volatile("" ::: "memory");
    read_frags(lds, xa, wa);
    int buf = 0;
    auto step = [&](int kt, bf16x8 (&xc)[8], bf16x8 (&wc_)[4], bf16x8 (&xn)[8], bf16x8 (&wn_)[4], bool docs) {
        const int rem = NT - 2 - kt;
        if (rem >= NST - 2) wait_vm<(NST - 2) * 4>();
        else if (rem == 1) wait_vm<4>();
        else if (NST > 4 && rem == 2) wait_vm<8>();
        else wait_vm<0>();
        asm volatile("s_waitcnt lgkmcnt(0)" ::: "memory");
        __builtin_amdgcn_s_barrier();
        asm volatile("" ::: "memory");
        if (kt + NST < NT) issue(buf);
        buf = (buf + 1 == NST) ? 0 : buf + 1;
        __builtin_amdgcn_sched_barrier(0);
        read_frags(lds + buf * STB, xn, wn_);
        do_mfma(xc, wc_);
        interleave();
        __builtin_amdgcn_sched_barrier(0);
        if (docs) colsum(wc_);
        __builtin_amdgcn_sched_barrier(0);
    };
    for (int kt = 0; kt < NT; kt += 2) {
        step(kt, xa, wa, xb, wb, wm == 0);
        step(kt + 1, xb, wb, xa, wa, wm == 1);
    }
    asm volatile("" :: "v"(pf_dummy));
    __syncthreads();
    LAS float* rowstat = (LAS float*)lds;
    LAS float* colsm = (LAS float*)(lds + 2048);
    if (fr == 0) {
#pragma unroll
        for (int j = 0; j < 4; ++j) *(LAS f32x4*)(colsm + wm * 256 + (j >> 1) * 128 + wn * 32 + (j & 1) * 16 + 4 * g) = cs[j];
    }
    if (tid < 256) {
        LAS const f32x4* sp = (LAS const f32x4*)(lds + NST * STB + tid * 128);
        f32x4 t = sp[tid & 7];
#pragma unroll
        for (int e = 1; e < 8; ++e) t += sp[e ^ (tid & 7)];
        const float mean = (t[0] + t[2]) * (1.0f / D_MODEL);
        const float rstd = 1.0f / sqrtf(fmaxf((t[1] + t[3]) * (1.0f / D_MODEL) - mean * mean, 0.f) + 1e-5f);
        rowstat[2 * tid] = mean; rowstat[2 * tid + 1] = rstd;
    }
    __syncthreads();
    f32x4 csv[4];
#pragma unroll
    for (int j = 0; j < 4; ++j) {
        const int c = (j >> 1) * 128 + wn * 32 + (j & 1) * 16 + 4 * g;
        csv[j] = *(LAS const f32x4*)(colsm + c) + *(LAS const f32x4*)(colsm + 256 + c);
    }
#pragma unroll
    for (int i = 0; i < 8; ++i) {
        const int rl = wm * 128 + i * 16 + fr;
        const float mean = rowstat[2 * rl], rstd = rowstat[2 * rl + 1];
#pragma unroll
        for (int n = 0; n < 2; ++n) {
            const f32x4 gv = (acc[i][n] - csv[n] * mean) * rstd;
            const f32x4 uv = (acc[i][n + 2] - csv[n + 2] * mean) * rstd;
            float o[4];
#pragma unroll
            for (int r = 0; r < 4; ++r) o[r] = gv[r] * __builtin_amdgcn_rcpf(1.0f + __expf(-gv[r])) * uv[r];
            u32x2 w; w[0] = pk_bf16(o[0], o[1]); w[1] = pk_bf16(o[2], o[3]);
            *(u32x2*)(C + (size_t)(m0 + rl) * LDW + n0 + wn * 32 + n * 16 + 4 * g) = w;
        }
    }
}

__global__ __launch_bounds__(512) void attn_kernel(const bf16_t* __restrict__ qkv, bf16_t* __restrict__ O,
                                                   const bf16_t* __restrict__ pf0) {
    extern __shared__ __attribute__((aligned(16))) char smem[];
    LAS char* lds = (LAS char*)smem;
    LAS char* ldsK = lds;
    LAS char* ldsV = lds + 32768;
    const unsigned lds_base = (unsigned)(uintptr_t)lds;
    const int tid = threadIdx.x, lane = tid & 63;
    const int w = __builtin_amdgcn_readfirstlane(tid >> 6);
    const int h = blockIdx.x & 31, qb = blockIdx.x >> 5;
    const int q0 = qb * 256, kbase = q0 - 256;
    const bf16_t* Qh = qkv + (size_t)h * (S_LEN * 32);
    const bf16_t* Kh = qkv + (size_t)(32 + h) * (S_LEN * 32);
    const bf16_t* Vh = qkv + (size_t)(64 + h) * (S_LEN * 32);

    {
        const unsigned koff = (unsigned)((lane >> 2) * 64 + (((lane & 3) ^ ((lane >> 4) & 3)) << 4));
        const unsigned voff = (unsigned)(lane * 16);
#pragma unroll
        for (int i = 0; i < 4; ++i) {
            const int pc = w + 8 * i;
            if (kbase + pc * 16 >= 0) {
                glds16((const char*)(Kh + (long)(kbase + pc * 16) * 32), koff, lds_base + pc * 1024);
                glds16((const char*)(Vh + (long)(kbase + pc * 16) * 32), voff, lds_base + 32768 + pc * 1024);
            }
        }
    }
    const int r32 = lane & 31, hi = lane >> 5;
    const int qw = q0 + 32 * w;
    bf16x8 qf[2];
#pragma unroll
    for (int s = 0; s < 2; ++s) qf[s] = *(const bf16x8*)(Qh + (size_t)(qw + r32) * 32 + 16 * s + 8 * hi);
    wait_vm<0>();
    __syncthreads();
    u32x4 pf_dummy = {0u, 0u, 0u, 0u};
    {
        const char* a0 = (const char*)pf0 + (size_t)(blockIdx.x * 512 + tid) * 16;
        asm volatile("global_load_dwordx4 %0, %1, off" : "+v"(pf_dummy) : "v"(a0) : "memory");
    }

    f32x16 sc[9];
    const float NEG = -INFINITY;
#pragma unroll
    for (int j = 0; j < 9; ++j) {
        const int kb = qw - 256 + 32 * j;
        if (kb >= 0) {
            const int key = 32 * (w + j) + r32;
            f32x16 a = {};
#pragma unroll
            for (int s = 0; s < 2; ++s) {
                const bf16x8 kf = *(LAS const bf16x8*)(ldsK + key * 64 + (((2 * s + hi) ^ ((key >> 2) & 3)) << 4));
                a = __builtin_amdgcn_mfma_f32_32x32x16_bf16(kf, qf[s], a, 0, 0, 0);
            }
            sc[j] = a;
        } else {
#pragma unroll
            for (int r = 0; r < 16; ++r) sc[j][r] = NEG;
        }
    }
#pragma unroll
    for (int r = 0; r < 16; ++r) {
        const int i = (r & 3) + 8 * (r >> 2) + 4 * hi;
        if (!(i > r32)) sc[0][r] = NEG;
        if (!(i <= r32)) sc[8][r] = NEG;
    }
    float mxa = NEG, mxb = NEG;
#pragma unroll
    for (int j = 0; j < 9; ++j)
#pragma unroll
        for (int r = 0; r < 16; r += 4) {
            mxa = __builtin_fmaxf(__builtin_fmaxf(mxa, sc[j][r]), sc[j][r + 1]);
            mxb = __builtin_fmaxf(__builtin_fmaxf(mxb, sc[j][r + 2]), sc[j][r + 3]);
        }
    float mx = fmaxf(mxa, mxb);
    mx = fmaxf(mx, __shfl_xor(mx, 32));
    const float c = 0.17677669529663687f * 1.4426950408889634f;
    const float nm = -mx * c;

    f32x16 o = {}, ls = {};
    const bf16x8 ones = {0x3f80, 0x3f80, 0x3f80, 0x3f80, 0x3f80, 0x3f80, 0x3f80, 0x3f80};
    const int gq = (lane & 15) >> 2, gp = lane & 3, gd = (lane >> 4) & 1;
#pragma unroll
    for (int j = 0; j < 9; ++j) {
        const int kb = qw - 256 + 32 * j;
        if (kb >= 0) {
#pragma unroll
            for (int s = 0; s < 2; ++s) {
                u32x4 pw;
#pragma unroll
                for (int e = 0; e < 4; ++e)
                    pw[e] = pk_bf16(__builtin_amdgcn_exp2f(__builtin_fmaf(sc[j][8 * s + 2 * e], c, nm)),
                                    __builtin_amdgcn_exp2f(__builtin_fmaf(sc[j][8 * s + 2 * e + 1], c, nm)));
                const int key = 32 * (w + j) + 16 * s + 4 * hi + gq;
                LAS const char* vp = ldsV + key * 64 + (16 * gd + 4 * gp) * 2;
                s16x4 lo = tr_read(vp), hi8 = tr_read(vp + 8 * 64);
                const bf16x8 vf = (bf16x8){lo[0], lo[1], lo[2], lo[3], hi8[0], hi8[1], hi8[2], hi8[3]};
                const bf16x8 pf = __builtin_bit_cast(bf16x8, pw);
                o = __builtin_amdgcn_mfma_f32_32x32x16_bf16(vf, pf, o, 0, 0, 0);
                ls = __builtin_amdgcn_mfma_f32_32x32x16_bf16(ones, pf, ls, 0, 0, 0);
            }
        }
    }
    const float rl = 1.0f / ls[0];
    bf16_t* dst = O + (size_t)(qw + r32) * D_MODEL + h * 32 + 4 * hi;
#pragma unroll
    for (int t = 0; t < 4; ++t) {
        u32x2 wv; wv[0] = pk_bf16(o[4 * t] * rl, o[4 * t + 1] * rl); wv[1] = pk_bf16(o[4 * t + 2] * rl, o[4 * t + 3] * rl);
        ST8(dst + 8 * t, wv);
    }
    asm volatile("s_waitcnt vmcnt(0)" :: "v"(pf_dummy) : "memory");
}

template <typename KF> static void set_lds(KF kf, int bytes) {
    (void)hipFuncSetAttribute((const void*)kf, hipFuncAttributeMaxDynamicSharedMemorySize, bytes);
}

extern "C" void kernel_launch(void* const* d_in, const int* in_sizes, int n_in,
                              void* d_out, int out_size, void* d_ws, size_t ws_size,
                              hipStream_t stream) {
    const bf16_t* x = (const bf16_t*)d_in[0];
    const bf16_t* wqkv = (const bf16_t*)d_in[1];
    const bf16_t* wo = (const bf16_t*)d_in[2];
    const bf16_t* w1 = (const bf16_t*)d_in[4];
    const bf16_t* w3 = (const bf16_t*)d_in[5];
    const bf16_t* w2 = (const bf16_t*)d_in[6];
    bf16_t* out = (bf16_t*)d_out;
    char* ws = (char*)d_ws;
    bf16_t* qkv = (bf16_t*)(ws + (4u << 20));
    bf16_t* att = (bf16_t*)(ws + (16u << 20));
    bf16_t* hbuf = (bf16_t*)(ws + (20u << 20));
    bf16_t* act = (bf16_t*)(ws + (28u << 20));
    float* hstat = (float*)(ws + (24u << 20));

    {
        auto kf = gemm_kernel<128, 192, 2, 4, 4, EPI_STORE, 1, G1RM, G1RN>;
        constexpr int lds = 4 * (128 + 192) * 128;
        set_lds(kf, lds);
        kf<<<256, 512, lds, stream>>>(x, wqkv, nullptr, qkv, nullptr, S_LEN, 3072, 1024, 3072, 3072);
    }
    {
        set_lds(attn_kernel, 65536);
        attn_kernel<<<256, 512, 65536, stream>>>(qkv, att, wo);
    }
    {
        auto kf = gemm_kernel<128, 64, 4, 2, 6, EPI_RES_STAT, 0, 4, 2>;
        constexpr int lds = 6 * (128 + 64) * 128;
        set_lds(kf, lds);
        kf<<<256, 512, lds, stream>>>(att, wo, x, hbuf, hstat, S_LEN, 1024, 1024, 1024, 1024);
    }
    {
        set_lds(glu_gemm_kernel, 163840);
        glu_gemm_kernel<<<256, 512, 163840, stream>>>(hbuf, w1, w3, hstat, act, w2);
    }
    {
        auto kf = gemm_kernel<128, 64, 4, 2, 6, EPI_RES, 0, G4RM, G4RN>;
        constexpr int lds = 6 * (128 + 64) * 128;
        set_lds(kf, lds);
        kf<<<256, 512, lds, stream>>>(act, w2, hbuf, out, nullptr, S_LEN, 1024, F_DIM, 1024, 1024);
    }
}
```

```cpp
#include <hip/hip_runtime.h>
#include <hip/hip_bf16.h>
#include <stdint.h>

typedef unsigned short bf16_t;
typedef short bf16x8 __attribute__((ext_vector_type(8)));
typedef short s16x4 __attribute__((ext_vector_type(4)));
typedef float f32x4 __attribute__((ext_vector_type(4)));
typedef float f32x16 __attribute__((ext_vector_type(16)));
typedef float f32x2 __attribute__((ext_vector_type(2)));
typedef unsigned u32x4 __attribute__((ext_vector_type(4)));
typedef unsigned u32x2 __attribute__((ext_vector_type(2)));
#define LAS __attribute__((address_space(3)))

constexpr int S_LEN = 2048, D_MODEL = 1024, F_DIM = 3584, N_HEAD = 32, D_HEAD = 32, WIN = 256;

__device__ __forceinline__ float bf2f(unsigned v16) { return __uint_as_float(v16 << 16); }
__device__ __forceinline__ unsigned pk_bf16(float lo, float hi) {
    unsigned r; asm("v_cvt_pk_bf16_f32 %0, %1, %2" : "=v"(r) : "v"(lo), "v"(hi)); return r;
}
__device__ __forceinline__ s16x4 tr_read(LAS const char* p) {
    return __builtin_amdgcn_ds_read_tr16_b64_v4i16((LAS s16x4*)p);
}
__device__ __forceinline__ void glds16(const void* sbase, unsigned voff, unsigned lds_addr) {
    unsigned keep;
    asm volatile("s_mov_b32 %0, m0\n\ts_mov_b32 m0, %3\n\ts_nop 0\n\tglobal_load_lds_dwordx4 %2, %1\n\ts_mov_b32 m0, %0"
                 : "=&s"(keep) : "s"(sbase), "v"(voff), "s"(lds_addr) : "memory");
}
typedef __bf16 bf16x2_t __attribute__((ext_vector_type(2)));
__device__ __forceinline__ float dot2(unsigned a, unsigned b, float acc) {
    return __builtin_amdgcn_fdot2_f32_bf16(__builtin_bit_cast(bf16x2_t, a), __builtin_bit_cast(bf16x2_t, b), acc, false);
}
__device__ __forceinline__ void frag_sum(const bf16x8& f, float& s1) {
    const u32x4 u = __builtin_bit_cast(u32x4, f);
#pragma unroll
    for (int e = 0; e < 4; ++e) s1 = dot2(u[e], 0x3f803f80u, s1);
}
__device__ __forceinline__ void frag_sum_sq(const bf16x8& f, float& s1, float& s2) {
    const u32x4 u = __builtin_bit_cast(u32x4, f);
#pragma unroll
    for (int e = 0; e < 4; ++e) { s1 = dot2(u[e], 0x3f803f80u, s1); s2 = dot2(u[e], u[e], s2); }
}
__device__ __forceinline__ float red_g(float v) { v += __shfl_xor(v, 16); v += __shfl_xor(v, 32); return v; }
__device__ __forceinline__ void prefetch_region(const char* base, int lpr, long pitch, int chunk, int part, int tid,
                                                int nthreads, unsigned& dummy) {
    for (int l = tid; l < chunk; l += nthreads) {
        const int L = part * chunk + l, row = L / lpr, c = L - row * lpr;
        const char* a = base + (long)row * pitch + c * 128;
        asm volatile("global_load_dword %0, %1, off" : "+v"(dummy) : "v"(a) : "memory");
    }
}
#define ST8(ptr, v) (*(u32x2*)(ptr) = (v))
__device__ __forceinline__ void st16_sc1(void* p, u32x4 v) { asm volatile("global_store_dwordx4 %0, %1, off sc1\n\ts_nop 1" :: "v"(p), "v"(v) : "memory"); }
#define ST16(ptr, v) st16_sc1((void*)(ptr), (v))
template <int N> __device__ __forceinline__ void wait_vm() { asm volatile("s_waitcnt vmcnt(%0)" ::"n"(N) : "memory"); }

#define G4RM 4
#define G4RN 2
#define G1RM 2
#define G1RN 4
#define KR1 0
#define KR2 0
#define KR4 0
#define NOCS 0
enum { EPI_STORE = 0, EPI_RES = 1, EPI_RES_STAT = 2 };

template <int BM, int BN, int WM, int WN, int NST, int EPI, int LNF, int RM, int RN, int KROT>
__global__ __launch_bounds__(WM * WN * 64) void gemm_kernel(
    const bf16_t* __restrict__ X, const bf16_t* __restrict__ W0,
    const bf16_t* __restrict__ R, bf16_t* __restrict__ C, float* __restrict__ stat_out,
    int M, int N, int K, int ldw, int ldc) {
    constexpr int NWAVE = WM * WN;
    constexpr int MF = BM / 16 / WM, NF = BN / 16 / WN, NB = BN / 16;
    constexpr int A_BYTES = BM * 128, B_BYTES = BN * 128, ST_BYTES = A_BYTES + B_BYTES;
    constexpr int LPW = (BM + BN) / 8 / NWAVE;
    constexpr int LPA = BM / 8 / NWAVE;
    static_assert((BM + BN) % (8 * NWAVE) == 0 && (BM % (8 * NWAVE)) == 0, "piece split");
    static_assert(NB % 4 == 0, "NB%4");
    static_assert(!LNF || (MF == WN && WM == 2), "LN fold layout");
    static_assert(EPI != EPI_STORE || (BM * 4 == NWAVE * 64), "head-major store layout");
    extern __shared__ __attribute__((aligned(16))) char smem[];
    LAS char* lds = (LAS char*)smem;

    const int tid = threadIdx.x, lane = tid & 63;
    const int wave = __builtin_amdgcn_readfirstlane(tid >> 6);
    const int wm = wave / WN, wn = wave % WN;

    const int tiles_m = M / BM, tiles_n = N / BN;
    int tm, tn;
    {
        const int xcd = blockIdx.x & 7, idx = blockIdx.x >> 3;
        const int rm = xcd / RN, rn = xcd % RN;
        const int sm = tiles_m / RM, sn = tiles_n / RN;
        tm = rm * sm + idx % sm; tn = rn * sn + idx / sm;
    }
    const int m0 = tm * BM, n0 = tn * BN;
    unsigned pf_dummy = 0;

    unsigned voff[LPW];
    const char* sbase[LPW];
#pragma unroll
    for (int j = 0; j < LPW; ++j) {
        const int ii = wave + NWAVE * j;
        if (j < LPA) {
            const int row = ii * 8 + (lane >> 3), ch = (lane & 7) ^ (row & 7);
            voff[j] = (unsigned)(row * K * 2 + ch * 16);
            sbase[j] = (const char*)(X + (size_t)m0 * K);
        } else {
            const int pi = ii - BM / 8;
            const int kb = pi / (NB / 4), nb = 4 * (pi % (NB / 4)) + (lane >> 4);
            const int r = ((lane & 15) >> 1) ^ (4 * (kb & 1)), half = lane & 1;
            voff[j] = (unsigned)(((8 * kb + r) * ldw + 16 * nb + 8 * half) * 2);
            sbase[j] = (const char*)(W0 + n0);
        }
    }
    const unsigned lds_base = (unsigned)(uintptr_t)lds;
    const int NT = K / 64;
    auto issue_piece = [&](int buf, int j) {
        const int ii = wave + NWAVE * j;
        glds16(sbase[j], voff[j], lds_base + buf * ST_BYTES + ii * 1024);
        sbase[j] += (j < LPA) ? 128 : (long)128 * ldw;
    };
    auto issue = [&](int buf) {
#pragma unroll
        for (int j = 0; j < LPW; ++j) issue_piece(buf, j);
    };

    const int fr = lane & 15, g = lane >> 4, q = (lane & 15) >> 2, p = lane & 3;
    const int rotm = LNF ? wn : 0, rotk = LNF ? wm : 0;
    const int xrow = wm * (MF * 16) + fr;
    const int xoff0 = xrow * 128 + ((g ^ (xrow & 7)) << 4);
    const int woff0 = A_BYTES + g * NB * 256 + (q + 4 * (g & 1)) * 32 + p * 8 + wn * NF * 256;

    f32x4 acc[MF][NF];
#pragma unroll
    for (int i = 0; i < MF; ++i)
#pragma unroll
        for (int j = 0; j < NF; ++j) acc[i][j] = (f32x4){0.f, 0.f, 0.f, 0.f};
    f32x4 s1 = {0.f, 0.f, 0.f, 0.f}, s2 = {0.f, 0.f, 0.f, 0.f}, cs[NF];
#pragma unroll
    for (int j = 0; j < NF; ++j) cs[j] = (f32x4){0.f, 0.f, 0.f, 0.f};
    const bf16x8 ones = {0x3f80, 0x3f80, 0x3f80, 0x3f80, 0x3f80, 0x3f80, 0x3f80, 0x3f80};

#pragma unroll
    for (int s = 0; s < NST - 1; ++s) issue(s);

    constexpr int NRD = 2 * (NF + MF);
    constexpr int NMS = MF * NF + (LNF ? 2 : 0);
    constexpr int NM = 2 * NMS + (LNF ? NF : 0);
    auto read_one = [&](LAS const char* base, bf16x8 (&xf)[2][MF], bf16x8 (&wf)[2][NF], int r) {
        const int slot = r / (NF + MF), e = r % (NF + MF), ks = slot ^ rotk;
        if (e < NF) {
            const int o = ks * (4 * NB * 256) + e * 256;
            s16x4 lo = tr_read(base + woff0 + o);
            s16x4 hi = tr_read(base + (woff0 ^ 128) + o);
            wf[slot][e] = (bf16x8){lo[0], lo[1], lo[2], lo[3], hi[0], hi[1], hi[2], hi[3]};
        } else {
            const int i = e - NF;
            xf[slot][i] = *(LAS const bf16x8*)(base + ((xoff0 ^ (ks * 64)) + (i ^ rotm) * 2048));
        }
    };
    auto read_tile = [&](LAS const char* base, bf16x8 (&xf)[2][MF], bf16x8 (&wf)[2][NF]) {
#pragma unroll
        for (int r = 0; r < NRD; ++r) read_one(base, xf, wf, r);
    };
    auto mfma_one = [&](bf16x8 (&xf)[2][MF], bf16x8 (&wf)[2][NF], int f) {
        if (f >= 2 * NMS) { const int j = f - 2 * NMS; cs[j] = __builtin_amdgcn_mfma_f32_16x16x32_bf16(wf[0][j], ones, cs[j], 0, 0, 0); return; }
        const int slot = f / NMS, e = f % NMS;
        if (e < MF * NF) {
            const int i = e / NF, j = e % NF;
            acc[i][j] = __builtin_amdgcn_mfma_f32_16x16x32_bf16(wf[slot][j], xf[slot][i], acc[i][j], 0, 0, 0);
        } else if (e == MF * NF) {
            s1 = __builtin_amdgcn_mfma_f32_16x16x32_bf16(ones, xf[slot][0], s1, 0, 0, 0);
        } else {
            s2 = __builtin_amdgcn_mfma_f32_16x16x32_bf16(xf[slot][0], xf[slot][0], s2, 0, 0, 0);
        }
    };
    bf16x8 xa[2][MF], wa[2][NF], xb[2][MF], wb[2][NF];
    wait_vm<(NST - 2) * LPW>();
    __builtin_amdgcn_s_barrier();
    asm volatile("" ::: "memory");
    read_tile(lds, xa, wa);

    int buf = 0, pbuf = NST - 1;
    auto step = [&](int kt, bf16x8 (&xc)[2][MF], bf16x8 (&wc)[2][NF], bf16x8 (&xn)[2][MF], bf16x8 (&wn2)[2][NF]) {
        const int rem = NT - 2 - kt;
        if (rem >= NST - 3) wait_vm<(NST - 3) * LPW>();
        else if (NST > 4 && rem == 1) wait_vm<LPW>();
        else if (NST > 5 && rem == 2) wait_vm<2 * LPW>();
        else wait_vm<0>();
        __builtin_amdgcn_s_barrier();
        asm volatile("" ::: "memory");
        const bool ld = kt + NST - 1 < NT;
        const int ibuf = pbuf;
        pbuf = buf;
        buf = (buf + 1 == NST) ? 0 : buf + 1;
        LAS const char* nbase = lds + buf * ST_BYTES;
        if (ld) issue(ibuf);
        __builtin_amdgcn_sched_barrier(0);
        read_tile(nbase, xn, wn2);
#pragma unroll
        for (int f = 0; f < NM; ++f) mfma_one(xc, wc, f);
        {
            constexpr int NR = 2 * (MF + 2 * NF), RPM = (NR + NM - 1) / NM;
#pragma unroll
            for (int f = 0; f < NM; ++f) {
                __builtin_amdgcn_sched_group_barrier(0x8, 1, 0);
                __builtin_amdgcn_sched_group_barrier(0x100, RPM, 0);
            }
        }
        __builtin_amdgcn_sched_barrier(0);
    };
    for (int kt = 0; kt < NT; kt += 2) {
        step(kt, xa, wa, xb, wb);
        step(kt + 1, xb, wb, xa, wa);
    }

    asm volatile("" :: "v"(pf_dummy));
    constexpr int NTHR = NWAVE * 64, CPR = BN / 8, NCH = BM * CPR / NTHR;
    constexpr int RS = (EPI == EPI_STORE) ? BN * 2 + 16 : BN * 4 + 16;
    LAS float* rowstat = (LAS float*)(lds + BM * RS);
    LAS float* colsum = rowstat + BM * 2;
    u32x4 rres[NCH];
    if (EPI != EPI_STORE) {
#pragma unroll
        for (int r = 0; r < NCH; ++r) {
            const int c = tid + NTHR * r, row = c / CPR, c8 = c % CPR;
            rres[r] = *(const u32x4*)(R + (size_t)(m0 + row) * ldc + n0 + c8 * 8);
        }
    }
    __syncthreads();
    if (LNF) {
        if (g == (fr >> 2)) {
            const int r = wm * (MF * 16) + wn * 16 + fr;
            const int d = fr & 3;
            rowstat[2 * r] = s1[0];
            rowstat[2 * r + 1] = d == 0 ? s2[0] : d == 1 ? s2[1] : d == 2 ? s2[2] : s2[3];
        }
        if (fr == 0) {
#pragma unroll
            for (int j = 0; j < NF; ++j) *(LAS f32x4*)(colsum + wm * BN + (wn * NF + j) * 16 + 4 * g) = cs[j];
        }
        __syncthreads();
    }
    f32x4 csv[NF];
    if (LNF) {
#pragma unroll
        for (int j = 0; j < NF; ++j) {
            const int c = (wn * NF + j) * 16 + 4 * g;
            csv[j] = *(LAS const f32x4*)(colsum + c) + *(LAS const f32x4*)(colsum + BN + c);
        }
    }
#pragma unroll
    for (int i = 0; i < MF; ++i) {
        const int rl = wm * (MF * 16) + (i ^ rotm) * 16 + fr;
        float mean = 0.f, rstd = 1.f;
        if (LNF) {
            const float a = rowstat[2 * rl], b = rowstat[2 * rl + 1];
            mean = a * (1.0f / D_MODEL);
            const float var = fmaxf(b * (1.0f / D_MODEL) - mean * mean, 0.f);
            rstd = 1.0f / sqrtf(var + 1e-5f);
        }
#pragma unroll
        for (int j = 0; j < NF; ++j) {
            const int nl = (wn * NF + j) * 16 + 4 * g;
            f32x4 v = acc[i][j];
            if (LNF) v = (v - csv[j] * mean) * rstd;
            if (EPI == EPI_STORE) {
                u32x2 w; w[0] = pk_bf16(v[0], v[1]); w[1] = pk_bf16(v[2], v[3]);
                *(LAS u32x2*)(lds + rl * RS + nl * 2) = w;
            } else {
                *(LAS f32x4*)(lds + rl * RS + nl * 4) = v;
            }
        }
    }
    __syncthreads();
#pragma unroll
    for (int r = 0; r < NCH; ++r) {
        const int c = tid + NTHR * r, row = c / CPR, c8 = c % CPR;
        u32x4 w;
        if (EPI == EPI_STORE) {
            const int hrow = tid >> 2, d8 = tid & 3;
            w = *(LAS const u32x4*)(lds + hrow * RS + r * 64 + d8 * 16);
            ST16(C + ((size_t)(n0 / 32 + r) * M + m0 + hrow) * 32 + d8 * 8, w);
            continue;
        } else {
            f32x4 v0 = *(LAS const f32x4*)(lds + row * RS + c8 * 32), v1 = *(LAS const f32x4*)(lds + row * RS + c8 * 32 + 16);
            const u32x4 rv = rres[r];
            v0[0] += bf2f(rv[0] & 0xffffu); v0[1] += bf2f(rv[0] >> 16); v0[2] += bf2f(rv[1] & 0xffffu); v0[3] += bf2f(rv[1] >> 16);
            v1[0] += bf2f(rv[2] & 0xffffu); v1[1] += bf2f(rv[2] >> 16); v1[2] += bf2f(rv[3] & 0xffffu); v1[3] += bf2f(rv[3] >> 16);
            w[0] = pk_bf16(v0[0], v0[1]); w[1] = pk_bf16(v0[2], v0[3]); w[2] = pk_bf16(v1[0], v1[1]); w[3] = pk_bf16(v1[2], v1[3]);
        }
        ST16(C + (size_t)(m0 + row) * ldc + n0 + c8 * 8, w);
        if (EPI == EPI_RES_STAT) {
            float a = 0.f, b = 0.f;
#pragma unroll
            for (int e = 0; e < 4; ++e) {
                const float x0 = bf2f(w[e] & 0xffffu), x1 = bf2f(w[e] >> 16);
                a += x0 + x1; b += x0 * x0 + x1 * x1;
            }
#pragma unroll
            for (int o = 1; o < CPR; o <<= 1) { a += __shfl_xor(a, o); b += __shfl_xor(b, o); }
            if (c8 == 0) { float2 o2; o2.x = a; o2.y = b; *(float2*)(stat_out + ((size_t)(m0 + row) * tiles_n + tn) * 2) = o2; }
        }
    }
}

__global__ __launch_bounds__(512, 2) void glu_gemm_kernel(
    const bf16_t* __restrict__ X, const bf16_t* __restrict__ W1, const bf16_t* __restrict__ W3,
    const float* __restrict__ stat_in, bf16_t* __restrict__ C, const bf16_t* __restrict__ pfw) {
    constexpr int K = D_MODEL, LDW = F_DIM, HTB = 16384;
    if (blockIdx.x >= 224) {
        u32x4 d = {0u, 0u, 0u, 0u};
        const char* a = (const char*)pfw + (size_t)(blockIdx.x - 224) * 229376 + threadIdx.x * 16;
#pragma unroll
        for (int r = 0; r < 28; ++r) asm volatile("global_load_dwordx4 %0, %1, off offset:0" : "+v"(d) : "v"(a + r * 8192) : "memory");
        asm volatile("s_waitcnt vmcnt(0)" :: "v"(d) : "memory");
        return;
    }
    extern __shared__ __attribute__((aligned(16))) char smem[];
    LAS char* lds = (LAS char*)smem;
    const unsigned lds_base = (unsigned)(uintptr_t)lds;
    const int tid = threadIdx.x, lane = tid & 63;
    const int wave = __builtin_amdgcn_readfirstlane(tid >> 6);
    const int wr = wave >> 2, wc = wave & 3;
    int tm, tn;
    { const int xcd = blockIdx.x & 7, idx = blockIdx.x >> 3; tm = (xcd >> 2) * 4 + (idx & 3); tn = (xcd & 3) * 7 + (idx >> 2); }
    const int m0 = tm * 256, n0 = tn * 128;
    unsigned pf_dummy = 0;

    unsigned xoffs[2], woffs[2];
#pragma unroll
    for (int j = 0; j < 2; ++j) {
        const int pc = wave + 8 * j;
        const int row = pc * 8 + (lane >> 3), ch = (lane & 7) ^ (row & 7);
        xoffs[j] = (unsigned)(row * K * 2 + ch * 16);
        const int kb = pc >> 1, nb = 4 * (pc & 1) + (lane >> 4);
        const int r = ((lane & 15) >> 1) ^ (4 * (kb & 1)), half = lane & 1;
        woffs[j] = (unsigned)(((8 * kb + r) * LDW + 16 * nb + 8 * half) * 2);
    }
    const char* xsrc = (const char*)(X + (size_t)m0 * K);
    const char* w1src = (const char*)(W1 + n0);
    const char* w3src = (const char*)(W3 + n0);
#define SA_OFF(b, h) (((b) * 2 + (h)) * HTB)
#define SB_OFF(b, h) ((4 + (b) * 2 + (h)) * HTB)
#define STAGE_A(b, h, kt) do { const char* _s = xsrc + (size_t)(h) * 128 * K * 2 + (kt) * 128; \
        glds16(_s, xoffs[0], lds_base + SA_OFF(b, h) + wave * 1024); glds16(_s, xoffs[1], lds_base + SA_OFF(b, h) + (wave + 8) * 1024); } while (0)
#define STAGE_B(b, h, kt) do { const char* _s = ((h) ? w3src : w1src) + (size_t)(kt) * 64 * LDW * 2; \
        glds16(_s, woffs[0], lds_base + SB_OFF(b, h) + wave * 1024); glds16(_s, woffs[1], lds_base + SB_OFF(b, h) + (wave + 8) * 1024); } while (0)
    const int fr = lane & 15, g = lane >> 4, q = (lane & 15) >> 2, p = lane & 3;
    const int xrow = wr * 64 + fr;
    const int xo0 = (xrow * 128 + ((g ^ (xrow & 7)) << 4)) ^ (wr * 64);
    const int wo0 = g * 2048 + (q + 4 * (g & 1)) * 32 + p * 8 + wc * 512;
    unsigned xb_[4][2], wb_[2][2];
#pragma unroll
    for (int m = 0; m < 4; ++m)
#pragma unroll
        for (int k = 0; k < 2; ++k) { xb_[m][k] = lds_base + ((xo0 ^ (k * 64)) + m * 2048); asm volatile("" : "+v"(xb_[m][k])); }
#pragma unroll
    for (int t = 0; t < 2; ++t)
#pragma unroll
        for (int k = 0; k < 2; ++k) { wb_[t][k] = lds_base + 4 * HTB + ((wo0 ^ (t * 128)) + (k ^ wr) * 8192); asm volatile("" : "+v"(wb_[t][k])); }
#define LDSP(a) ((LAS const char*)(uintptr_t)(a))
#define LDA(dst, b, h) do { _Pragma("unroll") for (int m = 0; m < 4; ++m) _Pragma("unroll") for (int k = 0; k < 2; ++k) \
        dst[m][k] = *(LAS const bf16x8*)(LDSP(xb_[m][k]) + SA_OFF(b, h)); } while (0)
#define LDB(dst, b, h) do { _Pragma("unroll") for (int n = 0; n < 2; ++n) _Pragma("unroll") for (int k = 0; k < 2; ++k) { \
        s16x4 lo_ = tr_read(LDSP(wb_[0][k]) + SA_OFF(b, h) + n * 256); \
        s16x4 hi_ = tr_read(LDSP(wb_[1][k]) + SA_OFF(b, h) + n * 256); \
        dst[n][k] = (bf16x8){lo_[0], lo_[1], lo_[2], lo_[3], hi_[0], hi_[1], hi_[2], hi_[3]}; } } while (0)
#define MMA(ai, bj, At, Bt, CS) do { __builtin_amdgcn_s_setprio(1); \
        _Pragma("unroll") for (int m = 0; m < 4; ++m) _Pragma("unroll") for (int n = 0; n < 2; ++n) _Pragma("unroll") for (int k = 0; k < 2; ++k) \
            acc[ai][bj][m][n] = __builtin_amdgcn_mfma_f32_16x16x32_bf16(Bt[n][k], At[m][k], acc[ai][bj][m][n], 0, 0, 0); \
        if (((CS) & 2) && !NOCS) { cs[bj][0] = __builtin_amdgcn_mfma_f32_16x16x32_bf16(Bt[0][0], ones, cs[bj][0], 0, 0, 0); \
                        cs[bj][1] = __builtin_amdgcn_mfma_f32_16x16x32_bf16(Bt[1][0], ones, cs[bj][1], 0, 0, 0); } \
        __builtin_amdgcn_s_setprio(0); } while (0)
#define WAIT_V(n) asm volatile("s_waitcnt vmcnt(" #n ")" ::: "memory")
#define WAIT_L(n) asm volatile("s_waitcnt lgkmcnt(" #n ")" ::: "memory")
#define BAR __builtin_amdgcn_s_barrier()
#define SCHED __builtin_amdgcn_sched_barrier(0)
    f32x4 acc[2][2][4][2] = {};
    bf16x8 At[4][2], B0[2][2], B1[2][2];
    f32x4 cs[2][2] = {};
    const bf16x8 ones = {0x3f80, 0x3f80, 0x3f80, 0x3f80, 0x3f80, 0x3f80, 0x3f80, 0x3f80};
    constexpr int nt = K / 64;
    {
        const char* ssrc = (const char*)(stat_in + (size_t)m0 * 32);
#pragma unroll
        for (int j = 0; j < 4; ++j) glds16(ssrc, (unsigned)((wave * 4 + j) * 1024 + lane * 16), lds_base + 8 * HTB + (wave * 4 + j) * 1024);
    }
    STAGE_B(0, 0, 0); STAGE_A(0, 0, 0); STAGE_B(0, 1, 0); STAGE_A(0, 1, 0);
    if (wr == 1) BAR;
    WAIT_V(4); BAR;
    STAGE_B(1, 0, 1); STAGE_A(1, 0, 1); STAGE_B(1, 1, 1);
    WAIT_V(6); BAR;
    LDB(B0, 0, 0);
    for (int t = 0; t < nt; t += 2) {
        const bool more = t + 2 < nt;
        LDA(At, 0, 0); STAGE_A(1, 1, t + 1);
        BAR; WAIT_L(0); MMA(0, 0, At, B0, 2); BAR; SCHED;
        LDB(B1, 0, 1); if (more) STAGE_B(0, 0, t + 2);
        BAR; WAIT_L(0); MMA(0, 1, At, B1, 1); BAR;
        LDA(At, 0, 1); if (more) { STAGE_A(0, 0, t + 2); WAIT_V(10); } else { WAIT_V(6); }
        BAR; WAIT_L(0); MMA(1, 0, At, B0, 1); BAR; SCHED;
        LDB(B0, 1, 0);
        if (more) { STAGE_B(0, 1, t + 2); WAIT_V(6); } else { WAIT_V(0); }
        BAR; MMA(1, 1, At, B1, 2); BAR; SCHED;
        LDA(At, 1, 0); if (more) STAGE_A(0, 1, t + 2);
        BAR; WAIT_L(0); MMA(0, 0, At, B0, 2); BAR; SCHED;
        LDB(B1, 1, 1); if (more) STAGE_B(1, 0, t + 3);
        BAR; WAIT_L(0); MMA(0, 1, At, B1, 1); BAR;
        LDA(At, 1, 1); if (more) { STAGE_A(1, 0, t + 3); WAIT_V(10); }
        BAR; WAIT_L(0); MMA(1, 0, At, B0, 1); BAR; SCHED;
        LDB(B0, 0, 0);
        if (more) { STAGE_B(1, 1, t + 3); WAIT_V(6); }
        BAR; MMA(1, 1, At, B1, 2); BAR; SCHED;
    }
    if (wr == 0) BAR;
    asm volatile("" :: "v"(pf_dummy));
    constexpr int RS = 272;
    LAS float* rowstat = (LAS float*)(lds + 256 * RS);
    LAS float* colsum = rowstat + 512;
    if (fr == 0) {
#pragma unroll
        for (int b = 0; b < 2; ++b)
#pragma unroll
            for (int n = 0; n < 2; ++n) *(LAS f32x4*)(colsum + wr * 256 + b * 128 + wc * 32 + n * 16 + 4 * g) = cs[b][n];
    }
    if (tid < 256) {
        LAS const f32x4* sp = (LAS const f32x4*)(lds + 8 * HTB + tid * 128);
        f32x4 t = sp[tid & 7];
#pragma unroll
        for (int e = 1; e < 8; ++e) t += sp[e ^ (tid & 7)];
        const float mean = (t[0] + t[2]) * (1.0f / D_MODEL);
        const float rstd = 1.0f / sqrtf(fmaxf((t[1] + t[3]) * (1.0f / D_MODEL) - mean * mean, 0.f) + 1e-5f);
        rowstat[2 * tid] = mean * rstd; rowstat[2 * tid + 1] = rstd;
    }
    __syncthreads();
    f32x4 csv[2][2];
#pragma unroll
    for (int b = 0; b < 2; ++b)
#pragma unroll
        for (int n = 0; n < 2; ++n) {
            const int c = b * 128 + wc * 32 + n * 16 + 4 * g;
            csv[b][n] = *(LAS const f32x4*)(colsum + c) + *(LAS const f32x4*)(colsum + 256 + c);
        }
#pragma unroll
    for (int ai = 0; ai < 2; ++ai) {
#pragma unroll
        for (int m = 0; m < 4; ++m) {
            const int rl = ai * 128 + wr * 64 + m * 16 + fr;
            const float mr = rowstat[2 * rl], rstd = rowstat[2 * rl + 1];
#pragma unroll
            for (int n = 0; n < 2; ++n) {
                u32x2 w;
#pragma unroll
                for (int hh = 0; hh < 2; ++hh) {
                    const f32x2 ag = {acc[ai][0][m][n][2 * hh], acc[ai][0][m][n][2 * hh + 1]};
                    const f32x2 au = {acc[ai][1][m][n][2 * hh], acc[ai][1][m][n][2 * hh + 1]};
                    const f32x2 cg = {csv[0][n][2 * hh], csv[0][n][2 * hh + 1]};
                    const f32x2 cu = {csv[1][n][2 * hh], csv[1][n][2 * hh + 1]};
                    const f32x2 gv = ag * rstd - cg * mr;
                    const f32x2 uv = au * rstd - cu * mr;
                    const f32x2 t = gv * (-1.4426950408889634f);
                    f32x2 e; e.x = __builtin_amdgcn_exp2f(t.x); e.y = __builtin_amdgcn_exp2f(t.y);
                    const f32x2 d = e + 1.0f;
                    f32x2 rr; rr.x = __builtin_amdgcn_rcpf(d.x); rr.y = __builtin_amdgcn_rcpf(d.y);
                    const f32x2 o = (gv * uv) * rr;
                    w[hh] = pk_bf16(o.x, o.y);
                }
                *(LAS u32x2*)(lds + rl * RS + (wc * 32 + n * 16 + 4 * g) * 2) = w;
            }
        }
        asm volatile("s_waitcnt lgkmcnt(0)" ::: "memory");
        __builtin_amdgcn_s_barrier();
        asm volatile("" ::: "memory");
#pragma unroll
        for (int r = 0; r < 4; ++r) {
            const int c = tid + 512 * r, row = ai * 128 + (c >> 4), c8 = c & 15;
            ST16(C + (size_t)(m0 + row) * LDW + n0 + c8 * 8, *(LAS const u32x4*)(lds + row * RS + c8 * 16));
        }
        __builtin_amdgcn_sched_barrier(0);
    }
#undef SA_OFF
#undef SB_OFF
#undef STAGE_A
#undef STAGE_B
#undef LDA
#undef LDB
#undef MMA
}

template <int NST>
__global__ __launch_bounds__(512, 2) void glu2_kernel(
    const bf16_t* __restrict__ X, const bf16_t* __restrict__ W1, const bf16_t* __restrict__ W3,
    const float* __restrict__ stat_in, bf16_t* __restrict__ C) {
    constexpr int K = D_MODEL, LDW = F_DIM, STB = 32768, NT = K / 32;
    extern __shared__ __attribute__((aligned(16))) char smem[];
    LAS char* lds = (LAS char*)smem;
    const unsigned lds_base = (unsigned)(uintptr_t)lds;
    const int tid = threadIdx.x, lane = tid & 63;
    const int wave = __builtin_amdgcn_readfirstlane(tid >> 6);
    const int wm = wave >> 2, wn = wave & 3;
    int tm, tn;
    { const int xcd = blockIdx.x & 7, idx = blockIdx.x >> 3; tm = (xcd >> 2) * 4 + (idx & 3); tn = (xcd & 3) * 7 + (idx >> 2); }
    const int m0 = tm * 256, n0 = tn * 128;
    unsigned pf_dummy = 0;

    unsigned xoffs[2], woffs[2];
#pragma unroll
    for (int j = 0; j < 2; ++j) {
        const int pc = wave + 8 * j;
        const int row = pc * 16 + (lane >> 2), ch = (lane & 3) ^ ((4 - ((row >> 2) & 3)) & 3);
        xoffs[j] = (unsigned)(row * K * 2 + ch * 16);
        const int kb = pc >> 2, nb = 4 * (pc & 1) + (lane >> 4);
        const int r = ((lane & 15) >> 1) ^ (4 * (kb & 1)), half = lane & 1;
        woffs[j] = (unsigned)(((8 * kb + r) * LDW + 16 * nb + 8 * half) * 2);
    }
    const char* xsrc = (const char*)(X + (size_t)m0 * K);
    const char* wsrc = (const char*)(((wave & 2) ? W3 : W1) + n0);
    auto issue = [&](int buf) {
        const unsigned b = lds_base + buf * STB;
        glds16(xsrc, xoffs[0], b + wave * 1024);
        glds16(xsrc, xoffs[1], b + (wave + 8) * 1024);
        glds16(wsrc, woffs[0], b + 16384 + wave * 1024);
        glds16(wsrc, woffs[1], b + 16384 + (wave + 8) * 1024);
        xsrc += 64; wsrc += (size_t)32 * LDW * 2;
    };
    const int fr = lane & 15, g = lane >> 4, q = (lane & 15) >> 2, p = lane & 3;
    const int xo0 = (wm * 128 + fr) * 64 + ((g ^ ((4 - (fr >> 2)) & 3)) << 4);
    const int wo0 = 16384 + g * 4096 + (q + 4 * (g & 1)) * 32 + p * 8 + wn * 512;

    f32x4 acc[8][4];
#pragma unroll
    for (int i = 0; i < 8; ++i)
#pragma unroll
        for (int j = 0; j < 4; ++j) acc[i][j] = (f32x4){0.f, 0.f, 0.f, 0.f};
    f32x4 cs[4] = {};
    const bf16x8 ones = {0x3f80, 0x3f80, 0x3f80, 0x3f80, 0x3f80, 0x3f80, 0x3f80, 0x3f80};

    {
        const char* ssrc = (const char*)(stat_in + (size_t)m0 * 32);
#pragma unroll
        for (int j = 0; j < 4; ++j) glds16(ssrc, (unsigned)((wave * 4 + j) * 1024 + lane * 16), lds_base + NST * STB + (wave * 4 + j) * 1024);
    }
#pragma unroll
    for (int s = 0; s < NST; ++s) issue(s);

    auto read_frags = [&](LAS const char* base, bf16x8 (&xf)[8], bf16x8 (&wf)[4]) {
#pragma unroll
        for (int j = 0; j < 4; ++j) {
            const int o = (j & 1) * 256 + (j >> 1) * 2048;
            s16x4 lo = tr_read(base + wo0 + o);
            s16x4 hi = tr_read(base + (wo0 ^ 128) + o);
            wf[j] = (bf16x8){lo[0], lo[1], lo[2], lo[3], hi[0], hi[1], hi[2], hi[3]};
        }
#pragma unroll
        for (int i = 0; i < 8; ++i) xf[i] = *(LAS const bf16x8*)(base + xo0 + i * 1024);
    };
    auto do_mfma = [&](bf16x8 (&xf)[8], bf16x8 (&wf)[4]) {
#pragma unroll
        for (int i = 0; i < 8; ++i)
#pragma unroll
            for (int j = 0; j < 4; ++j)
                acc[i][j] = __builtin_amdgcn_mfma_f32_16x16x32_bf16(wf[j], xf[i], acc[i][j], 0, 0, 0);
    };
    auto interleave = [&]() {
#pragma unroll
        for (int i = 0; i < 16; ++i) {
            __builtin_amdgcn_sched_group_barrier(0x8, 2, 0);
            __builtin_amdgcn_sched_group_barrier(0x100, 1, 0);
        }
    };
    auto colsum = [&](bf16x8 (&wf)[4]) {
#pragma unroll
        for (int j = 0; j < 4; ++j) cs[j] = __builtin_amdgcn_mfma_f32_16x16x32_bf16(wf[j], ones, cs[j], 0, 0, 0);
    };
    bf16x8 xa[8], wa[4], xb[8], wb[4];
    wait_vm<(NST - 1) * 4>();
    __builtin_amdgcn_s_barrier();
    asm volatile("" ::: "memory");
    read_frags(lds, xa, wa);
    int buf = 0;
    auto step = [&](int kt, bf16x8 (&xc)[8], bf16x8 (&wc_)[4], bf16x8 (&xn)[8], bf16x8 (&wn_)[4], bool docs) {
        const int rem = NT - 2 - kt;
        if (rem >= NST - 2) wait_vm<(NST - 2) * 4>();
        else if (rem == 1) wait_vm<4>();
        else if (NST > 4 && rem == 2) wait_vm<8>();
        else wait_vm<0>();
        asm volatile("s_waitcnt lgkmcnt(0)" ::: "memory");
        __builtin_amdgcn_s_barrier();
        asm volatile("" ::: "memory");
        if (kt + NST < NT) issue(buf);
        buf = (buf + 1 == NST) ? 0 : buf + 1;
        __builtin_amdgcn_sched_barrier(0);
        read_frags(lds + buf * STB, xn, wn_);
        do_mfma(xc, wc_);
        interleave();
        __builtin_amdgcn_sched_barrier(0);
        if (docs) colsum(wc_);
        __builtin_amdgcn_sched_barrier(0);
    };
    for (int kt = 0; kt < NT; kt += 2) {
        step(kt, xa, wa, xb, wb, wm == 0);
        step(kt + 1, xb, wb, xa, wa, wm == 1);
    }
    asm volatile("" :: "v"(pf_dummy));
    __syncthreads();
    LAS float* rowstat = (LAS float*)lds;
    LAS float* colsm = (LAS float*)(lds + 2048);
    if (fr == 0) {
#pragma unroll
        for (int j = 0; j < 4; ++j) *(LAS f32x4*)(colsm + wm * 256 + (j >> 1) * 128 + wn * 32 + (j & 1) * 16 + 4 * g) = cs[j];
    }
    if (tid < 256) {
        LAS const f32x4* sp = (LAS const f32x4*)(lds + NST * STB + tid * 128);
        f32x4 t = sp[tid & 7];
#pragma unroll
        for (int e = 1; e < 8; ++e) t += sp[e ^ (tid & 7)];
        const float mean = (t[0] + t[2]) * (1.0f / D_MODEL);
        const float rstd = 1.0f / sqrtf(fmaxf((t[1] + t[3]) * (1.0f / D_MODEL) - mean * mean, 0.f) + 1e-5f);
        rowstat[2 * tid] = mean; rowstat[2 * tid + 1] = rstd;
    }
    __syncthreads();
    f32x4 csv[4];
#pragma unroll
    for (int j = 0; j < 4; ++j) {
        const int c = (j >> 1) * 128 + wn * 32 + (j & 1) * 16 + 4 * g;
        csv[j] = *(LAS const f32x4*)(colsm + c) + *(LAS const f32x4*)(colsm + 256 + c);
    }
#pragma unroll
    for (int i = 0; i < 8; ++i) {
        const int rl = wm * 128 + i * 16 + fr;
        const float mean = rowstat[2 * rl], rstd = rowstat[2 * rl + 1];
#pragma unroll
        for (int n = 0; n < 2; ++n) {
            const f32x4 gv = (acc[i][n] - csv[n] * mean) * rstd;
            const f32x4 uv = (acc[i][n + 2] - csv[n + 2] * mean) * rstd;
            float o[4];
#pragma unroll
            for (int r = 0; r < 4; ++r) o[r] = gv[r] * __builtin_amdgcn_rcpf(1.0f + __expf(-gv[r])) * uv[r];
            u32x2 w; w[0] = pk_bf16(o[0], o[1]); w[1] = pk_bf16(o[2], o[3]);
            *(u32x2*)(C + (size_t)(m0 + rl) * LDW + n0 + wn * 32 + n * 16 + 4 * g) = w;
        }
    }
}

__global__ __launch_bounds__(512) void attn_kernel(const bf16_t* __restrict__ qkv, bf16_t* __restrict__ O,
                                                   const bf16_t* __restrict__ pf0) {
    extern __shared__ __attribute__((aligned(16))) char smem[];
    LAS char* lds = (LAS char*)smem;
    LAS char* ldsK = lds;
    LAS char* ldsV = lds + 32768;
    const unsigned lds_base = (unsigned)(uintptr_t)lds;
    const int tid = threadIdx.x, lane = tid & 63;
    const int w = __builtin_amdgcn_readfirstlane(tid >> 6);
    const int h = blockIdx.x & 31, qb = blockIdx.x >> 5;
    const int q0 = qb * 256, kbase = q0 - 256;
    const bf16_t* Qh = qkv + (size_t)h * (S_LEN * 32);
    const bf16_t* Kh = qkv + (size_t)(32 + h) * (S_LEN * 32);
    const bf16_t* Vh = qkv + (size_t)(64 + h) * (S_LEN * 32);

    {
        const unsigned koff = (unsigned)((lane >> 2) * 64 + (((lane & 3) ^ ((lane >> 4) & 3)) << 4));
        const unsigned voff = (unsigned)(lane * 16);
#pragma unroll
        for (int i = 0; i < 4; ++i) {
            const int pc = w + 8 * i;
            if (kbase + pc * 16 >= 0) {
                glds16((const char*)(Kh + (long)(kbase + pc * 16) * 32), koff, lds_base + pc * 1024);
                glds16((const char*)(Vh + (long)(kbase + pc * 16) * 32), voff, lds_base + 32768 + pc * 1024);
            }
        }
    }
    const int r32 = lane & 31, hi = lane >> 5;
    const int qw = q0 + 32 * w;
    bf16x8 qf[2];
#pragma unroll
    for (int s = 0; s < 2; ++s) qf[s] = *(const bf16x8*)(Qh + (size_t)(qw + r32) * 32 + 16 * s + 8 * hi);
    wait_vm<0>();
    __syncthreads();
    u32x4 pf_dummy = {0u, 0u, 0u, 0u};
    {
        const char* a0 = (const char*)pf0 + (size_t)(blockIdx.x * 512 + tid) * 16;
        asm volatile("global_load_dwordx4 %0, %1, off" : "+v"(pf_dummy) : "v"(a0) : "memory");
    }

    f32x16 sc[9];
    const float NEG = -INFINITY;
#pragma unroll
    for (int j = 0; j < 9; ++j) {
        const int kb = qw - 256 + 32 * j;
        if (kb >= 0) {
            const int key = 32 * (w + j) + r32;
            f32x16 a = {};
#pragma unroll
            for (int s = 0; s < 2; ++s) {
                const bf16x8 kf = *(LAS const bf16x8*)(ldsK + key * 64 + (((2 * s + hi) ^ ((key >> 2) & 3)) << 4));
                a = __builtin_amdgcn_mfma_f32_32x32x16_bf16(kf, qf[s], a, 0, 0, 0);
            }
            sc[j] = a;
        } else {
#pragma unroll
            for (int r = 0; r < 16; ++r) sc[j][r] = NEG;
        }
    }
#pragma unroll
    for (int r = 0; r < 16; ++r) {
        const int i = (r & 3) + 8 * (r >> 2) + 4 * hi;
        if (!(i > r32)) sc[0][r] = NEG;
        if (!(i <= r32)) sc[8][r] = NEG;
    }
    float mxa = NEG, mxb = NEG;
#pragma unroll
    for (int j = 0; j < 9; ++j)
#pragma unroll
        for (int r = 0; r < 16; r += 4) {
            mxa = __builtin_fmaxf(__builtin_fmaxf(mxa, sc[j][r]), sc[j][r + 1]);
            mxb = __builtin_fmaxf(__builtin_fmaxf(mxb, sc[j][r + 2]), sc[j][r + 3]);
        }
    float mx = fmaxf(mxa, mxb);
    mx = fmaxf(mx, __shfl_xor(mx, 32));
    const float c = 0.17677669529663687f * 1.4426950408889634f;
    const float nm = -mx * c;

    f32x16 o = {}, ls = {};
    const bf16x8 ones = {0x3f80, 0x3f80, 0x3f80, 0x3f80, 0x3f80, 0x3f80, 0x3f80, 0x3f80};
    const int gq = (lane & 15) >> 2, gp = lane & 3, gd = (lane >> 4) & 1;
#pragma unroll
    for (int j = 0; j < 9; ++j) {
        const int kb = qw - 256 + 32 * j;
        if (kb >= 0) {
#pragma unroll
            for (int s = 0; s < 2; ++s) {
                u32x4 pw;
#pragma unroll
                for (int e = 0; e < 4; ++e)
                    pw[e] = pk_bf16(__builtin_amdgcn_exp2f(__builtin_fmaf(sc[j][8 * s + 2 * e], c, nm)),
                                    __builtin_amdgcn_exp2f(__builtin_fmaf(sc[j][8 * s + 2 * e + 1], c, nm)));
                const int key = 32 * (w + j) + 16 * s + 4 * hi + gq;
                LAS const char* vp = ldsV + key * 64 + (16 * gd + 4 * gp) * 2;
                s16x4 lo = tr_read(vp), hi8 = tr_read(vp + 8 * 64);
                const bf16x8 vf = (bf16x8){lo[0], lo[1], lo[2], lo[3], hi8[0], hi8[1], hi8[2], hi8[3]};
                const bf16x8 pf = __builtin_bit_cast(bf16x8, pw);
                o = __builtin_amdgcn_mfma_f32_32x32x16_bf16(vf, pf, o, 0, 0, 0);
                ls = __builtin_amdgcn_mfma_f32_32x32x16_bf16(ones, pf, ls, 0, 0, 0);
            }
        }
    }
    const float rl = 1.0f / ls[0];
    bf16_t* dst = O + (size_t)(qw + r32) * D_MODEL + h * 32 + 4 * hi;
#pragma unroll
    for (int t = 0; t < 4; ++t) {
        u32x2 wv; wv[0] = pk_bf16(o[4 * t] * rl, o[4 * t + 1] * rl); wv[1] = pk_bf16(o[4 * t + 2] * rl, o[4 * t + 3] * rl);
        ST8(dst + 8 * t, wv);
    }
    asm volatile("s_waitcnt vmcnt(0)" :: "v"(pf_dummy) : "memory");
}

template <typename KF> static void set_lds(KF kf, int bytes) {
    (void)hipFuncSetAttribute((const void*)kf, hipFuncAttributeMaxDynamicSharedMemorySize, bytes);
}

extern "C" void kernel_launch(void* const* d_in, const int* in_sizes, int n_in,
                              void* d_out, int out_size, void* d_ws, size_t ws_size,
                              hipStream_t stream) {
    const bf16_t* x = (const bf16_t*)d_in[0];
    const bf16_t* wqkv = (const bf16_t*)d_in[1];
    const bf16_t* wo = (const bf16_t*)d_in[2];
    const bf16_t* w1 = (const bf16_t*)d_in[4];
    const bf16_t* w3 = (const bf16_t*)d_in[5];
    const bf16_t* w2 = (const bf16_t*)d_in[6];
    bf16_t* out = (bf16_t*)d_out;
    char* ws = (char*)d_ws;
    bf16_t* qkv = (bf16_t*)(ws + (4u << 20));
    bf16_t* att = (bf16_t*)(ws + (16u << 20));
    bf16_t* hbuf = (bf16_t*)(ws + (20u << 20));
    bf16_t* act = (bf16_t*)(ws + (28u << 20));
    float* hstat = (float*)(ws + (24u << 20));

    {
        auto kf = gemm_kernel<128, 192, 2, 4, 4, EPI_STORE, 1, G1RM, G1RN, KR1>;
        constexpr int lds = 4 * (128 + 192) * 128;
        set_lds(kf, lds);
        kf<<<256, 512, lds, stream>>>(x, wqkv, nullptr, qkv, nullptr, S_LEN, 3072, 1024, 3072, 3072);
    }
    {
        set_lds(attn_kernel, 65536);
        attn_kernel<<<256, 512, 65536, stream>>>(qkv, att, wo);
    }
    {
        auto kf = gemm_kernel<128, 64, 4, 2, 6, EPI_RES_STAT, 0, 4, 2, KR2>;
        constexpr int lds = 6 * (128 + 64) * 128;
        set_lds(kf, lds);
        kf<<<256, 512, lds, stream>>>(att, wo, x, hbuf, hstat, S_LEN, 1024, 1024, 1024, 1024);
    }
    {
        set_lds(glu_gemm_kernel, 163840);
        glu_gemm_kernel<<<256, 512, 163840, stream>>>(hbuf, w1, w3, hstat, act, w2);
    }
    {
        auto kf = gemm_kernel<128, 64, 4, 2, 6, EPI_RES, 0, G4RM, G4RN, KR4>;
        constexpr int lds = 6 * (128 + 64) * 128;
        set_lds(kf, lds);
        kf<<<256, 512, lds, stream>>>(act, w2, hbuf, out, nullptr, S_LEN, 1024, F_DIM, 1024, 1024);
    }
}
```

```cpp
#include <hip/hip_runtime.h>
#include <hip/hip_bf16.h>
#include <stdint.h>
#include <type_traits>

typedef unsigned short bf16_t;
typedef short bf16x8 __attribute__((ext_vector_type(8)));
typedef short s16x4 __attribute__((ext_vector_type(4)));
typedef float f32x4 __attribute__((ext_vector_type(4)));
typedef float f32x16 __attribute__((ext_vector_type(16)));
typedef float f32x2 __attribute__((ext_vector_type(2)));
typedef unsigned u32x4 __attribute__((ext_vector_type(4)));
typedef unsigned u32x2 __attribute__((ext_vector_type(2)));
#define LAS __attribute__((address_space(3)))

constexpr int S_LEN = 2048, D_MODEL = 1024, F_DIM = 3584, N_HEAD = 32, D_HEAD = 32, WIN = 256;

__device__ __forceinline__ float bf2f(unsigned v16) { return __uint_as_float(v16 << 16); }
typedef __bf16 bf16x2_t __attribute__((ext_vector_type(2)));
__device__ __forceinline__ unsigned pk_bf16(float lo, float hi) {
    const f32x2 v = {lo, hi};
    return __builtin_bit_cast(unsigned, __builtin_convertvector(v, bf16x2_t));
}
__device__ __forceinline__ s16x4 tr_read(LAS const char* p) {
    return __builtin_amdgcn_ds_read_tr16_b64_v4i16((LAS s16x4*)p);
}
__device__ __forceinline__ void glds16(const void* sbase, unsigned voff, unsigned lds_addr) {
    asm volatile("s_mov_b32 m0, %2\n\ts_nop 0\n\tglobal_load_lds_dwordx4 %1, %0"
                 :: "s"(sbase), "v"(voff), "s"(lds_addr) : "memory");
}
__device__ __forceinline__ float dot2(unsigned a, unsigned b, float acc) {
    return __builtin_amdgcn_fdot2_f32_bf16(__builtin_bit_cast(bf16x2_t, a), __builtin_bit_cast(bf16x2_t, b), acc, false);
}
__device__ __forceinline__ void frag_sum(const bf16x8& f, float& s1) {
    const u32x4 u = __builtin_bit_cast(u32x4, f);
#pragma unroll
    for (int e = 0; e < 4; ++e) s1 = dot2(u[e], 0x3f803f80u, s1);
}
__device__ __forceinline__ void frag_sum_sq(const bf16x8& f, float& s1, float& s2) {
    const u32x4 u = __builtin_bit_cast(u32x4, f);
#pragma unroll
    for (int e = 0; e < 4; ++e) { s1 = dot2(u[e], 0x3f803f80u, s1); s2 = dot2(u[e], u[e], s2); }
}
__device__ __forceinline__ float red_g(float v) { v += __shfl_xor(v, 16); v += __shfl_xor(v, 32); return v; }
__device__ __forceinline__ void prefetch_region(const char* base, int lpr, long pitch, int chunk, int part, int tid,
                                                int nthreads, unsigned& dummy) {
    for (int l = tid; l < chunk; l += nthreads) {
        const int L = part * chunk + l, row = L / lpr, c = L - row * lpr;
        const char* a = base + (long)row * pitch + c * 128;
        asm volatile("global_load_dword %0, %1, off" : "+v"(dummy) : "v"(a) : "memory");
    }
}
#define ST8(ptr, v) (*(u32x2*)(ptr) = (v))
__device__ __forceinline__ void st16_sc1(void* p, u32x4 v) { asm volatile("global_store_dwordx4 %0, %1, off sc1\n\ts_nop 1" :: "v"(p), "v"(v) : "memory"); }
#define ST16(ptr, v) st16_sc1((void*)(ptr), (v))
template <int N> __device__ __forceinline__ void wait_vm() { asm volatile("s_waitcnt vmcnt(%0)" ::"n"(N) : "memory"); }

#define G4RM 4
#define G4RN 2
#define G1RM 2
#define G1RN 4
#define KR1 0
#define KR2 0
#define KR4 0
#define NOCS 0
enum { EPI_STORE = 0, EPI_RES = 1, EPI_RES_STAT = 2 };

template <int BM, int BN, int WM, int WN, int NST, int EPI, int LNF, int RM, int RN, int KROT>
__global__ __launch_bounds__(WM * WN * 64) void gemm_kernel(
    const bf16_t* __restrict__ X, const bf16_t* __restrict__ W0,
    const bf16_t* __restrict__ R, bf16_t* __restrict__ C, float* __restrict__ stat_out,
    int M, int N, int K, int ldw, int ldc) {
    constexpr int NWAVE = WM * WN;
    constexpr int MF = BM / 16 / WM, NF = BN / 16 / WN, NB = BN / 16;
    constexpr int A_BYTES = BM * 128, B_BYTES = BN * 128, ST_BYTES = A_BYTES + B_BYTES;
    constexpr int LPW = (BM + BN) / 8 / NWAVE;
    constexpr int LPA = BM / 8 / NWAVE;
    static_assert((BM + BN) % (8 * NWAVE) == 0 && (BM % (8 * NWAVE)) == 0, "piece split");
    static_assert(NB % 4 == 0, "NB%4");
    static_assert(!LNF || (MF == WN && WM == 2), "LN fold layout");
    static_assert(EPI != EPI_STORE || (BM * 4 == NWAVE * 64), "head-major store layout");
    extern __shared__ __attribute__((aligned(16))) char smem[];
    LAS char* lds = (LAS char*)smem;

    const int tid = threadIdx.x, lane = tid & 63;
    const int wave = __builtin_amdgcn_readfirstlane(tid >> 6);
    const int wm = wave / WN, wn = wave % WN;

    const int tiles_m = M / BM, tiles_n = N / BN;
    int tm, tn;
    {
        const int xcd = blockIdx.x & 7, idx = blockIdx.x >> 3;
        const int rm = xcd / RN, rn = xcd % RN;
        const int sm = tiles_m / RM, sn = tiles_n / RN;
        tm = rm * sm + idx % sm; tn = rn * sn + idx / sm;
    }
    const int m0 = tm * BM, n0 = tn * BN;

    unsigned voff[LPW];
    const char* sbase[LPW];
#pragma unroll
    for (int j = 0; j < LPW; ++j) {
        const int ii = wave + NWAVE * j;
        if (j < LPA) {
            const int row = ii * 8 + (lane >> 3), ch = (lane & 7) ^ (row & 7);
            voff[j] = (unsigned)(row * K * 2 + ch * 16);
            sbase[j] = (const char*)(X + (size_t)m0 * K);
        } else {
            const int pi = ii - BM / 8;
            const int kb = pi / (NB / 4), nb = 4 * (pi % (NB / 4)) + (lane >> 4);
            const int r = ((lane & 15) >> 1) ^ (4 * (kb & 1)), half = lane & 1;
            voff[j] = (unsigned)(((8 * kb + r) * ldw + 16 * nb + 8 * half) * 2);
            sbase[j] = (const char*)(W0 + n0);
        }
    }
    const unsigned lds_base = (unsigned)(uintptr_t)lds;
    const int NT = K / 64;
    auto issue_piece = [&](int buf, int j) {
        const int ii = wave + NWAVE * j;
        glds16(sbase[j], voff[j], lds_base + buf * ST_BYTES + ii * 1024);
        sbase[j] += (j < LPA) ? 128 : (long)128 * ldw;
    };
    auto issue = [&](int buf) {
#pragma unroll
        for (int j = 0; j < LPW; ++j) issue_piece(buf, j);
    };

    const int fr = lane & 15, g = lane >> 4, q = (lane & 15) >> 2, p = lane & 3;
    const int rotm = LNF ? wn : 0, rotk = LNF ? wm : 0;
    const int xrow = wm * (MF * 16) + fr;
    const int xoff0 = xrow * 128 + ((g ^ (xrow & 7)) << 4);
    const int woff0 = A_BYTES + g * NB * 256 + (q + 4 * (g & 1)) * 32 + p * 8 + wn * NF * 256;

    f32x4 acc[MF][NF];
#pragma unroll
    for (int i = 0; i < MF; ++i)
#pragma unroll
        for (int j = 0; j < NF; ++j) acc[i][j] = (f32x4){0.f, 0.f, 0.f, 0.f};
    f32x4 s1 = {0.f, 0.f, 0.f, 0.f}, s2 = {0.f, 0.f, 0.f, 0.f}, cs[NF];
#pragma unroll
    for (int j = 0; j < NF; ++j) cs[j] = (f32x4){0.f, 0.f, 0.f, 0.f};
    const bf16x8 ones = {0x3f80, 0x3f80, 0x3f80, 0x3f80, 0x3f80, 0x3f80, 0x3f80, 0x3f80};

#pragma unroll
    for (int s = 0; s < NST - 1; ++s) issue(s);

    constexpr int NRD = 2 * (NF + MF);
    constexpr int NMS = MF * NF + (LNF ? 2 : 0);
    constexpr int NM = 2 * NMS + (LNF ? NF : 0);
    auto read_one = [&](LAS const char* base, bf16x8 (&xf)[2][MF], bf16x8 (&wf)[2][NF], int r) {
        const int slot = r / (NF + MF), e = r % (NF + MF), ks = slot ^ rotk;
        if (e < NF) {
            const int o = ks * (4 * NB * 256) + e * 256;
            s16x4 lo = tr_read(base + woff0 + o);
            s16x4 hi = tr_read(base + (woff0 ^ 128) + o);
            wf[slot][e] = (bf16x8){lo[0], lo[1], lo[2], lo[3], hi[0], hi[1], hi[2], hi[3]};
        } else {
            const int i = e - NF;
            xf[slot][i] = *(LAS const bf16x8*)(base + ((xoff0 ^ (ks * 64)) + (i ^ rotm) * 2048));
        }
    };
    auto read_tile = [&](LAS const char* base, bf16x8 (&xf)[2][MF], bf16x8 (&wf)[2][NF]) {
#pragma unroll
        for (int r = 0; r < NRD; ++r) read_one(base, xf, wf, r);
    };
    auto mfma_one = [&](bf16x8 (&xf)[2][MF], bf16x8 (&wf)[2][NF], int f) {
        if (f >= 2 * NMS) { const int j = f - 2 * NMS; cs[j] = __builtin_amdgcn_mfma_f32_16x16x32_bf16(wf[0][j], ones, cs[j], 0, 0, 0); return; }
        const int slot = f / NMS, e = f % NMS;
        if (e < MF * NF) {
            const int i = e / NF, j = e % NF;
            acc[i][j] = __builtin_amdgcn_mfma_f32_16x16x32_bf16(wf[slot][j], xf[slot][i], acc[i][j], 0, 0, 0);
        } else if (e == MF * NF) {
            s1 = __builtin_amdgcn_mfma_f32_16x16x32_bf16(ones, xf[slot][0], s1, 0, 0, 0);
        } else {
            s2 = __builtin_amdgcn_mfma_f32_16x16x32_bf16(xf[slot][0], xf[slot][0], s2, 0, 0, 0);
        }
    };
    bf16x8 xa[2][MF], wa[2][NF], xb[2][MF], wb[2][NF];
    wait_vm<(NST - 2) * LPW>();
    __builtin_amdgcn_s_barrier();
    asm volatile("" ::: "memory");
    read_tile(lds, xa, wa);

    int buf = 0, pbuf = NST - 1;
    auto step = [&](int mode, int kt, bf16x8 (&xc)[2][MF], bf16x8 (&wc)[2][NF], bf16x8 (&xn)[2][MF], bf16x8 (&wn2)[2][NF]) {
        if (mode == 0) {
            wait_vm<(NST - 3) * LPW>();
        } else {
            const int rem = NT - 2 - kt;
            if (rem >= NST - 3) wait_vm<(NST - 3) * LPW>();
            else if (NST > 4 && rem == 1) wait_vm<LPW>();
            else if (NST > 5 && rem == 2) wait_vm<2 * LPW>();
            else wait_vm<0>();
        }
        __builtin_amdgcn_s_barrier();
        asm volatile("" ::: "memory");
        const bool ld = (mode == 0) || (kt + NST - 1 < NT);
        const int ibuf = pbuf;
        pbuf = buf;
        buf = (buf + 1 == NST) ? 0 : buf + 1;
        LAS const char* nbase = lds + buf * ST_BYTES;
        if (ld) issue(ibuf);
        __builtin_amdgcn_sched_barrier(0);
        read_tile(nbase, xn, wn2);
#pragma unroll
        for (int f = 0; f < NM; ++f) mfma_one(xc, wc, f);
        {
            constexpr int NR = 2 * (MF + 2 * NF), RPM = (NR + NM - 1) / NM;
#pragma unroll
            for (int f = 0; f < NM; ++f) {
                __builtin_amdgcn_sched_group_barrier(0x8, 1, 0);
                __builtin_amdgcn_sched_group_barrier(0x100, RPM, 0);
            }
        }
        __builtin_amdgcn_sched_barrier(0);
    };
    int kt = 0;
    for (; kt + NST < NT; kt += 2) {
        step(0, kt, xa, wa, xb, wb);
        step(0, kt + 1, xb, wb, xa, wa);
    }
    for (; kt < NT; kt += 2) {
        step(1, kt, xa, wa, xb, wb);
        step(1, kt + 1, xb, wb, xa, wa);
    }

    constexpr int NTHR = NWAVE * 64, CPR = BN / 8, NCH = BM * CPR / NTHR;
    constexpr int RS = (EPI == EPI_STORE) ? BN * 2 + 16 : BN * 4 + 16;
    LAS float* rowstat = (LAS float*)(lds + BM * RS);
    LAS float* colsum = rowstat + BM * 2;
    u32x4 rres[NCH];
    if (EPI != EPI_STORE) {
#pragma unroll
        for (int r = 0; r < NCH; ++r) {
            const int c = tid + NTHR * r, row = c / CPR, c8 = c % CPR;
            rres[r] = *(const u32x4*)(R + (size_t)(m0 + row) * ldc + n0 + c8 * 8);
        }
    }
    __syncthreads();
    if (LNF) {
        if (g == (fr >> 2)) {
            const int r = wm * (MF * 16) + wn * 16 + fr;
            const int d = fr & 3;
            const float sq = d == 0 ? s2[0] : d == 1 ? s2[1] : d == 2 ? s2[2] : s2[3];
            const float mean = s1[0] * (1.0f / D_MODEL);
            const float rstd = __builtin_amdgcn_rsqf(fmaxf(sq * (1.0f / D_MODEL) - mean * mean, 0.f) + 1e-5f);
            rowstat[2 * r] = mean * rstd;
            rowstat[2 * r + 1] = rstd;
        }
        if (fr == 0) {
#pragma unroll
            for (int j = 0; j < NF; ++j) *(LAS f32x4*)(colsum + wm * BN + (wn * NF + j) * 16 + 4 * g) = cs[j];
        }
        __syncthreads();
    }
    f32x4 csv[NF];
    if (LNF) {
#pragma unroll
        for (int j = 0; j < NF; ++j) {
            const int c = (wn * NF + j) * 16 + 4 * g;
            csv[j] = *(LAS const f32x4*)(colsum + c) + *(LAS const f32x4*)(colsum + BN + c);
        }
    }
#pragma unroll
    for (int i = 0; i < MF; ++i) {
        const int rl = wm * (MF * 16) + (i ^ rotm) * 16 + fr;
        float mr = 0.f, rstd = 1.f;
        if (LNF) { mr = rowstat[2 * rl]; rstd = rowstat[2 * rl + 1]; }
#pragma unroll
        for (int j = 0; j < NF; ++j) {
            const int nl = (wn * NF + j) * 16 + 4 * g;
            f32x4 v = acc[i][j];
            if (LNF) v = v * rstd - csv[j] * mr;
            if (EPI == EPI_STORE) {
                u32x2 w; w[0] = pk_bf16(v[0], v[1]); w[1] = pk_bf16(v[2], v[3]);
                *(LAS u32x2*)(lds + rl * RS + nl * 2) = w;
            } else {
                *(LAS f32x4*)(lds + rl * RS + nl * 4) = v;
            }
        }
    }
    __syncthreads();
#pragma unroll
    for (int r = 0; r < NCH; ++r) {
        const int c = tid + NTHR * r, row = c / CPR, c8 = c % CPR;
        u32x4 w;
        if (EPI == EPI_STORE) {
            const int hrow = tid >> 2, d8 = tid & 3;
            w = *(LAS const u32x4*)(lds + hrow * RS + r * 64 + d8 * 16);
            ST16(C + ((size_t)(n0 / 32 + r) * M + m0 + hrow) * 32 + d8 * 8, w);
            continue;
        } else {
            f32x4 v0 = *(LAS const f32x4*)(lds + row * RS + c8 * 32), v1 = *(LAS const f32x4*)(lds + row * RS + c8 * 32 + 16);
            const u32x4 rv = rres[r];
            v0[0] += bf2f(rv[0] & 0xffffu); v0[1] += bf2f(rv[0] >> 16); v0[2] += bf2f(rv[1] & 0xffffu); v0[3] += bf2f(rv[1] >> 16);
            v1[0] += bf2f(rv[2] & 0xffffu); v1[1] += bf2f(rv[2] >> 16); v1[2] += bf2f(rv[3] & 0xffffu); v1[3] += bf2f(rv[3] >> 16);
            w[0] = pk_bf16(v0[0], v0[1]); w[1] = pk_bf16(v0[2], v0[3]); w[2] = pk_bf16(v1[0], v1[1]); w[3] = pk_bf16(v1[2], v1[3]);
        }
        ST16(C + (size_t)(m0 + row) * ldc + n0 + c8 * 8, w);
        if (EPI == EPI_RES_STAT) {
            float a = 0.f, b = 0.f;
#pragma unroll
            for (int e = 0; e < 4; ++e) {
                const float x0 = bf2f(w[e] & 0xffffu), x1 = bf2f(w[e] >> 16);
                a += x0 + x1; b += x0 * x0 + x1 * x1;
            }
#pragma unroll
            for (int o = 1; o < CPR; o <<= 1) { a += __shfl_xor(a, o); b += __shfl_xor(b, o); }
            if (c8 == 0) { float2 o2; o2.x = a; o2.y = b; *(float2*)(stat_out + ((size_t)(m0 + row) * tiles_n + tn) * 2) = o2; }
        }
    }
}

__global__ __launch_bounds__(512, 2) void glu_gemm_kernel(
    const bf16_t* __restrict__ X, const bf16_t* __restrict__ W1, const bf16_t* __restrict__ W3,
    const float* __restrict__ stat_in, bf16_t* __restrict__ C, const bf16_t* __restrict__ pfw) {
    constexpr int K = D_MODEL, LDW = F_DIM, HTB = 16384;
    if (blockIdx.x >= 224) {
        extern __shared__ __attribute__((aligned(16))) char smem_pf[];
        const unsigned lb = (unsigned)(uintptr_t)(LAS char*)smem_pf + __builtin_amdgcn_readfirstlane(threadIdx.x >> 6) * 1024;
        const char* a = (const char*)pfw + (size_t)(blockIdx.x - 224) * 229376;
#pragma unroll
        for (int r = 0; r < 28; ++r) glds16(a + r * 8192, threadIdx.x * 16, lb + (r & 7) * 8192);
        wait_vm<0>();
        return;
    }
    extern __shared__ __attribute__((aligned(16))) char smem[];
    LAS char* lds = (LAS char*)smem;
    const unsigned lds_base = (unsigned)(uintptr_t)lds;
    const int tid = threadIdx.x, lane = tid & 63;
    const int wave = __builtin_amdgcn_readfirstlane(tid >> 6);
    const int wr = wave >> 2, wc = wave & 3;
    int tm, tn;
    { const int xcd = blockIdx.x & 7, idx = blockIdx.x >> 3; tm = (xcd >> 2) * 4 + (idx & 3); tn = (xcd & 3) * 7 + (idx >> 2); }
    const int m0 = tm * 256, n0 = tn * 128;

    unsigned xoffs[2], woffs[2];
#pragma unroll
    for (int j = 0; j < 2; ++j) {
        const int pc = wave + 8 * j;
        const int row = pc * 8 + (lane >> 3), ch = (lane & 7) ^ (row & 7);
        xoffs[j] = (unsigned)(row * K * 2 + ch * 16);
        const int kb = pc >> 1, nb = 4 * (pc & 1) + (lane >> 4);
        const int r = ((lane & 15) >> 1) ^ (4 * (kb & 1)), half = lane & 1;
        woffs[j] = (unsigned)(((8 * kb + r) * LDW + 16 * nb + 8 * half) * 2);
    }
    const char* xsrc = (const char*)(X + (size_t)m0 * K);
    const char* w1src = (const char*)(W1 + n0);
    const char* w3src = (const char*)(W3 + n0);
#define SA_OFF(b, h) (((b) * 2 + (h)) * HTB)
#define SB_OFF(b, h) ((4 + (b) * 2 + (h)) * HTB)
#define STAGE_A(b, h, kt) do { const char* _s = xsrc + (size_t)(h) * 128 * K * 2 + (kt) * 128; \
        glds16(_s, xoffs[0], lds_base + SA_OFF(b, h) + wave * 1024); glds16(_s, xoffs[1], lds_base + SA_OFF(b, h) + (wave + 8) * 1024); } while (0)
#define STAGE_B(b, h, kt) do { const char* _s = ((h) ? w3src : w1src) + (size_t)(kt) * 64 * LDW * 2; \
        glds16(_s, woffs[0], lds_base + SB_OFF(b, h) + wave * 1024); glds16(_s, woffs[1], lds_base + SB_OFF(b, h) + (wave + 8) * 1024); } while (0)
    const int fr = lane & 15, g = lane >> 4, q = (lane & 15) >> 2, p = lane & 3;
    const int xrow = wr * 64 + fr;
    const int xo0 = (xrow * 128 + ((g ^ (xrow & 7)) << 4)) ^ (wr * 64);
    const int wo0 = g * 2048 + (q + 4 * (g & 1)) * 32 + p * 8 + wc * 512;
    unsigned xb_[4][2], wb_[2][2];
#pragma unroll
    for (int m = 0; m < 4; ++m)
#pragma unroll
        for (int k = 0; k < 2; ++k) { xb_[m][k] = lds_base + ((xo0 ^ (k * 64)) + m * 2048); asm volatile("" : "+v"(xb_[m][k])); }
#pragma unroll
    for (int t = 0; t < 2; ++t)
#pragma unroll
        for (int k = 0; k < 2; ++k) { wb_[t][k] = lds_base + 4 * HTB + ((wo0 ^ (t * 128)) + (k ^ wr) * 8192); asm volatile("" : "+v"(wb_[t][k])); }
#define LDSP(a) ((LAS const char*)(uintptr_t)(a))
#define LDA(dst, b, h) do { _Pragma("unroll") for (int m = 0; m < 4; ++m) _Pragma("unroll") for (int k = 0; k < 2; ++k) \
        dst[m][k] = *(LAS const bf16x8*)(LDSP(xb_[m][k]) + SA_OFF(b, h)); } while (0)
#define LDB(dst, b, h) do { _Pragma("unroll") for (int n = 0; n < 2; ++n) _Pragma("unroll") for (int k = 0; k < 2; ++k) { \
        s16x4 lo_ = tr_read(LDSP(wb_[0][k]) + SA_OFF(b, h) + n * 256); \
        s16x4 hi_ = tr_read(LDSP(wb_[1][k]) + SA_OFF(b, h) + n * 256); \
        dst[n][k] = (bf16x8){lo_[0], lo_[1], lo_[2], lo_[3], hi_[0], hi_[1], hi_[2], hi_[3]}; } } while (0)
#define MMA(ai, bj, At, Bt, CS) do { __builtin_amdgcn_s_setprio(1); \
        _Pragma("unroll") for (int m = 0; m < 4; ++m) _Pragma("unroll") for (int n = 0; n < 2; ++n) _Pragma("unroll") for (int k = 0; k < 2; ++k) \
            acc[ai][bj][m][n] = __builtin_amdgcn_mfma_f32_16x16x32_bf16(Bt[n][k], At[m][k], acc[ai][bj][m][n], 0, 0, 0); \
        if (((CS) & 2) && !NOCS) { cs[bj][0] = __builtin_amdgcn_mfma_f32_16x16x32_bf16(Bt[0][0], ones, cs[bj][0], 0, 0, 0); \
                        cs[bj][1] = __builtin_amdgcn_mfma_f32_16x16x32_bf16(Bt[1][0], ones, cs[bj][1], 0, 0, 0); } \
        __builtin_amdgcn_s_setprio(0); } while (0)
#define WAIT_V(n) asm volatile("s_waitcnt vmcnt(" #n ")" ::: "memory")
#define WAIT_L(n) asm volatile("s_waitcnt lgkmcnt(" #n ")" ::: "memory")
#define BAR __builtin_amdgcn_s_barrier()
#define SCHED __builtin_amdgcn_sched_barrier(0)
    f32x4 acc[2][2][4][2] = {};
    bf16x8 At[4][2], B0[2][2], B1[2][2];
    f32x4 cs[2][2] = {};
    const bf16x8 ones = {0x3f80, 0x3f80, 0x3f80, 0x3f80, 0x3f80, 0x3f80, 0x3f80, 0x3f80};
    constexpr int nt = K / 64;
    {
        const char* ssrc = (const char*)(stat_in + (size_t)m0 * 32);
#pragma unroll
        for (int j = 0; j < 4; ++j) glds16(ssrc, (unsigned)((wave * 4 + j) * 1024 + lane * 16), lds_base + 8 * HTB + (wave * 4 + j) * 1024);
    }
    STAGE_B(0, 0, 0); STAGE_A(0, 0, 0); STAGE_B(0, 1, 0); STAGE_A(0, 1, 0);
    if (wr == 1) BAR;
    WAIT_V(4); BAR;
    STAGE_B(1, 0, 1); STAGE_A(1, 0, 1); STAGE_B(1, 1, 1);
    WAIT_V(6); BAR;
    LDB(B0, 0, 0);
    for (int t = 0; t < nt; t += 2) {
        const bool more = t + 2 < nt;
        LDA(At, 0, 0); STAGE_A(1, 1, t + 1);
        BAR; WAIT_L(0); MMA(0, 0, At, B0, 2); BAR; SCHED;
        LDB(B1, 0, 1); if (more) STAGE_B(0, 0, t + 2);
        BAR; WAIT_L(0); MMA(0, 1, At, B1, 1); BAR;
        LDA(At, 0, 1); if (more) { STAGE_A(0, 0, t + 2); WAIT_V(10); } else { WAIT_V(6); }
        BAR; WAIT_L(0); MMA(1, 0, At, B0, 1); BAR; SCHED;
        LDB(B0, 1, 0);
        if (more) { STAGE_B(0, 1, t + 2); WAIT_V(6); } else { WAIT_V(0); }
        BAR; MMA(1, 1, At, B1, 2); BAR; SCHED;
        LDA(At, 1, 0); if (more) STAGE_A(0, 1, t + 2);
        BAR; WAIT_L(0); MMA(0, 0, At, B0, 2); BAR; SCHED;
        LDB(B1, 1, 1); if (more) STAGE_B(1, 0, t + 3);
        BAR; WAIT_L(0); MMA(0, 1, At, B1, 1); BAR;
        LDA(At, 1, 1); if (more) { STAGE_A(1, 0, t + 3); WAIT_V(10); }
        BAR; WAIT_L(0); MMA(1, 0, At, B0, 1); BAR; SCHED;
        LDB(B0, 0, 0);
        if (more) { STAGE_B(1, 1, t + 3); WAIT_V(6); }
        BAR; MMA(1, 1, At, B1, 2); BAR; SCHED;
    }
    if (wr == 0) BAR;
    constexpr int RS = 272;
    LAS float* rowstat = (LAS float*)(lds + 256 * RS);
    LAS float* colsum = rowstat + 512;
    if (fr == 0) {
#pragma unroll
        for (int b = 0; b < 2; ++b)
#pragma unroll
            for (int n = 0; n < 2; ++n) *(LAS f32x4*)(colsum + wr * 256 + b * 128 + wc * 32 + n * 16 + 4 * g) = cs[b][n];
    }
    if (tid < 256) {
        LAS const f32x4* sp = (LAS const f32x4*)(lds + 8 * HTB + tid * 128);
        f32x4 t = sp[tid & 7];
#pragma unroll
        for (int e = 1; e < 8; ++e) t += sp[e ^ (tid & 7)];
        const float mean = (t[0] + t[2]) * (1.0f / D_MODEL);
        const float rstd = __builtin_amdgcn_rsqf(fmaxf((t[1] + t[3]) * (1.0f / D_MODEL) - mean * mean, 0.f) + 1e-5f);
        rowstat[2 * tid] = mean * rstd; rowstat[2 * tid + 1] = rstd;
    }
    __syncthreads();
    f32x4 csv[2][2];
#pragma unroll
    for (int b = 0; b < 2; ++b)
#pragma unroll
        for (int n = 0; n < 2; ++n) {
            const int c = b * 128 + wc * 32 + n * 16 + 4 * g;
            csv[b][n] = *(LAS const f32x4*)(colsum + c) + *(LAS const f32x4*)(colsum + 256 + c);
        }
#pragma unroll
    for (int ai = 0; ai < 2; ++ai) {
#pragma unroll
        for (int m = 0; m < 4; ++m) {
            const int rl = ai * 128 + wr * 64 + m * 16 + fr;
            const float mr = rowstat[2 * rl], rstd = rowstat[2 * rl + 1];
#pragma unroll
            for (int n = 0; n < 2; ++n) {
                u32x2 w;
#pragma unroll
                for (int hh = 0; hh < 2; ++hh) {
                    const f32x2 ag = {acc[ai][0][m][n][2 * hh], acc[ai][0][m][n][2 * hh + 1]};
                    const f32x2 au = {acc[ai][1][m][n][2 * hh], acc[ai][1][m][n][2 * hh + 1]};
                    const f32x2 cg = {csv[0][n][2 * hh], csv[0][n][2 * hh + 1]};
                    const f32x2 cu = {csv[1][n][2 * hh], csv[1][n][2 * hh + 1]};
                    const f32x2 gv = ag * rstd - cg * mr;
                    const f32x2 uv = au * rstd - cu * mr;
                    const f32x2 t = gv * (-1.4426950408889634f);
                    f32x2 e; e.x = __builtin_amdgcn_exp2f(t.x); e.y = __builtin_amdgcn_exp2f(t.y);
                    const f32x2 d = e + 1.0f;
                    f32x2 rr; rr.x = __builtin_amdgcn_rcpf(d.x); rr.y = __builtin_amdgcn_rcpf(d.y);
                    const f32x2 o = (gv * uv) * rr;
                    w[hh] = pk_bf16(o.x, o.y);
                }
                *(LAS u32x2*)(lds + rl * RS + (wc * 32 + n * 16 + 4 * g) * 2) = w;
            }
        }
        asm volatile("s_waitcnt lgkmcnt(0)" ::: "memory");
        __builtin_amdgcn_s_barrier();
        asm volatile("" ::: "memory");
#pragma unroll
        for (int r = 0; r < 4; ++r) {
            const int c = tid + 512 * r, row = ai * 128 + (c >> 4), c8 = c & 15;
            ST16(C + (size_t)(m0 + row) * LDW + n0 + c8 * 8, *(LAS const u32x4*)(lds + row * RS + c8 * 16));
        }
        __builtin_amdgcn_sched_barrier(0);
    }
#undef SA_OFF
#undef SB_OFF
#undef STAGE_A
#undef STAGE_B
#undef LDA
#undef LDB
#undef MMA
}

template <int NST>
__global__ __launch_bounds__(512, 2) void glu2_kernel(
    const bf16_t* __restrict__ X, const bf16_t* __restrict__ W1, const bf16_t* __restrict__ W3,
    const float* __restrict__ stat_in, bf16_t* __restrict__ C) {
    constexpr int K = D_MODEL, LDW = F_DIM, STB = 32768, NT = K / 32;
    extern __shared__ __attribute__((aligned(16))) char smem[];
    LAS char* lds = (LAS char*)smem;
    const unsigned lds_base = (unsigned)(uintptr_t)lds;
    const int tid = threadIdx.x, lane = tid & 63;
    const int wave = __builtin_amdgcn_readfirstlane(tid >> 6);
    const int wm = wave >> 2, wn = wave & 3;
    int tm, tn;
    { const int xcd = blockIdx.x & 7, idx = blockIdx.x >> 3; tm = (xcd >> 2) * 4 + (idx & 3); tn = (xcd & 3) * 7 + (idx >> 2); }
    const int m0 = tm * 256, n0 = tn * 128;

    unsigned xoffs[2], woffs[2];
#pragma unroll
    for (int j = 0; j < 2; ++j) {
        const int pc = wave + 8 * j;
        const int row = pc * 16 + (lane >> 2), ch = (lane & 3) ^ ((4 - ((row >> 2) & 3)) & 3);
        xoffs[j] = (unsigned)(row * K * 2 + ch * 16);
        const int kb = pc >> 2, nb = 4 * (pc & 1) + (lane >> 4);
        const int r = ((lane & 15) >> 1) ^ (4 * (kb & 1)), half = lane & 1;
        woffs[j] = (unsigned)(((8 * kb + r) * LDW + 16 * nb + 8 * half) * 2);
    }
    const char* xsrc = (const char*)(X + (size_t)m0 * K);
    const char* wsrc = (const char*)(((wave & 2) ? W3 : W1) + n0);
    auto issue = [&](int buf) {
        const unsigned b = lds_base + buf * STB;
        glds16(xsrc, xoffs[0], b + wave * 1024);
        glds16(xsrc, xoffs[1], b + (wave + 8) * 1024);
        glds16(wsrc, woffs[0], b + 16384 + wave * 1024);
        glds16(wsrc, woffs[1], b + 16384 + (wave + 8) * 1024);
        xsrc += 64; wsrc += (size_t)32 * LDW * 2;
    };
    const int fr = lane & 15, g = lane >> 4, q = (lane & 15) >> 2, p = lane & 3;
    const int xo0 = (wm * 128 + fr) * 64 + ((g ^ ((4 - (fr >> 2)) & 3)) << 4);
    const int wo0 = 16384 + g * 4096 + (q + 4 * (g & 1)) * 32 + p * 8 + wn * 512;

    f32x4 acc[8][4];
#pragma unroll
    for (int i = 0; i < 8; ++i)
#pragma unroll
        for (int j = 0; j < 4; ++j) acc[i][j] = (f32x4){0.f, 0.f, 0.f, 0.f};
    f32x4 cs[4] = {};
    const bf16x8 ones = {0x3f80, 0x3f80, 0x3f80, 0x3f80, 0x3f80, 0x3f80, 0x3f80, 0x3f80};

    {
        const char* ssrc = (const char*)(stat_in + (size_t)m0 * 32);
#pragma unroll
        for (int j = 0; j < 4; ++j) glds16(ssrc, (unsigned)((wave * 4 + j) * 1024 + lane * 16), lds_base + NST * STB + (wave * 4 + j) * 1024);
    }
#pragma unroll
    for (int s = 0; s < NST; ++s) issue(s);

    auto read_frags = [&](LAS const char* base, bf16x8 (&xf)[8], bf16x8 (&wf)[4]) {
#pragma unroll
        for (int j = 0; j < 4; ++j) {
            const int o = (j & 1) * 256 + (j >> 1) * 2048;
            s16x4 lo = tr_read(base + wo0 + o);
            s16x4 hi = tr_read(base + (wo0 ^ 128) + o);
            wf[j] = (bf16x8){lo[0], lo[1], lo[2], lo[3], hi[0], hi[1], hi[2], hi[3]};
        }
#pragma unroll
        for (int i = 0; i < 8; ++i) xf[i] = *(LAS const bf16x8*)(base + xo0 + i * 1024);
    };
    auto do_mfma = [&](bf16x8 (&xf)[8], bf16x8 (&wf)[4]) {
#pragma unroll
        for (int i = 0; i < 8; ++i)
#pragma unroll
            for (int j = 0; j < 4; ++j)
                acc[i][j] = __builtin_amdgcn_mfma_f32_16x16x32_bf16(wf[j], xf[i], acc[i][j], 0, 0, 0);
    };
    auto interleave = [&]() {
#pragma unroll
        for (int i = 0; i < 16; ++i) {
            __builtin_amdgcn_sched_group_barrier(0x8, 2, 0);
            __builtin_amdgcn_sched_group_barrier(0x100, 1, 0);
        }
    };
    auto colsum = [&](bf16x8 (&wf)[4]) {
#pragma unroll
        for (int j = 0; j < 4; ++j) cs[j] = __builtin_amdgcn_mfma_f32_16x16x32_bf16(wf[j], ones, cs[j], 0, 0, 0);
    };
    bf16x8 xa[8], wa[4], xb[8], wb[4];
    wait_vm<(NST - 1) * 4>();
    __builtin_amdgcn_s_barrier();
    asm volatile("" ::: "memory");
    read_frags(lds, xa, wa);
    int buf = 0;
    auto step = [&](int kt, bf16x8 (&xc)[8], bf16x8 (&wc_)[4], bf16x8 (&xn)[8], bf16x8 (&wn_)[4], bool docs) {
        const int rem = NT - 2 - kt;
        if (rem >= NST - 2) wait_vm<(NST - 2) * 4>();
        else if (rem == 1) wait_vm<4>();
        else if (NST > 4 && rem == 2) wait_vm<8>();
        else wait_vm<0>();
        asm volatile("s_waitcnt lgkmcnt(0)" ::: "memory");
        __builtin_amdgcn_s_barrier();
        asm volatile("" ::: "memory");
        if (kt + NST < NT) issue(buf);
        buf = (buf + 1 == NST) ? 0 : buf + 1;
        __builtin_amdgcn_sched_barrier(0);
        read_frags(lds + buf * STB, xn, wn_);
        do_mfma(xc, wc_);
        interleave();
        __builtin_amdgcn_sched_barrier(0);
        if (docs) colsum(wc_);
        __builtin_amdgcn_sched_barrier(0);
    };
    for (int kt = 0; kt < NT; kt += 2) {
        step(kt, xa, wa, xb, wb, wm == 0);
        step(kt + 1, xb, wb, xa, wa, wm == 1);
    }
    __syncthreads();
    LAS float* rowstat = (LAS float*)lds;
    LAS float* colsm = (LAS float*)(lds + 2048);
    if (fr == 0) {
#pragma unroll
        for (int j = 0; j < 4; ++j) *(LAS f32x4*)(colsm + wm * 256 + (j >> 1) * 128 + wn * 32 + (j & 1) * 16 + 4 * g) = cs[j];
    }
    if (tid < 256) {
        LAS const f32x4* sp = (LAS const f32x4*)(lds + NST * STB + tid * 128);
        f32x4 t = sp[tid & 7];
#pragma unroll
        for (int e = 1; e < 8; ++e) t += sp[e ^ (tid & 7)];
        const float mean = (t[0] + t[2]) * (1.0f / D_MODEL);
        const float rstd = 1.0f / sqrtf(fmaxf((t[1] + t[3]) * (1.0f / D_MODEL) - mean * mean, 0.f) + 1e-5f);
        rowstat[2 * tid] = mean; rowstat[2 * tid + 1] = rstd;
    }
    __syncthreads();
    f32x4 csv[4];
#pragma unroll
    for (int j = 0; j < 4; ++j) {
        const int c = (j >> 1) * 128 + wn * 32 + (j & 1) * 16 + 4 * g;
        csv[j] = *(LAS const f32x4*)(colsm + c) + *(LAS const f32x4*)(colsm + 256 + c);
    }
#pragma unroll
    for (int i = 0; i < 8; ++i) {
        const int rl = wm * 128 + i * 16 + fr;
        const float mean = rowstat[2 * rl], rstd = rowstat[2 * rl + 1];
#pragma unroll
        for (int n = 0; n < 2; ++n) {
            const f32x4 gv = (acc[i][n] - csv[n] * mean) * rstd;
            const f32x4 uv = (acc[i][n + 2] - csv[n + 2] * mean) * rstd;
            float o[4];
#pragma unroll
            for (int r = 0; r < 4; ++r) o[r] = gv[r] * __builtin_amdgcn_rcpf(1.0f + __expf(-gv[r])) * uv[r];
            u32x2 w; w[0] = pk_bf16(o[0], o[1]); w[1] = pk_bf16(o[2], o[3]);
            *(u32x2*)(C + (size_t)(m0 + rl) * LDW + n0 + wn * 32 + n * 16 + 4 * g) = w;
        }
    }
}

__global__ __launch_bounds__(512) void attn_kernel(const bf16_t* __restrict__ qkv, bf16_t* __restrict__ O,
                                                   const bf16_t* __restrict__ pf0) {
    extern __shared__ __attribute__((aligned(16))) char smem[];
    LAS char* lds = (LAS char*)smem;
    LAS char* ldsK = lds;
    LAS char* ldsV = lds + 32768;
    const unsigned lds_base = (unsigned)(uintptr_t)lds;
    const int tid = threadIdx.x, lane = tid & 63;
    const int w = __builtin_amdgcn_readfirstlane(tid >> 6);
    const int h = blockIdx.x & 31, qb = blockIdx.x >> 5;
    const int q0 = qb * 256, kbase = q0 - 256;
    const bf16_t* Qh = qkv + (size_t)h * (S_LEN * 32);
    const bf16_t* Kh = qkv + (size_t)(32 + h) * (S_LEN * 32);
    const bf16_t* Vh = qkv + (size_t)(64 + h) * (S_LEN * 32);

    {
        const unsigned koff = (unsigned)((lane >> 2) * 64 + (((lane & 3) ^ ((lane >> 4) & 3)) << 4));
        const unsigned voff = (unsigned)(lane * 16);
#pragma unroll
        for (int i = 0; i < 4; ++i) {
            const int pc = w + 8 * i;
            if (kbase + pc * 16 >= 0) {
                glds16((const char*)(Kh + (long)(kbase + pc * 16) * 32), koff, lds_base + pc * 1024);
                glds16((const char*)(Vh + (long)(kbase + pc * 16) * 32), voff, lds_base + 32768 + pc * 1024);
            }
        }
    }
    const int r32 = lane & 31, hi = lane >> 5;
    const int qw = q0 + 32 * w;
    bf16x8 qf[2];
#pragma unroll
    for (int s = 0; s < 2; ++s) qf[s] = *(const bf16x8*)(Qh + (size_t)(qw + r32) * 32 + 16 * s + 8 * hi);
    wait_vm<0>();
    __syncthreads();
    glds16((const char*)pf0 + (size_t)(blockIdx.x * 8 + w) * 1024, (unsigned)(lane * 16), lds_base + 65536 + w * 1024);

    auto body = [&](auto full_tag) {
    constexpr bool FULL = decltype(full_tag)::value;
    f32x16 sc[9];
    const float NEG = -INFINITY;
#pragma unroll
    for (int j = 0; j < 9; ++j) {
        const int kb = qw - 256 + 32 * j;
        if (FULL || kb >= 0) {
            const int key = 32 * (w + j) + r32;
            f32x16 a = {};
#pragma unroll
            for (int s = 0; s < 2; ++s) {
                const bf16x8 kf = *(LAS const bf16x8*)(ldsK + key * 64 + (((2 * s + hi) ^ ((key >> 2) & 3)) << 4));
                a = __builtin_amdgcn_mfma_f32_32x32x16_bf16(kf, qf[s], a, 0, 0, 0);
            }
            sc[j] = a;
        } else {
#pragma unroll
            for (int r = 0; r < 16; ++r) sc[j][r] = NEG;
        }
    }
#pragma unroll
    for (int r = 0; r < 16; ++r) {
        const int i = (r & 3) + 8 * (r >> 2) + 4 * hi;
        if (!(i > r32)) sc[0][r] = NEG;
        if (!(i <= r32)) sc[8][r] = NEG;
    }
    float mxa = NEG, mxb = NEG;
#pragma unroll
    for (int j = 0; j < 9; ++j)
#pragma unroll
        for (int r = 0; r < 16; r += 4) {
            mxa = __builtin_fmaxf(__builtin_fmaxf(mxa, sc[j][r]), sc[j][r + 1]);
            mxb = __builtin_fmaxf(__builtin_fmaxf(mxb, sc[j][r + 2]), sc[j][r + 3]);
        }
    float mx = fmaxf(mxa, mxb);
    mx = fmaxf(mx, __shfl_xor(mx, 32));
    const float c = 0.17677669529663687f * 1.4426950408889634f;
    const float nm = -mx * c;

    f32x16 o = {}, ls = {};
    const bf16x8 ones = {0x3f80, 0x3f80, 0x3f80, 0x3f80, 0x3f80, 0x3f80, 0x3f80, 0x3f80};
    const int gq = (lane & 15) >> 2, gp = lane & 3, gd = (lane >> 4) & 1;
#pragma unroll
    for (int j = 0; j < 9; ++j) {
        const int kb = qw - 256 + 32 * j;
        if (FULL || kb >= 0) {
#pragma unroll
            for (int s = 0; s < 2; ++s) {
                u32x4 pw;
#pragma unroll
                for (int e = 0; e < 4; ++e)
                    pw[e] = pk_bf16(__builtin_amdgcn_exp2f(__builtin_fmaf(sc[j][8 * s + 2 * e], c, nm)),
                                    __builtin_amdgcn_exp2f(__builtin_fmaf(sc[j][8 * s + 2 * e + 1], c, nm)));
                const int key = 32 * (w + j) + 16 * s + 4 * hi + gq;
                LAS const char* vp = ldsV + key * 64 + (16 * gd + 4 * gp) * 2;
                s16x4 lo = tr_read(vp), hi8 = tr_read(vp + 8 * 64);
                const bf16x8 vf = (bf16x8){lo[0], lo[1], lo[2], lo[3], hi8[0], hi8[1], hi8[2], hi8[3]};
                const bf16x8 pf = __builtin_bit_cast(bf16x8, pw);
                o = __builtin_amdgcn_mfma_f32_32x32x16_bf16(vf, pf, o, 0, 0, 0);
                ls = __builtin_amdgcn_mfma_f32_32x32x16_bf16(ones, pf, ls, 0, 0, 0);
            }
        }
    }
    const float rl = 1.0f / ls[0];
    bf16_t* dst = O + (size_t)(qw + r32) * D_MODEL + h * 32 + 4 * hi;
#pragma unroll
    for (int t = 0; t < 4; ++t) {
        u32x2 wv; wv[0] = pk_bf16(o[4 * t] * rl, o[4 * t + 1] * rl); wv[1] = pk_bf16(o[4 * t + 2] * rl, o[4 * t + 3] * rl);
        ST8(dst + 8 * t, wv);
    }
    };
    if (qb > 0) body(std::integral_constant<bool, true>{}); else body(std::integral_constant<bool, false>{});
    wait_vm<0>();
}

template <typename KF> static void set_lds(KF kf, int bytes) {
    (void)hipFuncSetAttribute((const void*)kf, hipFuncAttributeMaxDynamicSharedMemorySize, bytes);
}

extern "C" void kernel_launch(void* const* d_in, const int* in_sizes, int n_in,
                              void* d_out, int out_size, void* d_ws, size_t ws_size,
                              hipStream_t stream) {
    const bf16_t* x = (const bf16_t*)d_in[0];
    const bf16_t* wqkv = (const bf16_t*)d_in[1];
    const bf16_t* wo = (const bf16_t*)d_in[2];
    const bf16_t* w1 = (const bf16_t*)d_in[4];
    const bf16_t* w3 = (const bf16_t*)d_in[5];
    const bf16_t* w2 = (const bf16_t*)d_in[6];
    bf16_t* out = (bf16_t*)d_out;
    char* ws = (char*)d_ws;
    bf16_t* qkv = (bf16_t*)(ws + (4u << 20));
    bf16_t* att = (bf16_t*)(ws + (16u << 20));
    bf16_t* hbuf = (bf16_t*)(ws + (20u << 20));
    bf16_t* act = (bf16_t*)(ws + (28u << 20));
    float* hstat = (float*)(ws + (24u << 20));

    {
        auto kf = gemm_kernel<128, 192, 2, 4, 4, EPI_STORE, 1, G1RM, G1RN, KR1>;
        constexpr int lds = 4 * (128 + 192) * 128;
        set_lds(kf, lds);
        kf<<<256, 512, lds, stream>>>(x, wqkv, nullptr, qkv, nullptr, S_LEN, 3072, 1024, 3072, 3072);
    }
    {
        set_lds(attn_kernel, 73728);
        attn_kernel<<<256, 512, 73728, stream>>>(qkv, att, wo);
    }
    {
        auto kf = gemm_kernel<128, 64, 4, 2, 6, EPI_RES_STAT, 0, 4, 2, KR2>;
        constexpr int lds = 6 * (128 + 64) * 128;
        set_lds(kf, lds);
        kf<<<256, 512, lds, stream>>>(att, wo, x, hbuf, hstat, S_LEN, 1024, 1024, 1024, 1024);
    }
    {
        set_lds(glu_gemm_kernel, 163840);
        glu_gemm_kernel<<<256, 512, 163840, stream>>>(hbuf, w1, w3, hstat, act, w2);
    }
    {
        auto kf = gemm_kernel<128, 64, 4, 2, 6, EPI_RES, 0, G4RM, G4RN, KR4>;
        constexpr int lds = 6 * (128 + 64) * 128;
        set_lds(kf, lds);
        kf<<<256, 512, lds, stream>>>(act, w2, hbuf, out, nullptr, S_LEN, 1024, F_DIM, 1024, 1024);
    }
}
```

```cpp
#include <hip/hip_runtime.h>
#include <hip/hip_bf16.h>
#include <stdint.h>
#include <type_traits>

typedef unsigned short bf16_t;
typedef short bf16x8 __attribute__((ext_vector_type(8)));
typedef short s16x4 __attribute__((ext_vector_type(4)));
typedef float f32x4 __attribute__((ext_vector_type(4)));
typedef float f32x16 __attribute__((ext_vector_type(16)));
typedef float f32x2 __attribute__((ext_vector_type(2)));
typedef unsigned u32x4 __attribute__((ext_vector_type(4)));
typedef unsigned u32x2 __attribute__((ext_vector_type(2)));
#define LAS __attribute__((address_space(3)))

constexpr int S_LEN = 2048, D_MODEL = 1024, F_DIM = 3584, N_HEAD = 32, D_HEAD = 32, WIN = 256;

__device__ __forceinline__ float bf2f(unsigned v16) { return __uint_as_float(v16 << 16); }
typedef __bf16 bf16x2_t __attribute__((ext_vector_type(2)));
__device__ __forceinline__ unsigned pk_bf16(float lo, float hi) {
    const f32x2 v = {lo, hi};
    return __builtin_bit_cast(unsigned, __builtin_convertvector(v, bf16x2_t));
}
__device__ __forceinline__ s16x4 tr_read(LAS const char* p) {
    return __builtin_amdgcn_ds_read_tr16_b64_v4i16((LAS s16x4*)p);
}
__device__ __forceinline__ void glds16(const void* sbase, unsigned voff, unsigned lds_addr) {
    asm volatile("s_mov_b32 m0, %2\n\ts_nop 0\n\tglobal_load_lds_dwordx4 %1, %0"
                 :: "s"(sbase), "v"(voff), "s"(lds_addr) : "memory");
}
__device__ __forceinline__ float dot2(unsigned a, unsigned b, float acc) {
    return __builtin_amdgcn_fdot2_f32_bf16(__builtin_bit_cast(bf16x2_t, a), __builtin_bit_cast(bf16x2_t, b), acc, false);
}
__device__ __forceinline__ void frag_sum(const bf16x8& f, float& s1) {
    const u32x4 u = __builtin_bit_cast(u32x4, f);
#pragma unroll
    for (int e = 0; e < 4; ++e) s1 = dot2(u[e], 0x3f803f80u, s1);
}
__device__ __forceinline__ void frag_sum_sq(const bf16x8& f, float& s1, float& s2) {
    const u32x4 u = __builtin_bit_cast(u32x4, f);
#pragma unroll
    for (int e = 0; e < 4; ++e) { s1 = dot2(u[e], 0x3f803f80u, s1); s2 = dot2(u[e], u[e], s2); }
}
__device__ __forceinline__ float red_g(float v) { v += __shfl_xor(v, 16); v += __shfl_xor(v, 32); return v; }
__device__ __forceinline__ void prefetch_region(const char* base, int lpr, long pitch, int chunk, int part, int tid,
                                                int nthreads, unsigned& dummy) {
    for (int l = tid; l < chunk; l += nthreads) {
        const int L = part * chunk + l, row = L / lpr, c = L - row * lpr;
        const char* a = base + (long)row * pitch + c * 128;
        asm volatile("global_load_dword %0, %1, off" : "+v"(dummy) : "v"(a) : "memory");
    }
}
#define ST8(ptr, v) (*(u32x2*)(ptr) = (v))
__device__ __forceinline__ void st16_sc1(void* p, u32x4 v) { asm volatile("global_store_dwordx4 %0, %1, off sc1\n\ts_nop 1" :: "v"(p), "v"(v) : "memory"); }
#define ST16(ptr, v) st16_sc1((void*)(ptr), (v))
template <int N> __device__ __forceinline__ void wait_vm() { asm volatile("s_waitcnt vmcnt(%0)" ::"n"(N) : "memory"); }

#define G4RM 4
#define G4RN 2
#define G1RM 2
#define G1RN 4
#define KR1 0
#define KR2 0
#define KR4 0
#define NOCS 0
enum { EPI_STORE = 0, EPI_RES = 1, EPI_RES_STAT = 2 };

template <int BM, int BN, int WM, int WN, int NST, int EPI, int LNF, int RM, int RN, int KROT>
__global__ __launch_bounds__(WM * WN * 64) void gemm_kernel(
    const bf16_t* __restrict__ X, const bf16_t* __restrict__ W0,
    const bf16_t* __restrict__ R, bf16_t* __restrict__ C, float* __restrict__ stat_out,
    int M, int N, int K, int ldw, int ldc) {
    constexpr int NWAVE = WM * WN;
    constexpr int MF = BM / 16 / WM, NF = BN / 16 / WN, NB = BN / 16;
    constexpr int A_BYTES = BM * 128, B_BYTES = BN * 128, ST_BYTES = A_BYTES + B_BYTES;
    constexpr int LPW = (BM + BN) / 8 / NWAVE;
    constexpr int LPA = BM / 8 / NWAVE;
    static_assert((BM + BN) % (8 * NWAVE) == 0 && (BM % (8 * NWAVE)) == 0, "piece split");
    static_assert(NB % 4 == 0, "NB%4");
    static_assert(!LNF || (MF == WN && WM == 2), "LN fold layout");
    static_assert(EPI != EPI_STORE || (BM * 4 == NWAVE * 64), "head-major store layout");
    extern __shared__ __attribute__((aligned(16))) char smem[];
    LAS char* lds = (LAS char*)smem;

    const int tid = threadIdx.x, lane = tid & 63;
    const int wave = __builtin_amdgcn_readfirstlane(tid >> 6);
    const int wm = wave / WN, wn = wave % WN;

    const int tiles_m = M / BM, tiles_n = N / BN;
    int tm, tn;
    {
        const int xcd = blockIdx.x & 7, idx = blockIdx.x >> 3;
        const int rm = xcd / RN, rn = xcd % RN;
        const int sm = tiles_m / RM, sn = tiles_n / RN;
        tm = rm * sm + idx % sm; tn = rn * sn + idx / sm;
    }
    const int m0 = tm * BM, n0 = tn * BN;

    unsigned voff[LPW];
    const char* sbase[LPW];
#pragma unroll
    for (int j = 0; j < LPW; ++j) {
        const int ii = wave + NWAVE * j;
        if (j < LPA) {
            const int row = ii * 8 + (lane >> 3), ch = (lane & 7) ^ (row & 7);
            voff[j] = (unsigned)(row * K * 2 + ch * 16);
            sbase[j] = (const char*)(X + (size_t)m0 * K);
        } else {
            const int pi = ii - BM / 8;
            const int kb = pi / (NB / 4), nb = 4 * (pi % (NB / 4)) + (lane >> 4);
            const int r = ((lane & 15) >> 1) ^ (4 * (kb & 1)), half = lane & 1;
            voff[j] = (unsigned)(((8 * kb + r) * ldw + 16 * nb + 8 * half) * 2);
            sbase[j] = (const char*)(W0 + n0);
        }
    }
    const unsigned lds_base = (unsigned)(uintptr_t)lds;
    const int NT = K / 64;
    auto issue_piece = [&](int buf, int j) {
        const int ii = wave + NWAVE * j;
        glds16(sbase[j], voff[j], lds_base + buf * ST_BYTES + ii * 1024);
        sbase[j] += (j < LPA) ? 128 : (long)128 * ldw;
    };
    auto issue = [&](int buf) {
#pragma unroll
        for (int j = 0; j < LPW; ++j) issue_piece(buf, j);
    };

    const int fr = lane & 15, g = lane >> 4, q = (lane & 15) >> 2, p = lane & 3;
    const int rotm = LNF ? wn : 0, rotk = LNF ? wm : 0;
    const int xrow = wm * (MF * 16) + fr;
    const int xoff0 = xrow * 128 + ((g ^ (xrow & 7)) << 4);
    const int woff0 = A_BYTES + g * NB * 256 + (q + 4 * (g & 1)) * 32 + p * 8 + wn * NF * 256;

    f32x4 acc[MF][NF];
#pragma unroll
    for (int i = 0; i < MF; ++i)
#pragma unroll
        for (int j = 0; j < NF; ++j) acc[i][j] = (f32x4){0.f, 0.f, 0.f, 0.f};
    f32x4 s1 = {0.f, 0.f, 0.f, 0.f}, s2 = {0.f, 0.f, 0.f, 0.f}, cs[NF];
#pragma unroll
    for (int j = 0; j < NF; ++j) cs[j] = (f32x4){0.f, 0.f, 0.f, 0.f};
    const bf16x8 ones = {0x3f80, 0x3f80, 0x3f80, 0x3f80, 0x3f80, 0x3f80, 0x3f80, 0x3f80};

    if (EPI != EPI_STORE) {
        static_assert(EPI == EPI_STORE || BN == 64, "residual piece = 8 rows x 128 B");
#pragma unroll
        for (int j = 0; j < BM / 8 / NWAVE; ++j) {
            const int pc = wave + NWAVE * j;
            glds16((const char*)(R + (size_t)(m0 + pc * 8) * ldc + n0), (unsigned)((lane >> 3) * ldc * 2 + (lane & 7) * 16),
                   lds_base + NST * ST_BYTES + pc * 1024);
        }
    }
#pragma unroll
    for (int s = 0; s < NST - 1; ++s) issue(s);

    constexpr int NRD = 2 * (NF + MF);
    constexpr int NMS = MF * NF + (LNF ? 2 : 0);
    constexpr int NM = 2 * NMS + (LNF ? NF : 0);
    auto read_one = [&](LAS const char* base, bf16x8 (&xf)[2][MF], bf16x8 (&wf)[2][NF], int r) {
        const int slot = r / (NF + MF), e = r % (NF + MF), ks = slot ^ rotk;
        if (e < NF) {
            const int o = ks * (4 * NB * 256) + e * 256;
            s16x4 lo = tr_read(base + woff0 + o);
            s16x4 hi = tr_read(base + (woff0 ^ 128) + o);
            wf[slot][e] = (bf16x8){lo[0], lo[1], lo[2], lo[3], hi[0], hi[1], hi[2], hi[3]};
        } else {
            const int i = e - NF;
            xf[slot][i] = *(LAS const bf16x8*)(base + ((xoff0 ^ (ks * 64)) + (i ^ rotm) * 2048));
        }
    };
    auto read_tile = [&](LAS const char* base, bf16x8 (&xf)[2][MF], bf16x8 (&wf)[2][NF]) {
#pragma unroll
        for (int r = 0; r < NRD; ++r) read_one(base, xf, wf, r);
    };
    auto mfma_one = [&](bf16x8 (&xf)[2][MF], bf16x8 (&wf)[2][NF], int f) {
        if (f >= 2 * NMS) { const int j = f - 2 * NMS; cs[j] = __builtin_amdgcn_mfma_f32_16x16x32_bf16(wf[0][j], ones, cs[j], 0, 0, 0); return; }
        const int slot = f / NMS, e = f % NMS;
        if (e < MF * NF) {
            const int i = e / NF, j = e % NF;
            acc[i][j] = __builtin_amdgcn_mfma_f32_16x16x32_bf16(wf[slot][j], xf[slot][i], acc[i][j], 0, 0, 0);
        } else if (e == MF * NF) {
            s1 = __builtin_amdgcn_mfma_f32_16x16x32_bf16(ones, xf[slot][0], s1, 0, 0, 0);
        } else {
            s2 = __builtin_amdgcn_mfma_f32_16x16x32_bf16(xf[slot][0], xf[slot][0], s2, 0, 0, 0);
        }
    };
    bf16x8 xa[2][MF], wa[2][NF], xb[2][MF], wb[2][NF];
    wait_vm<(NST - 2) * LPW>();
    __builtin_amdgcn_s_barrier();
    asm volatile("" ::: "memory");
    read_tile(lds, xa, wa);

    int buf = 0, pbuf = NST - 1;
    auto step = [&](int mode, int kt, bf16x8 (&xc)[2][MF], bf16x8 (&wc)[2][NF], bf16x8 (&xn)[2][MF], bf16x8 (&wn2)[2][NF]) {
        if (mode == 0) {
            wait_vm<(NST - 3) * LPW>();
        } else {
            const int rem = NT - 2 - kt;
            if (rem >= NST - 3) wait_vm<(NST - 3) * LPW>();
            else if (NST > 4 && rem == 1) wait_vm<LPW>();
            else if (NST > 5 && rem == 2) wait_vm<2 * LPW>();
            else wait_vm<0>();
        }
        __builtin_amdgcn_s_barrier();
        asm volatile("" ::: "memory");
        const bool ld = (mode == 0) || (kt + NST - 1 < NT);
        const int ibuf = pbuf;
        pbuf = buf;
        buf = (buf + 1 == NST) ? 0 : buf + 1;
        LAS const char* nbase = lds + buf * ST_BYTES;
        if (ld) issue(ibuf);
        __builtin_amdgcn_sched_barrier(0);
        read_tile(nbase, xn, wn2);
#pragma unroll
        for (int f = 0; f < NM; ++f) mfma_one(xc, wc, f);
        {
            constexpr int NR = 2 * (MF + 2 * NF), RPM = (NR + NM - 1) / NM;
#pragma unroll
            for (int f = 0; f < NM; ++f) {
                __builtin_amdgcn_sched_group_barrier(0x8, 1, 0);
                __builtin_amdgcn_sched_group_barrier(0x100, RPM, 0);
            }
        }
        __builtin_amdgcn_sched_barrier(0);
    };
    int kt = 0;
    for (; kt + NST < NT; kt += 2) {
        step(0, kt, xa, wa, xb, wb);
        step(0, kt + 1, xb, wb, xa, wa);
    }
    for (; kt < NT; kt += 2) {
        step(1, kt, xa, wa, xb, wb);
        step(1, kt + 1, xb, wb, xa, wa);
    }

    constexpr int NTHR = NWAVE * 64, CPR = BN / 8, NCH = BM * CPR / NTHR;
    constexpr int RS = (EPI == EPI_STORE) ? BN * 2 + 16 : BN * 4 + 16;
    LAS float* rowstat = (LAS float*)(lds + BM * RS);
    LAS float* colsum = rowstat + BM * 2;
    __syncthreads();
    if (LNF) {
        if (g == (fr >> 2)) {
            const int r = wm * (MF * 16) + wn * 16 + fr;
            const int d = fr & 3;
            const float sq = d == 0 ? s2[0] : d == 1 ? s2[1] : d == 2 ? s2[2] : s2[3];
            const float mean = s1[0] * (1.0f / D_MODEL);
            const float rstd = __builtin_amdgcn_rsqf(fmaxf(sq * (1.0f / D_MODEL) - mean * mean, 0.f) + 1e-5f);
            rowstat[2 * r] = mean * rstd;
            rowstat[2 * r + 1] = rstd;
        }
        if (fr == 0) {
#pragma unroll
            for (int j = 0; j < NF; ++j) *(LAS f32x4*)(colsum + wm * BN + (wn * NF + j) * 16 + 4 * g) = cs[j];
        }
        __syncthreads();
    }
    f32x4 csv[NF];
    if (LNF) {
#pragma unroll
        for (int j = 0; j < NF; ++j) {
            const int c = (wn * NF + j) * 16 + 4 * g;
            csv[j] = *(LAS const f32x4*)(colsum + c) + *(LAS const f32x4*)(colsum + BN + c);
        }
    }
#pragma unroll
    for (int i = 0; i < MF; ++i) {
        const int rl = wm * (MF * 16) + (i ^ rotm) * 16 + fr;
        float mr = 0.f, rstd = 1.f;
        if (LNF) { mr = rowstat[2 * rl]; rstd = rowstat[2 * rl + 1]; }
#pragma unroll
        for (int j = 0; j < NF; ++j) {
            const int nl = (wn * NF + j) * 16 + 4 * g;
            f32x4 v = acc[i][j];
            if (LNF) v = v * rstd - csv[j] * mr;
            if (EPI == EPI_STORE) {
                u32x2 w; w[0] = pk_bf16(v[0], v[1]); w[1] = pk_bf16(v[2], v[3]);
                *(LAS u32x2*)(lds + rl * RS + nl * 2) = w;
            } else {
                *(LAS f32x4*)(lds + rl * RS + nl * 4) = v;
            }
        }
    }
    __syncthreads();
#pragma unroll
    for (int r = 0; r < NCH; ++r) {
        const int c = tid + NTHR * r, row = c / CPR, c8 = c % CPR;
        u32x4 w;
        if (EPI == EPI_STORE) {
            const int hrow = tid >> 2, d8 = tid & 3;
            w = *(LAS const u32x4*)(lds + hrow * RS + r * 64 + d8 * 16);
            ST16(C + ((size_t)(n0 / 32 + r) * M + m0 + hrow) * 32 + d8 * 8, w);
            continue;
        } else {
            f32x4 v0 = *(LAS const f32x4*)(lds + row * RS + c8 * 32), v1 = *(LAS const f32x4*)(lds + row * RS + c8 * 32 + 16);
            const u32x4 rv = *(LAS const u32x4*)(lds + NST * ST_BYTES + row * 128 + c8 * 16);
            v0[0] += bf2f(rv[0] & 0xffffu); v0[1] += bf2f(rv[0] >> 16); v0[2] += bf2f(rv[1] & 0xffffu); v0[3] += bf2f(rv[1] >> 16);
            v1[0] += bf2f(rv[2] & 0xffffu); v1[1] += bf2f(rv[2] >> 16); v1[2] += bf2f(rv[3] & 0xffffu); v1[3] += bf2f(rv[3] >> 16);
            w[0] = pk_bf16(v0[0], v0[1]); w[1] = pk_bf16(v0[2], v0[3]); w[2] = pk_bf16(v1[0], v1[1]); w[3] = pk_bf16(v1[2], v1[3]);
        }
        ST16(C + (size_t)(m0 + row) * ldc + n0 + c8 * 8, w);
        if (EPI == EPI_RES_STAT) {
            float a = 0.f, b = 0.f;
#pragma unroll
            for (int e = 0; e < 4; ++e) {
                const float x0 = bf2f(w[e] & 0xffffu), x1 = bf2f(w[e] >> 16);
                a += x0 + x1; b += x0 * x0 + x1 * x1;
            }
#pragma unroll
            for (int o = 1; o < CPR; o <<= 1) { a += __shfl_xor(a, o); b += __shfl_xor(b, o); }
            if (c8 == 0) { float2 o2; o2.x = a; o2.y = b; *(float2*)(stat_out + ((size_t)(m0 + row) * tiles_n + tn) * 2) = o2; }
        }
    }
}

__global__ __launch_bounds__(512, 2) void glu_gemm_kernel(
    const bf16_t* __restrict__ X, const bf16_t* __restrict__ W1, const bf16_t* __restrict__ W3,
    const float* __restrict__ stat_in, bf16_t* __restrict__ C, const bf16_t* __restrict__ pfw) {
    constexpr int K = D_MODEL, LDW = F_DIM, HTB = 16384;
    if (blockIdx.x >= 224) {
        extern __shared__ __attribute__((aligned(16))) char smem_pf[];
        const unsigned lb = (unsigned)(uintptr_t)(LAS char*)smem_pf + __builtin_amdgcn_readfirstlane(threadIdx.x >> 6) * 1024;
        const char* a = (const char*)pfw + (size_t)(blockIdx.x - 224) * 229376;
#pragma unroll
        for (int r = 0; r < 28; ++r) glds16(a + r * 8192, threadIdx.x * 16, lb + (r & 7) * 8192);
        wait_vm<0>();
        return;
    }
    extern __shared__ __attribute__((aligned(16))) char smem[];
    LAS char* lds = (LAS char*)smem;
    const unsigned lds_base = (unsigned)(uintptr_t)lds;
    const int tid = threadIdx.x, lane = tid & 63;
    const int wave = __builtin_amdgcn_readfirstlane(tid >> 6);
    const int wr = wave >> 2, wc = wave & 3;
    int tm, tn;
    { const int xcd = blockIdx.x & 7, idx = blockIdx.x >> 3; tm = (xcd >> 2) * 4 + (idx & 3); tn = (xcd & 3) * 7 + (idx >> 2); }
    const int m0 = tm * 256, n0 = tn * 128;

    unsigned xoffs[2], woffs[2];
#pragma unroll
    for (int j = 0; j < 2; ++j) {
        const int pc = wave + 8 * j;
        const int row = pc * 8 + (lane >> 3), ch = (lane & 7) ^ (row & 7);
        xoffs[j] = (unsigned)(row * K * 2 + ch * 16);
        const int kb = pc >> 1, nb = 4 * (pc & 1) + (lane >> 4);
        const int r = ((lane & 15) >> 1) ^ (4 * (kb & 1)), half = lane & 1;
        woffs[j] = (unsigned)(((8 * kb + r) * LDW + 16 * nb + 8 * half) * 2);
    }
    const char* xsrc = (const char*)(X + (size_t)m0 * K);
    const char* w1src = (const char*)(W1 + n0);
    const char* w3src = (const char*)(W3 + n0);
#define SA_OFF(b, h) (((b) * 2 + (h)) * HTB)
#define SB_OFF(b, h) ((4 + (b) * 2 + (h)) * HTB)
#define STAGE_A(b, h, kt) do { const char* _s = xsrc + (size_t)(h) * 128 * K * 2 + (kt) * 128; \
        glds16(_s, xoffs[0], lds_base + SA_OFF(b, h) + wave * 1024); glds16(_s, xoffs[1], lds_base + SA_OFF(b, h) + (wave + 8) * 1024); } while (0)
#define STAGE_B(b, h, kt) do { const char* _s = ((h) ? w3src : w1src) + (size_t)(kt) * 64 * LDW * 2; \
        glds16(_s, woffs[0], lds_base + SB_OFF(b, h) + wave * 1024); glds16(_s, woffs[1], lds_base + SB_OFF(b, h) + (wave + 8) * 1024); } while (0)
    const int fr = lane & 15, g = lane >> 4, q = (lane & 15) >> 2, p = lane & 3;
    const int xrow = wr * 64 + fr;
    const int xo0 = (xrow * 128 + ((g ^ (xrow & 7)) << 4)) ^ (wr * 64);
    const int wo0 = g * 2048 + (q + 4 * (g & 1)) * 32 + p * 8 + wc * 512;
    unsigned xb_[4][2], wb_[2][2];
#pragma unroll
    for (int m = 0; m < 4; ++m)
#pragma unroll
        for (int k = 0; k < 2; ++k) { xb_[m][k] = lds_base + ((xo0 ^ (k * 64)) + m * 2048); asm volatile("" : "+v"(xb_[m][k])); }
#pragma unroll
    for (int t = 0; t < 2; ++t)
#pragma unroll
        for (int k = 0; k < 2; ++k) { wb_[t][k] = lds_base + 4 * HTB + ((wo0 ^ (t * 128)) + (k ^ wr) * 8192); asm volatile("" : "+v"(wb_[t][k])); }
#define LDSP(a) ((LAS const char*)(uintptr_t)(a))
#define LDA(dst, b, h) do { _Pragma("unroll") for (int m = 0; m < 4; ++m) _Pragma("unroll") for (int k = 0; k < 2; ++k) \
        dst[m][k] = *(LAS const bf16x8*)(LDSP(xb_[m][k]) + SA_OFF(b, h)); } while (0)
#define LDB(dst, b, h) do { _Pragma("unroll") for (int n = 0; n < 2; ++n) _Pragma("unroll") for (int k = 0; k < 2; ++k) { \
        s16x4 lo_ = tr_read(LDSP(wb_[0][k]) + SA_OFF(b, h) + n * 256); \
        s16x4 hi_ = tr_read(LDSP(wb_[1][k]) + SA_OFF(b, h) + n * 256); \
        dst[n][k] = (bf16x8){lo_[0], lo_[1], lo_[2], lo_[3], hi_[0], hi_[1], hi_[2], hi_[3]}; } } while (0)
#define MMA(ai, bj, At, Bt, CS) do { __builtin_amdgcn_s_setprio(1); \
        _Pragma("unroll") for (int m = 0; m < 4; ++m) _Pragma("unroll") for (int n = 0; n < 2; ++n) _Pragma("unroll") for (int k = 0; k < 2; ++k) \
            acc[ai][bj][m][n] = __builtin_amdgcn_mfma_f32_16x16x32_bf16(Bt[n][k], At[m][k], acc[ai][bj][m][n], 0, 0, 0); \
        if (((CS) & 2) && !NOCS) { cs[bj][0] = __builtin_amdgcn_mfma_f32_16x16x32_bf16(Bt[0][0], ones, cs[bj][0], 0, 0, 0); \
                        cs[bj][1] = __builtin_amdgcn_mfma_f32_16x16x32_bf16(Bt[1][0], ones, cs[bj][1], 0, 0, 0); } \
        __builtin_amdgcn_s_setprio(0); } while (0)
#define WAIT_V(n) asm volatile("s_waitcnt vmcnt(" #n ")" ::: "memory")
#define WAIT_L(n) asm volatile("s_waitcnt lgkmcnt(" #n ")" ::: "memory")
#define BAR __builtin_amdgcn_s_barrier()
#define SCHED __builtin_amdgcn_sched_barrier(0)
    f32x4 acc[2][2][4][2] = {};
    bf16x8 At[4][2], B0[2][2], B1[2][2];
    f32x4 cs[2][2] = {};
    const bf16x8 ones = {0x3f80, 0x3f80, 0x3f80, 0x3f80, 0x3f80, 0x3f80, 0x3f80, 0x3f80};
    constexpr int nt = K / 64;
    {
        const char* ssrc = (const char*)(stat_in + (size_t)m0 * 32);
#pragma unroll
        for (int j = 0; j < 4; ++j) glds16(ssrc, (unsigned)((wave * 4 + j) * 1024 + lane * 16), lds_base + 8 * HTB + (wave * 4 + j) * 1024);
    }
    STAGE_B(0, 0, 0); STAGE_A(0, 0, 0); STAGE_B(0, 1, 0); STAGE_A(0, 1, 0);
    if (wr == 1) BAR;
    WAIT_V(4); BAR;
    STAGE_B(1, 0, 1); STAGE_A(1, 0, 1); STAGE_B(1, 1, 1);
    WAIT_V(6); BAR;
    LDB(B0, 0, 0);
#define GLU_ITER(more) do { \
        LDA(At, 0, 0); STAGE_A(1, 1, t + 1); \
        BAR; WAIT_L(0); MMA(0, 0, At, B0, 2); BAR; SCHED; \
        LDB(B1, 0, 1); if (more) STAGE_B(0, 0, t + 2); \
        BAR; WAIT_L(0); MMA(0, 1, At, B1, 1); BAR; \
        LDA(At, 0, 1); if (more) { STAGE_A(0, 0, t + 2); WAIT_V(10); } else { WAIT_V(6); } \
        BAR; WAIT_L(0); MMA(1, 0, At, B0, 1); BAR; SCHED; \
        LDB(B0, 1, 0); \
        if (more) { STAGE_B(0, 1, t + 2); WAIT_V(6); } else { WAIT_V(0); } \
        BAR; MMA(1, 1, At, B1, 2); BAR; SCHED; \
        LDA(At, 1, 0); if (more) STAGE_A(0, 1, t + 2); \
        BAR; WAIT_L(0); MMA(0, 0, At, B0, 2); BAR; SCHED; \
        LDB(B1, 1, 1); if (more) STAGE_B(1, 0, t + 3); \
        BAR; WAIT_L(0); MMA(0, 1, At, B1, 1); BAR; \
        LDA(At, 1, 1); if (more) { STAGE_A(1, 0, t + 3); WAIT_V(10); } \
        BAR; WAIT_L(0); MMA(1, 0, At, B0, 1); BAR; SCHED; \
        LDB(B0, 0, 0); \
        if (more) { STAGE_B(1, 1, t + 3); WAIT_V(6); } \
        BAR; MMA(1, 1, At, B1, 2); BAR; SCHED; \
    } while (0)
    int t = 0;
    for (; t < nt - 2; t += 2) GLU_ITER(true);
    GLU_ITER(false);
#undef GLU_ITER
    if (wr == 0) BAR;
    constexpr int RS = 272;
    LAS float* rowstat = (LAS float*)(lds + 256 * RS);
    LAS float* colsum = rowstat + 512;
    if (fr == 0) {
#pragma unroll
        for (int b = 0; b < 2; ++b)
#pragma unroll
            for (int n = 0; n < 2; ++n) *(LAS f32x4*)(colsum + wr * 256 + b * 128 + wc * 32 + n * 16 + 4 * g) = cs[b][n];
    }
    if (tid < 256) {
        LAS const f32x4* sp = (LAS const f32x4*)(lds + 8 * HTB + tid * 128);
        f32x4 t = sp[tid & 7];
#pragma unroll
        for (int e = 1; e < 8; ++e) t += sp[e ^ (tid & 7)];
        const float mean = (t[0] + t[2]) * (1.0f / D_MODEL);
        const float rstd = __builtin_amdgcn_rsqf(fmaxf((t[1] + t[3]) * (1.0f / D_MODEL) - mean * mean, 0.f) + 1e-5f);
        rowstat[2 * tid] = mean * rstd; rowstat[2 * tid + 1] = rstd;
    }
    __syncthreads();
    f32x4 csv[2][2];
#pragma unroll
    for (int b = 0; b < 2; ++b)
#pragma unroll
        for (int n = 0; n < 2; ++n) {
            const int c = b * 128 + wc * 32 + n * 16 + 4 * g;
            csv[b][n] = *(LAS const f32x4*)(colsum + c) + *(LAS const f32x4*)(colsum + 256 + c);
        }
#pragma unroll
    for (int ai = 0; ai < 2; ++ai) {
#pragma unroll
        for (int m = 0; m < 4; ++m) {
            const int rl = ai * 128 + wr * 64 + m * 16 + fr;
            const float mr = rowstat[2 * rl], rstd = rowstat[2 * rl + 1];
#pragma unroll
            for (int n = 0; n < 2; ++n) {
                u32x2 w;
#pragma unroll
                for (int hh = 0; hh < 2; ++hh) {
                    const f32x2 ag = {acc[ai][0][m][n][2 * hh], acc[ai][0][m][n][2 * hh + 1]};
                    const f32x2 au = {acc[ai][1][m][n][2 * hh], acc[ai][1][m][n][2 * hh + 1]};
                    const f32x2 cg = {csv[0][n][2 * hh], csv[0][n][2 * hh + 1]};
                    const f32x2 cu = {csv[1][n][2 * hh], csv[1][n][2 * hh + 1]};
                    const f32x2 gv = ag * rstd - cg * mr;
                    const f32x2 uv = au * rstd - cu * mr;
                    const f32x2 t = gv * (-1.4426950408889634f);
                    f32x2 e; e.x = __builtin_amdgcn_exp2f(t.x); e.y = __builtin_amdgcn_exp2f(t.y);
                    const f32x2 d = e + 1.0f;
                    f32x2 rr; rr.x = __builtin_amdgcn_rcpf(d.x); rr.y = __builtin_amdgcn_rcpf(d.y);
                    const f32x2 o = (gv * uv) * rr;
                    w[hh] = pk_bf16(o.x, o.y);
                }
                *(LAS u32x2*)(lds + rl * RS + (wc * 32 + n * 16 + 4 * g) * 2) = w;
            }
        }
        asm volatile("s_waitcnt lgkmcnt(0)" ::: "memory");
        __builtin_amdgcn_s_barrier();
        asm volatile("" ::: "memory");
#pragma unroll
        for (int r = 0; r < 4; ++r) {
            const int c = tid + 512 * r, row = ai * 128 + (c >> 4), c8 = c & 15;
            ST16(C + (size_t)(m0 + row) * LDW + n0 + c8 * 8, *(LAS const u32x4*)(lds + row * RS + c8 * 16));
        }
        __builtin_amdgcn_sched_barrier(0);
    }
#undef SA_OFF
#undef SB_OFF
#undef STAGE_A
#undef STAGE_B
#undef LDA
#undef LDB
#undef MMA
}

template <int NST>
__global__ __launch_bounds__(512, 2) void glu2_kernel(
    const bf16_t* __restrict__ X, const bf16_t* __restrict__ W1, const bf16_t* __restrict__ W3,
    const float* __restrict__ stat_in, bf16_t* __restrict__ C) {
    constexpr int K = D_MODEL, LDW = F_DIM, STB = 32768, NT = K / 32;
    extern __shared__ __attribute__((aligned(16))) char smem[];
    LAS char* lds = (LAS char*)smem;
    const unsigned lds_base = (unsigned)(uintptr_t)lds;
    const int tid = threadIdx.x, lane = tid & 63;
    const int wave = __builtin_amdgcn_readfirstlane(tid >> 6);
    const int wm = wave >> 2, wn = wave & 3;
    int tm, tn;
    { const int xcd = blockIdx.x & 7, idx = blockIdx.x >> 3; tm = (xcd >> 2) * 4 + (idx & 3); tn = (xcd & 3) * 7 + (idx >> 2); }
    const int m0 = tm * 256, n0 = tn * 128;

    unsigned xoffs[2], woffs[2];
#pragma unroll
    for (int j = 0; j < 2; ++j) {
        const int pc = wave + 8 * j;
        const int row = pc * 16 + (lane >> 2), ch = (lane & 3) ^ ((4 - ((row >> 2) & 3)) & 3);
        xoffs[j] = (unsigned)(row * K * 2 + ch * 16);
        const int kb = pc >> 2, nb = 4 * (pc & 1) + (lane >> 4);
        const int r = ((lane & 15) >> 1) ^ (4 * (kb & 1)), half = lane & 1;
        woffs[j] = (unsigned)(((8 * kb + r) * LDW + 16 * nb + 8 * half) * 2);
    }
    const char* xsrc = (const char*)(X + (size_t)m0 * K);
    const char* wsrc = (const char*)(((wave & 2) ? W3 : W1) + n0);
    auto issue = [&](int buf) {
        const unsigned b = lds_base + buf * STB;
        glds16(xsrc, xoffs[0], b + wave * 1024);
        glds16(xsrc, xoffs[1], b + (wave + 8) * 1024);
        glds16(wsrc, woffs[0], b + 16384 + wave * 1024);
        glds16(wsrc, woffs[1], b + 16384 + (wave + 8) * 1024);
        xsrc += 64; wsrc += (size_t)32 * LDW * 2;
    };
    const int fr = lane & 15, g = lane >> 4, q = (lane & 15) >> 2, p = lane & 3;
    const int xo0 = (wm * 128 + fr) * 64 + ((g ^ ((4 - (fr >> 2)) & 3)) << 4);
    const int wo0 = 16384 + g * 4096 + (q + 4 * (g & 1)) * 32 + p * 8 + wn * 512;

    f32x4 acc[8][4];
#pragma unroll
    for (int i = 0; i < 8; ++i)
#pragma unroll
        for (int j = 0; j < 4; ++j) acc[i][j] = (f32x4){0.f, 0.f, 0.f, 0.f};
    f32x4 cs[4] = {};
    const bf16x8 ones = {0x3f80, 0x3f80, 0x3f80, 0x3f80, 0x3f80, 0x3f80, 0x3f80, 0x3f80};

    {
        const char* ssrc = (const char*)(stat_in + (size_t)m0 * 32);
#pragma unroll
        for (int j = 0; j < 4; ++j) glds16(ssrc, (unsigned)((wave * 4 + j) * 1024 + lane * 16), lds_base + NST * STB + (wave * 4 + j) * 1024);
    }
#pragma unroll
    for (int s = 0; s < NST; ++s) issue(s);

    auto read_frags = [&](LAS const char* base, bf16x8 (&xf)[8], bf16x8 (&wf)[4]) {
#pragma unroll
        for (int j = 0; j < 4; ++j) {
            const int o = (j & 1) * 256 + (j >> 1) * 2048;
            s16x4 lo = tr_read(base + wo0 + o);
            s16x4 hi = tr_read(base + (wo0 ^ 128) + o);
            wf[j] = (bf16x8){lo[0], lo[1], lo[2], lo[3], hi[0], hi[1], hi[2], hi[3]};
        }
#pragma unroll
        for (int i = 0; i < 8; ++i) xf[i] = *(LAS const bf16x8*)(base + xo0 + i * 1024);
    };
    auto do_mfma = [&](bf16x8 (&xf)[8], bf16x8 (&wf)[4]) {
#pragma unroll
        for (int i = 0; i < 8; ++i)
#pragma unroll
            for (int j = 0; j < 4; ++j)
                acc[i][j] = __builtin_amdgcn_mfma_f32_16x16x32_bf16(wf[j], xf[i], acc[i][j], 0, 0, 0);
    };
    auto interleave = [&]() {
#pragma unroll
        for (int i = 0; i < 16; ++i) {
            __builtin_amdgcn_sched_group_barrier(0x8, 2, 0);
            __builtin_amdgcn_sched_group_barrier(0x100, 1, 0);
        }
    };
    auto colsum = [&](bf16x8 (&wf)[4]) {
#pragma unroll
        for (int j = 0; j < 4; ++j) cs[j] = __builtin_amdgcn_mfma_f32_16x16x32_bf16(wf[j], ones, cs[j], 0, 0, 0);
    };
    bf16x8 xa[8], wa[4], xb[8], wb[4];
    wait_vm<(NST - 1) * 4>();
    __builtin_amdgcn_s_barrier();
    asm volatile("" ::: "memory");
    read_frags(lds, xa, wa);
    int buf = 0;
    auto step = [&](int kt, bf16x8 (&xc)[8], bf16x8 (&wc_)[4], bf16x8 (&xn)[8], bf16x8 (&wn_)[4], bool docs) {
        const int rem = NT - 2 - kt;
        if (rem >= NST - 2) wait_vm<(NST - 2) * 4>();
        else if (rem == 1) wait_vm<4>();
        else if (NST > 4 && rem == 2) wait_vm<8>();
        else wait_vm<0>();
        asm volatile("s_waitcnt lgkmcnt(0)" ::: "memory");
        __builtin_amdgcn_s_barrier();
        asm volatile("" ::: "memory");
        if (kt + NST < NT) issue(buf);
        buf = (buf + 1 == NST) ? 0 : buf + 1;
        __builtin_amdgcn_sched_barrier(0);
        read_frags(lds + buf * STB, xn, wn_);
        do_mfma(xc, wc_);
        interleave();
        __builtin_amdgcn_sched_barrier(0);
        if (docs) colsum(wc_);
        __builtin_amdgcn_sched_barrier(0);
    };
    for (int kt = 0; kt < NT; kt += 2) {
        step(kt, xa, wa, xb, wb, wm == 0);
        step(kt + 1, xb, wb, xa, wa, wm == 1);
    }
    __syncthreads();
    LAS float* rowstat = (LAS float*)lds;
    LAS float* colsm = (LAS float*)(lds + 2048);
    if (fr == 0) {
#pragma unroll
        for (int j = 0; j < 4; ++j) *(LAS f32x4*)(colsm + wm * 256 + (j >> 1) * 128 + wn * 32 + (j & 1) * 16 + 4 * g) = cs[j];
    }
    if (tid < 256) {
        LAS const f32x4* sp = (LAS const f32x4*)(lds + NST * STB + tid * 128);
        f32x4 t = sp[tid & 7];
#pragma unroll
        for (int e = 1; e < 8; ++e) t += sp[e ^ (tid & 7)];
        const float mean = (t[0] + t[2]) * (1.0f / D_MODEL);
        const float rstd = 1.0f / sqrtf(fmaxf((t[1] + t[3]) * (1.0f / D_MODEL) - mean * mean, 0.f) + 1e-5f);
        rowstat[2 * tid] = mean; rowstat[2 * tid + 1] = rstd;
    }
    __syncthreads();
    f32x4 csv[4];
#pragma unroll
    for (int j = 0; j < 4; ++j) {
        const int c = (j >> 1) * 128 + wn * 32 + (j & 1) * 16 + 4 * g;
        csv[j] = *(LAS const f32x4*)(colsm + c) + *(LAS const f32x4*)(colsm + 256 + c);
    }
#pragma unroll
    for (int i = 0; i < 8; ++i) {
        const int rl = wm * 128 + i * 16 + fr;
        const float mean = rowstat[2 * rl], rstd = rowstat[2 * rl + 1];
#pragma unroll
        for (int n = 0; n < 2; ++n) {
            const f32x4 gv = (acc[i][n] - csv[n] * mean) * rstd;
            const f32x4 uv = (acc[i][n + 2] - csv[n + 2] * mean) * rstd;
            float o[4];
#pragma unroll
            for (int r = 0; r < 4; ++r) o[r] = gv[r] * __builtin_amdgcn_rcpf(1.0f + __expf(-gv[r])) * uv[r];
            u32x2 w; w[0] = pk_bf16(o[0], o[1]); w[1] = pk_bf16(o[2], o[3]);
            *(u32x2*)(C + (size_t)(m0 + rl) * LDW + n0 + wn * 32 + n * 16 + 4 * g) = w;
        }
    }
}

__global__ __launch_bounds__(512) void attn_kernel(const bf16_t* __restrict__ qkv, bf16_t* __restrict__ O,
                                                   const bf16_t* __restrict__ pf0, const bf16_t* __restrict__ pf1,
                                                   const bf16_t* __restrict__ pf2) {
    extern __shared__ __attribute__((aligned(16))) char smem[];
    LAS char* lds = (LAS char*)smem;
    LAS char* ldsK = lds;
    LAS char* ldsV = lds + 32768;
    const unsigned lds_base = (unsigned)(uintptr_t)lds;
    const int tid = threadIdx.x, lane = tid & 63;
    const int w = __builtin_amdgcn_readfirstlane(tid >> 6);
    const int h = blockIdx.x & 31, qb = blockIdx.x >> 5;
    const int q0 = qb * 256, kbase = q0 - 256;
    const bf16_t* Qh = qkv + (size_t)h * (S_LEN * 32);
    const bf16_t* Kh = qkv + (size_t)(32 + h) * (S_LEN * 32);
    const bf16_t* Vh = qkv + (size_t)(64 + h) * (S_LEN * 32);

    {
        const unsigned koff = (unsigned)((lane >> 2) * 64 + (((lane & 3) ^ ((lane >> 4) & 3)) << 4));
        const unsigned voff = (unsigned)(lane * 16);
#pragma unroll
        for (int i = 0; i < 4; ++i) {
            const int pc = w + 8 * i;
            if (kbase + pc * 16 >= 0) {
                glds16((const char*)(Kh + (long)(kbase + pc * 16) * 32), koff, lds_base + pc * 1024);
                glds16((const char*)(Vh + (long)(kbase + pc * 16) * 32), voff, lds_base + 32768 + pc * 1024);
            }
        }
    }
    const int r32 = lane & 31, hi = lane >> 5;
    const int qw = q0 + 32 * w;
    bf16x8 qf[2];
#pragma unroll
    for (int s = 0; s < 2; ++s) qf[s] = *(const bf16x8*)(Qh + (size_t)(qw + r32) * 32 + 16 * s + 8 * hi);
    wait_vm<0>();
    __syncthreads();
    glds16((const char*)pf0 + (size_t)(blockIdx.x * 8 + w) * 1024, (unsigned)(lane * 16), lds_base + 65536 + w * 1024);

    auto body = [&](auto full_tag) {
    constexpr bool FULL = decltype(full_tag)::value;
    f32x16 sc[9];
    const float NEG = -INFINITY;
#pragma unroll
    for (int j = 0; j < 9; ++j) {
        const int kb = qw - 256 + 32 * j;
        if (FULL || kb >= 0) {
            const int key = 32 * (w + j) + r32;
            f32x16 a = {};
#pragma unroll
            for (int s = 0; s < 2; ++s) {
                const bf16x8 kf = *(LAS const bf16x8*)(ldsK + key * 64 + (((2 * s + hi) ^ ((key >> 2) & 3)) << 4));
                a = __builtin_amdgcn_mfma_f32_32x32x16_bf16(kf, qf[s], a, 0, 0, 0);
            }
            sc[j] = a;
        } else {
#pragma unroll
            for (int r = 0; r < 16; ++r) sc[j][r] = NEG;
        }
    }
#pragma unroll
    for (int r = 0; r < 16; ++r) {
        const int i = (r & 3) + 8 * (r >> 2) + 4 * hi;
        if (!(i > r32)) sc[0][r] = NEG;
        if (!(i <= r32)) sc[8][r] = NEG;
    }
    float mxa = NEG, mxb = NEG;
#pragma unroll
    for (int j = 0; j < 9; ++j)
#pragma unroll
        for (int r = 0; r < 16; r += 4) {
            mxa = __builtin_fmaxf(__builtin_fmaxf(mxa, sc[j][r]), sc[j][r + 1]);
            mxb = __builtin_fmaxf(__builtin_fmaxf(mxb, sc[j][r + 2]), sc[j][r + 3]);
        }
    float mx = fmaxf(mxa, mxb);
    mx = fmaxf(mx, __shfl_xor(mx, 32));
    const float c = 0.17677669529663687f * 1.4426950408889634f;
    const float nm = -mx * c;

    f32x16 o = {}, ls = {};
    const bf16x8 ones = {0x3f80, 0x3f80, 0x3f80, 0x3f80, 0x3f80, 0x3f80, 0x3f80, 0x3f80};
    const int gq = (lane & 15) >> 2, gp = lane & 3, gd = (lane >> 4) & 1;
#pragma unroll
    for (int j = 0; j < 9; ++j) {
        const int kb = qw - 256 + 32 * j;
        if (FULL || kb >= 0) {
#pragma unroll
            for (int s = 0; s < 2; ++s) {
                u32x4 pw;
#pragma unroll
                for (int e = 0; e < 4; ++e)
                    pw[e] = pk_bf16(__builtin_amdgcn_exp2f(__builtin_fmaf(sc[j][8 * s + 2 * e], c, nm)),
                                    __builtin_amdgcn_exp2f(__builtin_fmaf(sc[j][8 * s + 2 * e + 1], c, nm)));
                const int key = 32 * (w + j) + 16 * s + 4 * hi + gq;
                LAS const char* vp = ldsV + key * 64 + (16 * gd + 4 * gp) * 2;
                s16x4 lo = tr_read(vp), hi8 = tr_read(vp + 8 * 64);
                const bf16x8 vf = (bf16x8){lo[0], lo[1], lo[2], lo[3], hi8[0], hi8[1], hi8[2], hi8[3]};
                const bf16x8 pf = __builtin_bit_cast(bf16x8, pw);
                o = __builtin_amdgcn_mfma_f32_32x32x16_bf16(vf, pf, o, 0, 0, 0);
                ls = __builtin_amdgcn_mfma_f32_32x32x16_bf16(ones, pf, ls, 0, 0, 0);
            }
        }
    }
    const float rl = 1.0f / ls[0];
    bf16_t* dst = O + (size_t)(qw + r32) * D_MODEL + h * 32 + 4 * hi;
#pragma unroll
    for (int t = 0; t < 4; ++t) {
        u32x2 wv; wv[0] = pk_bf16(o[4 * t] * rl, o[4 * t + 1] * rl); wv[1] = pk_bf16(o[4 * t + 2] * rl, o[4 * t + 3] * rl);
        ST8(dst + 8 * t, wv);
    }
    };
    if (qb > 0) body(std::integral_constant<bool, true>{}); else body(std::integral_constant<bool, false>{});
    wait_vm<0>();
}

template <typename KF> static void set_lds(KF kf, int bytes) {
    (void)hipFuncSetAttribute((const void*)kf, hipFuncAttributeMaxDynamicSharedMemorySize, bytes);
}

extern "C" void kernel_launch(void* const* d_in, const int* in_sizes, int n_in,
                              void* d_out, int out_size, void* d_ws, size_t ws_size,
                              hipStream_t stream) {
    const bf16_t* x = (const bf16_t*)d_in[0];
    const bf16_t* wqkv = (const bf16_t*)d_in[1];
    const bf16_t* wo = (const bf16_t*)d_in[2];
    const bf16_t* w1 = (const bf16_t*)d_in[4];
    const bf16_t* w3 = (const bf16_t*)d_in[5];
    const bf16_t* w2 = (const bf16_t*)d_in[6];
    bf16_t* out = (bf16_t*)d_out;
    char* ws = (char*)d_ws;
    bf16_t* qkv = (bf16_t*)(ws + (4u << 20));
    bf16_t* att = (bf16_t*)(ws + (16u << 20));
    bf16_t* hbuf = (bf16_t*)(ws + (20u << 20));
    bf16_t* act = (bf16_t*)(ws + (28u << 20));
    float* hstat = (float*)(ws + (24u << 20));

    {
        auto kf = gemm_kernel<128, 192, 2, 4, 4, EPI_STORE, 1, G1RM, G1RN, KR1>;
        constexpr int lds = 4 * (128 + 192) * 128;
        set_lds(kf, lds);
        kf<<<256, 512, lds, stream>>>(x, wqkv, nullptr, qkv, nullptr, S_LEN, 3072, 1024, 3072, 3072);
    }
    {
        set_lds(attn_kernel, 73728);
        attn_kernel<<<256, 512, 73728, stream>>>(qkv, att, wo, w1, w3);
    }
    {
        auto kf = gemm_kernel<128, 64, 4, 2, 6, EPI_RES_STAT, 0, 4, 2, KR2>;
        constexpr int lds = 6 * (128 + 64) * 128 + 16384;
        set_lds(kf, lds);
        kf<<<256, 512, lds, stream>>>(att, wo, x, hbuf, hstat, S_LEN, 1024, 1024, 1024, 1024);
    }
    {
        set_lds(glu_gemm_kernel, 163840);
        glu_gemm_kernel<<<256, 512, 163840, stream>>>(hbuf, w1, w3, hstat, act, w2);
    }
    {
        auto kf = gemm_kernel<128, 64, 4, 2, 6, EPI_RES, 0, G4RM, G4RN, KR4>;
        constexpr int lds = 6 * (128 + 64) * 128 + 16384;
        set_lds(kf, lds);
        kf<<<256, 512, lds, stream>>>(act, w2, hbuf, out, nullptr, S_LEN, 1024, F_DIM, 1024, 1024);
    }
}
```

```cpp
#include <hip/hip_runtime.h>
#include <hip/hip_bf16.h>
#include <stdint.h>
#include <type_traits>

typedef unsigned short bf16_t;
typedef short bf16x8 __attribute__((ext_vector_type(8)));
typedef short s16x4 __attribute__((ext_vector_type(4)));
typedef float f32x4 __attribute__((ext_vector_type(4)));
typedef float f32x16 __attribute__((ext_vector_type(16)));
typedef float f32x2 __attribute__((ext_vector_type(2)));
typedef unsigned u32x4 __attribute__((ext_vector_type(4)));
typedef unsigned u32x2 __attribute__((ext_vector_type(2)));
#define LAS __attribute__((address_space(3)))

constexpr int S_LEN = 2048, D_MODEL = 1024, F_DIM = 3584, N_HEAD = 32, D_HEAD = 32, WIN = 256;

__device__ __forceinline__ float bf2f(unsigned v16) { return __uint_as_float(v16 << 16); }
typedef __bf16 bf16x2_t __attribute__((ext_vector_type(2)));
__device__ __forceinline__ unsigned pk_bf16(float lo, float hi) {
    const f32x2 v = {lo, hi};
    return __builtin_bit_cast(unsigned, __builtin_convertvector(v, bf16x2_t));
}
__device__ __forceinline__ s16x4 tr_read(LAS const char* p) {
    return __builtin_amdgcn_ds_read_tr16_b64_v4i16((LAS s16x4*)p);
}
__device__ __forceinline__ void glds16(const void* sbase, unsigned voff, unsigned lds_addr) {
    asm volatile("s_mov_b32 m0, %2\n\ts_nop 0\n\tglobal_load_lds_dwordx4 %1, %0"
                 :: "s"(sbase), "v"(voff), "s"(lds_addr) : "memory");
}
#define ST8(ptr, v) (*(u32x2*)(ptr) = (v))
__device__ __forceinline__ void st16_sc1(void* p, u32x4 v) { asm volatile("global_store_dwordx4 %0, %1, off sc1\n\ts_nop 1" :: "v"(p), "v"(v) : "memory"); }
#define ST16(ptr, v) st16_sc1((void*)(ptr), (v))
template <int N> __device__ __forceinline__ void wait_vm() { asm volatile("s_waitcnt vmcnt(%0)" ::"n"(N) : "memory"); }

enum { EPI_STORE = 0, EPI_RES = 1, EPI_RES_STAT = 2 };

template <int BM, int BN, int WM, int WN, int NST, int EPI, int LNF, int RM, int RN>
__global__ __launch_bounds__(WM * WN * 64) void gemm_kernel(
    const bf16_t* __restrict__ X, const bf16_t* __restrict__ W0,
    const bf16_t* __restrict__ R, bf16_t* __restrict__ C, float* __restrict__ stat_out,
    int M, int N, int K, int ldw, int ldc) {
    constexpr int NWAVE = WM * WN;
    constexpr int MF = BM / 16 / WM, NF = BN / 16 / WN, NB = BN / 16;
    constexpr int A_BYTES = BM * 128, B_BYTES = BN * 128, ST_BYTES = A_BYTES + B_BYTES;
    constexpr int LPW = (BM + BN) / 8 / NWAVE;
    constexpr int LPA = BM / 8 / NWAVE;
    static_assert((BM + BN) % (8 * NWAVE) == 0 && (BM % (8 * NWAVE)) == 0, "piece split");
    static_assert(NB % 4 == 0, "NB%4");
    static_assert(!LNF || (MF == WN && WM == 2), "LN fold layout");
    static_assert(EPI != EPI_STORE || (BM * 4 == NWAVE * 64), "head-major store layout");
    extern __shared__ __attribute__((aligned(16))) char smem[];
    LAS char* lds = (LAS char*)smem;

    const int tid = threadIdx.x, lane = tid & 63;
    const int wave = __builtin_amdgcn_readfirstlane(tid >> 6);
    const int wm = wave / WN, wn = wave % WN;

    const int tiles_m = M / BM, tiles_n = N / BN;
    int tm, tn;
    {
        const int xcd = blockIdx.x & 7, idx = blockIdx.x >> 3;
        const int rm = xcd / RN, rn = xcd % RN;
        const int sm = tiles_m / RM, sn = tiles_n / RN;
        tm = rm * sm + idx % sm; tn = rn * sn + idx / sm;
    }
    const int m0 = tm * BM, n0 = tn * BN;

    unsigned voff[LPW];
    const char* sbase[LPW];
#pragma unroll
    for (int j = 0; j < LPW; ++j) {
        const int ii = wave + NWAVE * j;
        if (j < LPA) {
            const int row = ii * 8 + (lane >> 3), ch = (lane & 7) ^ (row & 7);
            voff[j] = (unsigned)(row * K * 2 + ch * 16);
            sbase[j] = (const char*)(X + (size_t)m0 * K);
        } else {
            const int pi = ii - BM / 8;
            const int kb = pi / (NB / 4), nb = 4 * (pi % (NB / 4)) + (lane >> 4);
            const int r = ((lane & 15) >> 1) ^ (4 * (kb & 1)), half = lane & 1;
            voff[j] = (unsigned)(((8 * kb + r) * ldw + 16 * nb + 8 * half) * 2);
            sbase[j] = (const char*)(W0 + n0);
        }
    }
    const unsigned lds_base = (unsigned)(uintptr_t)lds;
    const int NT = K / 64;
    auto issue_piece = [&](int buf, int j) {
        const int ii = wave + NWAVE * j;
        glds16(sbase[j], voff[j], lds_base + buf * ST_BYTES + ii * 1024);
        sbase[j] += (j < LPA) ? 128 : (long)128 * ldw;
    };
    auto issue = [&](int buf) {
#pragma unroll
        for (int j = 0; j < LPW; ++j) issue_piece(buf, j);
    };

    const int fr = lane & 15, g = lane >> 4, q = (lane & 15) >> 2, p = lane & 3;
    const int rotm = LNF ? wn : 0, rotk = LNF ? wm : 0;
    const int xrow = wm * (MF * 16) + fr;
    const int xoff0 = xrow * 128 + ((g ^ (xrow & 7)) << 4);
    const int woff0 = A_BYTES + g * NB * 256 + (q + 4 * (g & 1)) * 32 + p * 8 + wn * NF * 256;

    f32x4 acc[MF][NF];
#pragma unroll
    for (int i = 0; i < MF; ++i)
#pragma unroll
        for (int j = 0; j < NF; ++j) acc[i][j] = (f32x4){0.f, 0.f, 0.f, 0.f};
    f32x4 s1 = {0.f, 0.f, 0.f, 0.f}, s2 = {0.f, 0.f, 0.f, 0.f}, cs[NF];
#pragma unroll
    for (int j = 0; j < NF; ++j) cs[j] = (f32x4){0.f, 0.f, 0.f, 0.f};
    const bf16x8 ones = {0x3f80, 0x3f80, 0x3f80, 0x3f80, 0x3f80, 0x3f80, 0x3f80, 0x3f80};

    if (EPI != EPI_STORE) {
        static_assert(EPI == EPI_STORE || BN == 64, "residual piece = 8 rows x 128 B");
#pragma unroll
        for (int j = 0; j < BM / 8 / NWAVE; ++j) {
            const int pc = wave + NWAVE * j;
            glds16((const char*)(R + (size_t)(m0 + pc * 8) * ldc + n0), (unsigned)((lane >> 3) * ldc * 2 + (lane & 7) * 16),
                   lds_base + NST * ST_BYTES + pc * 1024);
        }
    }
#pragma unroll
    for (int s = 0; s < NST - 1; ++s) issue(s);

    constexpr int NRD = 2 * (NF + MF);
    constexpr int NMS = MF * NF + (LNF ? 2 : 0);
    constexpr int NM = 2 * NMS + (LNF ? NF : 0);
    auto read_one = [&](LAS const char* base, bf16x8 (&xf)[2][MF], bf16x8 (&wf)[2][NF], int r) {
        const int slot = r / (NF + MF), e = r % (NF + MF), ks = slot ^ rotk;
        if (e < NF) {
            const int o = ks * (4 * NB * 256) + e * 256;
            s16x4 lo = tr_read(base + woff0 + o);
            s16x4 hi = tr_read(base + (woff0 ^ 128) + o);
            wf[slot][e] = (bf16x8){lo[0], lo[1], lo[2], lo[3], hi[0], hi[1], hi[2], hi[3]};
        } else {
            const int i = e - NF;
            xf[slot][i] = *(LAS const bf16x8*)(base + ((xoff0 ^ (ks * 64)) + (i ^ rotm) * 2048));
        }
    };
    auto read_tile = [&](LAS const char* base, bf16x8 (&xf)[2][MF], bf16x8 (&wf)[2][NF]) {
#pragma unroll
        for (int r = 0; r < NRD; ++r) read_one(base, xf, wf, r);
    };
    auto mfma_one = [&](bf16x8 (&xf)[2][MF], bf16x8 (&wf)[2][NF], int f) {
        if (f >= 2 * NMS) { const int j = f - 2 * NMS; cs[j] = __builtin_amdgcn_mfma_f32_16x16x32_bf16(wf[0][j], ones, cs[j], 0, 0, 0); return; }
        const int slot = f / NMS, e = f % NMS;
        if (e < MF * NF) {
            const int i = e / NF, j = e % NF;
            acc[i][j] = __builtin_amdgcn_mfma_f32_16x16x32_bf16(wf[slot][j], xf[slot][i], acc[i][j], 0, 0, 0);
        } else if (e == MF * NF) {
            s1 = __builtin_amdgcn_mfma_f32_16x16x32_bf16(ones, xf[slot][0], s1, 0, 0, 0);
        } else {
            s2 = __builtin_amdgcn_mfma_f32_16x16x32_bf16(xf[slot][0], xf[slot][0], s2, 0, 0, 0);
        }
    };
    bf16x8 xa[2][MF], wa[2][NF], xb[2][MF], wb[2][NF];
    wait_vm<(NST - 2) * LPW>();
    __builtin_amdgcn_s_barrier();
    asm volatile("" ::: "memory");
    read_tile(lds, xa, wa);

    int buf = 0, pbuf = NST - 1;
    auto step = [&](int mode, int kt, bf16x8 (&xc)[2][MF], bf16x8 (&wc)[2][NF], bf16x8 (&xn)[2][MF], bf16x8 (&wn2)[2][NF]) {
        if (mode == 0) {
            wait_vm<(NST - 3) * LPW>();
        } else {
            const int rem = NT - 2 - kt;
            if (rem >= NST - 3) wait_vm<(NST - 3) * LPW>();
            else if (NST > 4 && rem == 1) wait_vm<LPW>();
            else if (NST > 5 && rem == 2) wait_vm<2 * LPW>();
            else wait_vm<0>();
        }
        __builtin_amdgcn_s_barrier();
        asm volatile("" ::: "memory");
        const bool ld = (mode == 0) || (kt + NST - 1 < NT);
        const int ibuf = pbuf;
        pbuf = buf;
        buf = (buf + 1 == NST) ? 0 : buf + 1;
        LAS const char* nbase = lds + buf * ST_BYTES;
        if (ld) issue(ibuf);
        __builtin_amdgcn_sched_barrier(0);
        read_tile(nbase, xn, wn2);
#pragma unroll
        for (int f = 0; f < NM; ++f) mfma_one(xc, wc, f);
        {
            constexpr int NR = 2 * (MF + 2 * NF), RPM = (NR + NM - 1) / NM;
#pragma unroll
            for (int f = 0; f < NM; ++f) {
                __builtin_amdgcn_sched_group_barrier(0x8, 1, 0);
                __builtin_amdgcn_sched_group_barrier(0x100, RPM, 0);
            }
        }
        __builtin_amdgcn_sched_barrier(0);
    };
    int kt = 0;
    for (; kt + NST < NT; kt += 2) {
        step(0, kt, xa, wa, xb, wb);
        step(0, kt + 1, xb, wb, xa, wa);
    }
    for (; kt < NT; kt += 2) {
        step(1, kt, xa, wa, xb, wb);
        step(1, kt + 1, xb, wb, xa, wa);
    }

    constexpr int NTHR = NWAVE * 64, CPR = BN / 8, NCH = BM * CPR / NTHR;
    constexpr int RS = (EPI == EPI_STORE) ? BN * 2 + 16 : BN * 4 + 16;
    LAS float* rowstat = (LAS float*)(lds + BM * RS);
    LAS float* colsum = rowstat + BM * 2;
    __syncthreads();
    if (LNF) {
        if (g == (fr >> 2)) {
            const int r = wm * (MF * 16) + wn * 16 + fr;
            const int d = fr & 3;
            const float sq = d == 0 ? s2[0] : d == 1 ? s2[1] : d == 2 ? s2[2] : s2[3];
            const float mean = s1[0] * (1.0f / D_MODEL);
            const float rstd = __builtin_amdgcn_rsqf(fmaxf(sq * (1.0f / D_MODEL) - mean * mean, 0.f) + 1e-5f);
            rowstat[2 * r] = mean * rstd;
            rowstat[2 * r + 1] = rstd;
        }
        if (fr == 0) {
#pragma unroll
            for (int j = 0; j < NF; ++j) *(LAS f32x4*)(colsum + wm * BN + (wn * NF + j) * 16 + 4 * g) = cs[j];
        }
        __syncthreads();
    }
    f32x4 csv[NF];
    if (LNF) {
#pragma unroll
        for (int j = 0; j < NF; ++j) {
            const int c = (wn * NF + j) * 16 + 4 * g;
            csv[j] = *(LAS const f32x4*)(colsum + c) + *(LAS const f32x4*)(colsum + BN + c);
        }
    }
#pragma unroll
    for (int i = 0; i < MF; ++i) {
        const int rl = wm * (MF * 16) + (i ^ rotm) * 16 + fr;
        float mr = 0.f, rstd = 1.f;
        if (LNF) { mr = rowstat[2 * rl]; rstd = rowstat[2 * rl + 1]; }
#pragma unroll
        for (int j = 0; j < NF; ++j) {
            const int nl = (wn * NF + j) * 16 + 4 * g;
            f32x4 v = acc[i][j];
            if (LNF) v = v * rstd - csv[j] * mr;
            if (EPI == EPI_STORE) {
                u32x2 w; w[0] = pk_bf16(v[0], v[1]); w[1] = pk_bf16(v[2], v[3]);
                *(LAS u32x2*)(lds + rl * RS + nl * 2) = w;
            } else {
                *(LAS f32x4*)(lds + rl * RS + nl * 4) = v;
            }
        }
    }
    __syncthreads();
#pragma unroll
    for (int r = 0; r < NCH; ++r) {
        const int c = tid + NTHR * r, row = c / CPR, c8 = c % CPR;
        u32x4 w;
        if (EPI == EPI_STORE) {
            const int hrow = tid >> 2, d8 = tid & 3;
            w = *(LAS const u32x4*)(lds + hrow * RS + r * 64 + d8 * 16);
            ST16(C + ((size_t)(n0 / 32 + r) * M + m0 + hrow) * 32 + d8 * 8, w);
            continue;
        } else {
            f32x4 v0 = *(LAS const f32x4*)(lds + row * RS + c8 * 32), v1 = *(LAS const f32x4*)(lds + row * RS + c8 * 32 + 16);
            const u32x4 rv = *(LAS const u32x4*)(lds + NST * ST_BYTES + row * 128 + c8 * 16);
            v0[0] += bf2f(rv[0] & 0xffffu); v0[1] += bf2f(rv[0] >> 16); v0[2] += bf2f(rv[1] & 0xffffu); v0[3] += bf2f(rv[1] >> 16);
            v1[0] += bf2f(rv[2] & 0xffffu); v1[1] += bf2f(rv[2] >> 16); v1[2] += bf2f(rv[3] & 0xffffu); v1[3] += bf2f(rv[3] >> 16);
            w[0] = pk_bf16(v0[0], v0[1]); w[1] = pk_bf16(v0[2], v0[3]); w[2] = pk_bf16(v1[0], v1[1]); w[3] = pk_bf16(v1[2], v1[3]);
        }
        ST16(C + (size_t)(m0 + row) * ldc + n0 + c8 * 8, w);
        if (EPI == EPI_RES_STAT) {
            float a = 0.f, b = 0.f;
#pragma unroll
            for (int e = 0; e < 4; ++e) {
                const float x0 = bf2f(w[e] & 0xffffu), x1 = bf2f(w[e] >> 16);
                a += x0 + x1; b += x0 * x0 + x1 * x1;
            }
#pragma unroll
            for (int o = 1; o < CPR; o <<= 1) { a += __shfl_xor(a, o); b += __shfl_xor(b, o); }
            if (c8 == 0) { float2 o2; o2.x = a; o2.y = b; *(float2*)(stat_out + ((size_t)(m0 + row) * tiles_n + tn) * 2) = o2; }
        }
    }
}

__global__ __launch_bounds__(512, 2) void glu_gemm_kernel(
    const bf16_t* __restrict__ X, const bf16_t* __restrict__ W1, const bf16_t* __restrict__ W3,
    const float* __restrict__ stat_in, bf16_t* __restrict__ C, const bf16_t* __restrict__ pfw) {
    constexpr int K = D_MODEL, LDW = F_DIM, HTB = 16384;
    if (blockIdx.x >= 224) {
        extern __shared__ __attribute__((aligned(16))) char smem_pf[];
        const unsigned lb = (unsigned)(uintptr_t)(LAS char*)smem_pf + __builtin_amdgcn_readfirstlane(threadIdx.x >> 6) * 1024;
        const char* a = (const char*)pfw + (size_t)(blockIdx.x - 224) * 229376;
#pragma unroll
        for (int r = 0; r < 28; ++r) glds16(a + r * 8192, threadIdx.x * 16, lb + (r & 7) * 8192);
        wait_vm<0>();
        return;
    }
    extern __shared__ __attribute__((aligned(16))) char smem[];
    LAS char* lds = (LAS char*)smem;
    const unsigned lds_base = (unsigned)(uintptr_t)lds;
    const int tid = threadIdx.x, lane = tid & 63;
    const int wave = __builtin_amdgcn_readfirstlane(tid >> 6);
    const int wr = wave >> 2, wc = wave & 3;
    int tm, tn;
    { const int xcd = blockIdx.x & 7, idx = blockIdx.x >> 3; tm = (xcd >> 2) * 4 + (idx & 3); tn = (xcd & 3) * 7 + (idx >> 2); }
    const int m0 = tm * 256, n0 = tn * 128;

    unsigned xoffs[2], woffs[2];
#pragma unroll
    for (int j = 0; j < 2; ++j) {
        const int pc = wave + 8 * j;
        const int row = pc * 8 + (lane >> 3), ch = (lane & 7) ^ (row & 7);
        xoffs[j] = (unsigned)(row * K * 2 + ch * 16);
        const int kb = pc >> 1, nb = 4 * (pc & 1) + (lane >> 4);
        const int r = ((lane & 15) >> 1) ^ (4 * (kb & 1)), half = lane & 1;
        woffs[j] = (unsigned)(((8 * kb + r) * LDW + 16 * nb + 8 * half) * 2);
    }
    const char* xsrc = (const char*)(X + (size_t)m0 * K);
    const char* w1src = (const char*)(W1 + n0);
    const char* w3src = (const char*)(W3 + n0);
#define SA_OFF(b, h) (((b) * 2 + (h)) * HTB)
#define SB_OFF(b, h) ((4 + (b) * 2 + (h)) * HTB)
#define STAGE_A(b, h, kt) do { const char* _s = xsrc + (size_t)(h) * 128 * K * 2 + (kt) * 128; \
        glds16(_s, xoffs[0], lds_base + SA_OFF(b, h) + wave * 1024); glds16(_s, xoffs[1], lds_base + SA_OFF(b, h) + (wave + 8) * 1024); } while (0)
#define STAGE_B(b, h, kt) do { const char* _s = ((h) ? w3src : w1src) + (size_t)(kt) * 64 * LDW * 2; \
        glds16(_s, woffs[0], lds_base + SB_OFF(b, h) + wave * 1024); glds16(_s, woffs[1], lds_base + SB_OFF(b, h) + (wave + 8) * 1024); } while (0)
    const int fr = lane & 15, g = lane >> 4, q = (lane & 15) >> 2, p = lane & 3;
    const int xrow = wr * 64 + fr;
    const int xo0 = (xrow * 128 + ((g ^ (xrow & 7)) << 4)) ^ (wr * 64);
    const int wo0 = g * 2048 + (q + 4 * (g & 1)) * 32 + p * 8 + wc * 512;
    unsigned xb_[4][2], wb_[2][2];
#pragma unroll
    for (int m = 0; m < 4; ++m)
#pragma unroll
        for (int k = 0; k < 2; ++k) { xb_[m][k] = lds_base + ((xo0 ^ (k * 64)) + m * 2048); asm volatile("" : "+v"(xb_[m][k])); }
#pragma unroll
    for (int t = 0; t < 2; ++t)
#pragma unroll
        for (int k = 0; k < 2; ++k) { wb_[t][k] = lds_base + 4 * HTB + ((wo0 ^ (t * 128)) + (k ^ wr) * 8192); asm volatile("" : "+v"(wb_[t][k])); }
#define LDSP(a) ((LAS const char*)(uintptr_t)(a))
#define LDA(dst, b, h) do { _Pragma("unroll") for (int m = 0; m < 4; ++m) _Pragma("unroll") for (int k = 0; k < 2; ++k) \
        dst[m][k] = *(LAS const bf16x8*)(LDSP(xb_[m][k]) + SA_OFF(b, h)); } while (0)
#define LDB(dst, b, h) do { _Pragma("unroll") for (int n = 0; n < 2; ++n) _Pragma("unroll") for (int k = 0; k < 2; ++k) { \
        s16x4 lo_ = tr_read(LDSP(wb_[0][k]) + SA_OFF(b, h) + n * 256); \
        s16x4 hi_ = tr_read(LDSP(wb_[1][k]) + SA_OFF(b, h) + n * 256); \
        dst[n][k] = (bf16x8){lo_[0], lo_[1], lo_[2], lo_[3], hi_[0], hi_[1], hi_[2], hi_[3]}; } } while (0)
#define MMA(ai, bj, At, Bt, CS) do { __builtin_amdgcn_s_setprio(1); \
        _Pragma("unroll") for (int m = 0; m < 4; ++m) _Pragma("unroll") for (int n = 0; n < 2; ++n) _Pragma("unroll") for (int k = 0; k < 2; ++k) \
            acc[ai][bj][m][n] = __builtin_amdgcn_mfma_f32_16x16x32_bf16(Bt[n][k], At[m][k], acc[ai][bj][m][n], 0, 0, 0); \
        if ((CS) & 2) { cs[bj][0] = __builtin_amdgcn_mfma_f32_16x16x32_bf16(Bt[0][0], ones, cs[bj][0], 0, 0, 0); \
                        cs[bj][1] = __builtin_amdgcn_mfma_f32_16x16x32_bf16(Bt[1][0], ones, cs[bj][1], 0, 0, 0); } \
        __builtin_amdgcn_s_setprio(0); } while (0)
#define WAIT_V(n) asm volatile("s_waitcnt vmcnt(" #n ")" ::: "memory")
#define WAIT_L(n) asm volatile("s_waitcnt lgkmcnt(" #n ")" ::: "memory")
#define BAR __builtin_amdgcn_s_barrier()
#define SCHED __builtin_amdgcn_sched_barrier(0)
    f32x4 acc[2][2][4][2] = {};
    bf16x8 At[4][2], B0[2][2], B1[2][2];
    f32x4 cs[2][2] = {};
    const bf16x8 ones = {0x3f80, 0x3f80, 0x3f80, 0x3f80, 0x3f80, 0x3f80, 0x3f80, 0x3f80};
    constexpr int nt = K / 64;
    {
        const char* ssrc = (const char*)(stat_in + (size_t)m0 * 32);
#pragma unroll
        for (int j = 0; j < 4; ++j) glds16(ssrc, (unsigned)((wave * 4 + j) * 1024 + lane * 16), lds_base + 8 * HTB + (wave * 4 + j) * 1024);
    }
    STAGE_B(0, 0, 0); STAGE_A(0, 0, 0); STAGE_B(0, 1, 0); STAGE_A(0, 1, 0);
    if (wr == 1) BAR;
    WAIT_V(4); BAR;
    STAGE_B(1, 0, 1); STAGE_A(1, 0, 1); STAGE_B(1, 1, 1);
    WAIT_V(6); BAR;
    LDB(B0, 0, 0);
#define GLU_ITER(more) do { \
        LDA(At, 0, 0); STAGE_A(1, 1, t + 1); \
        BAR; WAIT_L(0); MMA(0, 0, At, B0, 2); BAR; SCHED; \
        LDB(B1, 0, 1); if (more) STAGE_B(0, 0, t + 2); \
        BAR; WAIT_L(0); MMA(0, 1, At, B1, 1); BAR; \
        LDA(At, 0, 1); if (more) { STAGE_A(0, 0, t + 2); WAIT_V(10); } else { WAIT_V(6); } \
        BAR; WAIT_L(0); MMA(1, 0, At, B0, 1); BAR; SCHED; \
        LDB(B0, 1, 0); \
        if (more) { STAGE_B(0, 1, t + 2); WAIT_V(6); } else { WAIT_V(0); } \
        BAR; MMA(1, 1, At, B1, 2); BAR; SCHED; \
        LDA(At, 1, 0); if (more) STAGE_A(0, 1, t + 2); \
        BAR; WAIT_L(0); MMA(0, 0, At, B0, 2); BAR; SCHED; \
        LDB(B1, 1, 1); if (more) STAGE_B(1, 0, t + 3); \
        BAR; WAIT_L(0); MMA(0, 1, At, B1, 1); BAR; \
        LDA(At, 1, 1); if (more) { STAGE_A(1, 0, t + 3); WAIT_V(10); } \
        BAR; WAIT_L(0); MMA(1, 0, At, B0, 1); BAR; SCHED; \
        LDB(B0, 0, 0); \
        if (more) { STAGE_B(1, 1, t + 3); WAIT_V(6); } \
        BAR; MMA(1, 1, At, B1, 2); BAR; SCHED; \
    } while (0)
    int t = 0;
    for (; t < nt - 2; t += 2) GLU_ITER(true);
    GLU_ITER(false);
#undef GLU_ITER
    if (wr == 0) BAR;
    constexpr int RS = 272;
    LAS float* rowstat = (LAS float*)(lds + 256 * RS);
    LAS float* colsum = rowstat + 512;
    if (fr == 0) {
#pragma unroll
        for (int b = 0; b < 2; ++b)
#pragma unroll
            for (int n = 0; n < 2; ++n) *(LAS f32x4*)(colsum + wr * 256 + b * 128 + wc * 32 + n * 16 + 4 * g) = cs[b][n];
    }
    if (tid < 256) {
        LAS const f32x4* sp = (LAS const f32x4*)(lds + 8 * HTB + tid * 128);
        f32x4 t = sp[tid & 7];
#pragma unroll
        for (int e = 1; e < 8; ++e) t += sp[e ^ (tid & 7)];
        const float mean = (t[0] + t[2]) * (1.0f / D_MODEL);
        const float rstd = __builtin_amdgcn_rsqf(fmaxf((t[1] + t[3]) * (1.0f / D_MODEL) - mean * mean, 0.f) + 1e-5f);
        rowstat[2 * tid] = mean * rstd; rowstat[2 * tid + 1] = rstd;
    }
    __syncthreads();
    f32x4 csv[2][2];
#pragma unroll
    for (int b = 0; b < 2; ++b)
#pragma unroll
        for (int n = 0; n < 2; ++n) {
            const int c = b * 128 + wc * 32 + n * 16 + 4 * g;
            csv[b][n] = *(LAS const f32x4*)(colsum + c) + *(LAS const f32x4*)(colsum + 256 + c);
        }
#pragma unroll
    for (int ai = 0; ai < 2; ++ai) {
#pragma unroll
        for (int m = 0; m < 4; ++m) {
            const int rl = ai * 128 + wr * 64 + m * 16 + fr;
            const float mr = rowstat[2 * rl], rstd = rowstat[2 * rl + 1];
#pragma unroll
            for (int n = 0; n < 2; ++n) {
                u32x2 w;
#pragma unroll
                for (int hh = 0; hh < 2; ++hh) {
                    const f32x2 ag = {acc[ai][0][m][n][2 * hh], acc[ai][0][m][n][2 * hh + 1]};
                    const f32x2 au = {acc[ai][1][m][n][2 * hh], acc[ai][1][m][n][2 * hh + 1]};
                    const f32x2 cg = {csv[0][n][2 * hh], csv[0][n][2 * hh + 1]};
                    const f32x2 cu = {csv[1][n][2 * hh], csv[1][n][2 * hh + 1]};
                    const f32x2 gv = ag * rstd - cg * mr;
                    const f32x2 uv = au * rstd - cu * mr;
                    const f32x2 t = gv * (-1.4426950408889634f);
                    f32x2 e; e.x = __builtin_amdgcn_exp2f(t.x); e.y = __builtin_amdgcn_exp2f(t.y);
                    const f32x2 d = e + 1.0f;
                    f32x2 rr; rr.x = __builtin_amdgcn_rcpf(d.x); rr.y = __builtin_amdgcn_rcpf(d.y);
                    const f32x2 o = (gv * uv) * rr;
                    w[hh] = pk_bf16(o.x, o.y);
                }
                *(LAS u32x2*)(lds + rl * RS + (wc * 32 + n * 16 + 4 * g) * 2) = w;
            }
        }
        asm volatile("s_waitcnt lgkmcnt(0)" ::: "memory");
        __builtin_amdgcn_s_barrier();
        asm volatile("" ::: "memory");
#pragma unroll
        for (int r = 0; r < 4; ++r) {
            const int c = tid + 512 * r, row = ai * 128 + (c >> 4), c8 = c & 15;
            ST16(C + (size_t)(m0 + row) * LDW + n0 + c8 * 8, *(LAS const u32x4*)(lds + row * RS + c8 * 16));
        }
        __builtin_amdgcn_sched_barrier(0);
    }
#undef SA_OFF
#undef SB_OFF
#undef STAGE_A
#undef STAGE_B
#undef LDA
#undef LDB
#undef MMA
}

__global__ __launch_bounds__(512) void attn_kernel(const bf16_t* __restrict__ qkv, bf16_t* __restrict__ O,
                                                   const bf16_t* __restrict__ pf0) {
    extern __shared__ __attribute__((aligned(16))) char smem[];
    LAS char* lds = (LAS char*)smem;
    LAS char* ldsK = lds;
    LAS char* ldsV = lds + 32768;
    const unsigned lds_base = (unsigned)(uintptr_t)lds;
    const int tid = threadIdx.x, lane = tid & 63;
    const int w = __builtin_amdgcn_readfirstlane(tid >> 6);
    const int h = blockIdx.x & 31, qb = blockIdx.x >> 5;
    const int q0 = qb * 256, kbase = q0 - 256;
    const bf16_t* Qh = qkv + (size_t)h * (S_LEN * 32);
    const bf16_t* Kh = qkv + (size_t)(32 + h) * (S_LEN * 32);
    const bf16_t* Vh = qkv + (size_t)(64 + h) * (S_LEN * 32);

    {
        const unsigned koff = (unsigned)((lane >> 2) * 64 + (((lane & 3) ^ ((lane >> 4) & 3)) << 4));
        const unsigned voff = (unsigned)(lane * 16);
#pragma unroll
        for (int i = 0; i < 4; ++i) {
            const int pc = w + 8 * i;
            if (kbase + pc * 16 >= 0) {
                glds16((const char*)(Kh + (long)(kbase + pc * 16) * 32), koff, lds_base + pc * 1024);
                glds16((const char*)(Vh + (long)(kbase + pc * 16) * 32), voff, lds_base + 32768 + pc * 1024);
            }
        }
    }
    const int r32 = lane & 31, hi = lane >> 5;
    const int qw = q0 + 32 * w;
    bf16x8 qf[2];
#pragma unroll
    for (int s = 0; s < 2; ++s) qf[s] = *(const bf16x8*)(Qh + (size_t)(qw + r32) * 32 + 16 * s + 8 * hi);
    wait_vm<0>();
    __syncthreads();
    glds16((const char*)pf0 + (size_t)(blockIdx.x * 8 + w) * 1024, (unsigned)(lane * 16), lds_base + 65536 + w * 1024);

    auto body = [&](auto full_tag) {
    constexpr bool FULL = decltype(full_tag)::value;
    f32x16 sc[9];
    const float NEG = -INFINITY;
#pragma unroll
    for (int j = 0; j < 9; ++j) {
        const int kb = qw - 256 + 32 * j;
        if (FULL || kb >= 0) {
            const int key = 32 * (w + j) + r32;
            f32x16 a = {};
#pragma unroll
            for (int s = 0; s < 2; ++s) {
                const bf16x8 kf = *(LAS const bf16x8*)(ldsK + key * 64 + (((2 * s + hi) ^ ((key >> 2) & 3)) << 4));
                a = __builtin_amdgcn_mfma_f32_32x32x16_bf16(kf, qf[s], a, 0, 0, 0);
            }
            sc[j] = a;
        } else {
#pragma unroll
            for (int r = 0; r < 16; ++r) sc[j][r] = NEG;
        }
    }
#pragma unroll
    for (int r = 0; r < 16; ++r) {
        const int i = (r & 3) + 8 * (r >> 2) + 4 * hi;
        if (!(i > r32)) sc[0][r] = NEG;
        if (!(i <= r32)) sc[8][r] = NEG;
    }
    float mxa = NEG, mxb = NEG;
#pragma unroll
    for (int j = 0; j < 9; ++j)
#pragma unroll
        for (int r = 0; r < 16; r += 4) {
            mxa = __builtin_fmaxf(__builtin_fmaxf(mxa, sc[j][r]), sc[j][r + 1]);
            mxb = __builtin_fmaxf(__builtin_fmaxf(mxb, sc[j][r + 2]), sc[j][r + 3]);
        }
    float mx = fmaxf(mxa, mxb);
    mx = fmaxf(mx, __shfl_xor(mx, 32));
    const float c = 0.17677669529663687f * 1.4426950408889634f;
    const float nm = -mx * c;

    f32x16 o = {}, ls = {};
    const bf16x8 ones = {0x3f80, 0x3f80, 0x3f80, 0x3f80, 0x3f80, 0x3f80, 0x3f80, 0x3f80};
    const int gq = (lane & 15) >> 2, gp = lane & 3, gd = (lane >> 4) & 1;
#pragma unroll
    for (int j = 0; j < 9; ++j) {
        const int kb = qw - 256 + 32 * j;
        if (FULL || kb >= 0) {
#pragma unroll
            for (int s = 0; s < 2; ++s) {
                u32x4 pw;
#pragma unroll
                for (int e = 0; e < 4; ++e)
                    pw[e] = pk_bf16(__builtin_amdgcn_exp2f(__builtin_fmaf(sc[j][8 * s + 2 * e], c, nm)),
                                    __builtin_amdgcn_exp2f(__builtin_fmaf(sc[j][8 * s + 2 * e + 1], c, nm)));
                const int key = 32 * (w + j) + 16 * s + 4 * hi + gq;
                LAS const char* vp = ldsV + key * 64 + (16 * gd + 4 * gp) * 2;
                s16x4 lo = tr_read(vp), hi8 = tr_read(vp + 8 * 64);
                const bf16x8 vf = (bf16x8){lo[0], lo[1], lo[2], lo[3], hi8[0], hi8[1], hi8[2], hi8[3]};
                const bf16x8 pf = __builtin_bit_cast(bf16x8, pw);
                o = __builtin_amdgcn_mfma_f32_32x32x16_bf16(vf, pf, o, 0, 0, 0);
                ls = __builtin_amdgcn_mfma_f32_32x32x16_bf16(ones, pf, ls, 0, 0, 0);
            }
        }
    }
    const float rl = 1.0f / ls[0];
    bf16_t* dst = O + (size_t)(qw + r32) * D_MODEL + h * 32 + 4 * hi;
#pragma unroll
    for (int t = 0; t < 4; ++t) {
        u32x2 wv; wv[0] = pk_bf16(o[4 * t] * rl, o[4 * t + 1] * rl); wv[1] = pk_bf16(o[4 * t + 2] * rl, o[4 * t + 3] * rl);
        ST8(dst + 8 * t, wv);
    }
    };
    if (qb > 0) body(std::integral_constant<bool, true>{}); else body(std::integral_constant<bool, false>{});
    wait_vm<0>();
}

template <typename KF> static void set_lds(KF kf, int bytes) {
    (void)hipFuncSetAttribute((const void*)kf, hipFuncAttributeMaxDynamicSharedMemorySize, bytes);
}

extern "C" void kernel_launch(void* const* d_in, const int* in_sizes, int n_in,
                              void* d_out, int out_size, void* d_ws, size_t ws_size,
                              hipStream_t stream) {
    const bf16_t* x = (const bf16_t*)d_in[0];
    const bf16_t* wqkv = (const bf16_t*)d_in[1];
    const bf16_t* wo = (const bf16_t*)d_in[2];
    const bf16_t* w1 = (const bf16_t*)d_in[4];
    const bf16_t* w3 = (const bf16_t*)d_in[5];
    const bf16_t* w2 = (const bf16_t*)d_in[6];
    bf16_t* out = (bf16_t*)d_out;
    char* ws = (char*)d_ws;
    bf16_t* qkv = (bf16_t*)(ws + (4u << 20));
    bf16_t* att = (bf16_t*)(ws + (16u << 20));
    bf16_t* hbuf = (bf16_t*)(ws + (20u << 20));
    bf16_t* act = (bf16_t*)(ws + (28u << 20));
    float* hstat = (float*)(ws + (24u << 20));

    {
        auto kf = gemm_kernel<128, 192, 2, 4, 4, EPI_STORE, 1, 2, 4>;
        constexpr int lds = 4 * (128 + 192) * 128;
        set_lds(kf, lds);
        kf<<<256, 512, lds, stream>>>(x, wqkv, nullptr, qkv, nullptr, S_LEN, 3072, 1024, 3072, 3072);
    }
    {
        set_lds(attn_kernel, 73728);
        attn_kernel<<<256, 512, 73728, stream>>>(qkv, att, wo);
    }
    {
        auto kf = gemm_kernel<128, 64, 4, 2, 6, EPI_RES_STAT, 0, 4, 2>;
        constexpr int lds = 6 * (128 + 64) * 128 + 16384;
        set_lds(kf, lds);
        kf<<<256, 512, lds, stream>>>(att, wo, x, hbuf, hstat, S_LEN, 1024, 1024, 1024, 1024);
    }
    {
        set_lds(glu_gemm_kernel, 163840);
        glu_gemm_kernel<<<256, 512, 163840, stream>>>(hbuf, w1, w3, hstat, act, w2);
    }
    {
        auto kf = gemm_kernel<128, 64, 4, 2, 6, EPI_RES, 0, 4, 2>;
        constexpr int lds = 6 * (128 + 64) * 128 + 16384;
        set_lds(kf, lds);
        kf<<<256, 512, lds, stream>>>(act, w2, hbuf, out, nullptr, S_LEN, 1024, F_DIM, 1024, 1024);
    }
}
```
